```python
import torch

def _softmax(x, axis=-1):
    return torch.softmax(x, dim=axis)
def _gelu(x):
    return torch.nn.functional.gelu(x, approximate="tanh")
def _silu(x):
    return torch.nn.functional.silu(x)
def _sigmoid(x):
    return torch.sigmoid(x)
def _layernorm(x, axis=-1, eps=1e-5):
    m = x.mean(dim=axis, keepdim=True)
    v = x.var(dim=axis, keepdim=True, unbiased=False)
    return (x - m) / torch.sqrt(v + eps)
def _scatter_add(tgt, idx, src, axis=0):
    return tgt.clone().index_add_(axis, idx, src)
def _segment_sum(src, seg, n):
    out = torch.zeros((n,) + src.shape[1:], dtype=src.dtype)
    return out.index_add_(0, seg, src)
def _pad(x, pw):
    flat = tuple(v for pair in reversed(pw) for v in pair)
    return torch.nn.functional.pad(x, flat)

kernel_name = 'standalone__gqa_block_cached__near_ridge'

def setup_inputs():

    _w = (lambda a: (lambda t: t.to(torch.bfloat16) if t.is_floating_point() else t)(torch.as_tensor(a)))
    B, Sq, D = 32, 112, 640
    Hq, Hkv, Sk, Dh = 32, 32, 512, 128
    assert Hq % Hkv == 0, (Hq, Hkv)
    return {
        "x": _w((lambda *s: torch.randn(*s, dtype=torch.bfloat16) * 0.8)(B, Sq, D)),
        "k_cache": _w((lambda *s: torch.randn(*s, dtype=torch.bfloat16) * 0.8)(B, Hkv, Sk, Dh)),
        "v_cache": _w((lambda *s: torch.randn(*s, dtype=torch.bfloat16) * 0.05)(B, Hkv, Sk, Dh)),
        "wq": _w((lambda *s: torch.randn(*s, dtype=torch.bfloat16) * 0.8)(D, Hq * Dh) * (D ** -0.5)),
        "wo": _w((lambda *s: torch.randn(*s, dtype=torch.bfloat16) * 0.05)(Hq * Dh, D) * ((Hq * Dh) ** -0.5)),
    }

def reference(x, k_cache, v_cache, wq, wo):
    B, Sq, D = x.shape
    Hq, Dh = 32, 128
    Hkv = k_cache.shape[1]
    rep = Hq // Hkv
    q = (lambda x, *d: x.permute(*d))((x @ wq).reshape(B, Sq, Hq, Dh), 0, 2, 1, 3)
    k = (lambda x, n, axis: torch.repeat_interleave(x, n, dim=axis))(k_cache, rep, axis=1)
    v = (lambda x, n, axis: torch.repeat_interleave(x, n, dim=axis))(v_cache, rep, axis=1)
    scores = torch.einsum("bhqd,bhkd->bhqk", q, k) * (Dh ** -0.5)
    p = _softmax(scores, axis=-1)
    o = torch.einsum("bhqk,bhkd->bhqd", p, v)
    return (lambda x, *d: x.permute(*d))(o, 0, 2, 1, 3).reshape(B, Sq, Hq * Dh) @ wo
```

```cpp
#include <hip/hip_runtime.h>
#include <hip/hip_bf16.h>
typedef unsigned short u16;
__device__ __forceinline__ float bf2f(u16 v){ return __uint_as_float(((unsigned)v)<<16); }
__device__ __forceinline__ u16 f2bf(float f){ unsigned u=__float_as_uint(f); u += 0x7fffu + ((u>>16)&1u); return (u16)(u>>16); }
__global__ void ref_gemm(const u16* A, const u16* B, u16* C, int M, int N, int K){
  __shared__ float As[16][17], Bs[16][17];
  int tx=threadIdx.x, ty=threadIdx.y; int row=blockIdx.y*16+ty, col=blockIdx.x*16+tx; float acc=0;
  for(int k0=0;k0<K;k0+=16){ As[ty][tx]=bf2f(A[(size_t)row*K+k0+tx]); Bs[ty][tx]=bf2f(B[(size_t)(k0+ty)*N+col]); __syncthreads();
    for(int k=0;k<16;++k) acc+=As[ty][k]*Bs[k][tx]; __syncthreads(); }
  C[(size_t)row*N+col]=f2bf(acc);
}
__global__ void ref_attn(const u16* Q, const u16* K, const u16* V, u16* O){
  int q=blockIdx.x%112, bh=blockIdx.x/112, h=bh%32, b=bh/32, t=threadIdx.x;
  __shared__ float qs[128], s[512], red[128];
  qs[t]=bf2f(Q[((size_t)(b*112+q))*4096+h*128+t]); __syncthreads();
  const u16* Kh=K+(size_t)bh*512*128; const u16* Vh=V+(size_t)bh*512*128;
  float mx=-1e30f;
  for(int j=t;j<512;j+=128){ float a=0; for(int d=0;d<128;++d) a+=qs[d]*bf2f(Kh[j*128+d]); a*=0.08838834764831845f; s[j]=a; mx=fmaxf(mx,a);}
  red[t]=mx; __syncthreads(); for(int o=64;o>0;o>>=1){ if(t<o) red[t]=fmaxf(red[t],red[t+o]); __syncthreads(); } mx=red[0]; __syncthreads();
  float sm=0; for(int j=t;j<512;j+=128){ float e=__expf(s[j]-mx); s[j]=e; sm+=e; }
  red[t]=sm; __syncthreads(); for(int o=64;o>0;o>>=1){ if(t<o) red[t]+=red[t+o]; __syncthreads(); } sm=red[0];
  float a=0; for(int j=0;j<512;++j) a+=s[j]*bf2f(Vh[j*128+t]);
  O[((size_t)(b*112+q))*4096+h*128+t]=f2bf(a/sm);
}
extern "C" void kernel_launch(void* const* d_in, const int* in_sizes, int n_in, void* d_out, int out_size, void* d_ws, size_t ws_size, hipStream_t stream){
  const u16* x=(const u16*)d_in[0]; const u16* k=(const u16*)d_in[1]; const u16* v=(const u16*)d_in[2]; const u16* wq=(const u16*)d_in[3]; const u16* wo=(const u16*)d_in[4];
  u16* Q=(u16*)d_ws; u16* O=Q+(size_t)3584*4096;
  ref_gemm<<<dim3(4096/16,3584/16),dim3(16,16),0,stream>>>(x,wq,Q,3584,4096,640);
  ref_attn<<<32*32*112,128,0,stream>>>(Q,k,v,O);
  ref_gemm<<<dim3(640/16,3584/16),dim3(16,16),0,stream>>>(O,wo,(u16*)d_out,3584,640,4096);
}
```

```cpp
#include <hip/hip_runtime.h>
#include <stdint.h>
typedef __attribute__((ext_vector_type(8))) __bf16 bf16x8;
typedef __attribute__((ext_vector_type(2))) __bf16 bf16x2;
typedef __attribute__((ext_vector_type(4))) float f32x4;
typedef __attribute__((ext_vector_type(16))) float f32x16;
typedef __attribute__((ext_vector_type(2))) float f32x2;
typedef __attribute__((ext_vector_type(4))) unsigned u32x4;
typedef __attribute__((ext_vector_type(2))) unsigned u32x2;
typedef __attribute__((ext_vector_type(4))) short s16x4;
#define LAS __attribute__((address_space(3)))
#define DI __device__ __forceinline__
typedef LAS char* lptr;
typedef __amdgpu_buffer_rsrc_t rsrc_t;
typedef __attribute__((address_space(1))) unsigned long long gu64;
#define RLX_AGENT __ATOMIC_RELAXED, __HIP_MEMORY_SCOPE_AGENT

constexpr int SLOT = 16384, XS = 6 * SLOT, XCH = 14336, PARK = XS + 2 * XCH, DUMMY = PARK + 2 * XCH, TAGW = DUMMY + 4096, CNT = TAGW + 16, APROG = CNT + 32, PPROG = APROG + 16,
              QPROG = PPROG + 16, STMP = CNT + 128, ARR = STMP + 512, LDS_BYTES = ARR + 1024;
constexpr float QSCALE = 0.08838834764831845f * 1.4426950408889634f;
constexpr unsigned OOB = 0x40000000u;
constexpr size_t WS_TF = 8192, WS_OF = 16384, WS_XF = WS_OF + 131072, WS_O = 1u << 20, WS_Q = 40u << 20, WS_WT = 48u << 20, WS_QT = 56u << 20, WS_PX = 64u << 20, WS_ST = 128u << 20;
constexpr int PO3 = 0, OWNP = 2 * SLOT;
constexpr unsigned P_Q2 = 12, P_Q3 = 22, P_XSFREE = 29;

DI unsigned rfl(unsigned x) { return __builtin_amdgcn_readfirstlane(x); }
DI unsigned cvtpk(float lo, float hi) { f32x2 v = {lo, hi}; bf16x2 b = __builtin_convertvector(v, bf16x2); return __builtin_bit_cast(unsigned, b); }
DI f32x4 mfma(u32x4 a, u32x4 b, f32x4 c) { return __builtin_amdgcn_mfma_f32_16x16x32_bf16(__builtin_bit_cast(bf16x8, a), __builtin_bit_cast(bf16x8, b), c, 0, 0, 0); }
DI void mfma_ip(f32x4& c, const u32x4& a, const u32x4& b) { asm volatile("v_mfma_f32_16x16x32_bf16 %0, %1, %2, %0" : "+v"(c) : "v"(a), "v"(b)); }
#define MFMA_DONE() asm volatile("s_nop 15")
DI f32x16 mfma32(u32x4 a, u32x4 b, f32x16 c) { return __builtin_amdgcn_mfma_f32_32x32x16_bf16(__builtin_bit_cast(bf16x8, a), __builtin_bit_cast(bf16x8, b), c, 0, 0, 0); }
DI u32x4 lds128(lptr p) { return *(const LAS u32x4*)p; }
DI int fresh_lane() { int l; asm volatile("v_mbcnt_lo_u32_b32 %0, -1, 0\n\tv_mbcnt_hi_u32_b32 %0, -1, %0" : "=v"(l)); return l; }
DI lptr lp(unsigned a) { return (lptr)(uintptr_t)a; }
DI u32x2 ldstr(lptr p) { return __builtin_bit_cast(u32x2, __builtin_amdgcn_ds_read_tr16_b64_v4i16((LAS s16x4*)p)); }
DI rsrc_t mkrsrc(const void* p, unsigned bytes) { return __builtin_amdgcn_make_buffer_rsrc((void*)p, 0, bytes, 0x00020000); }
DI float xmax32(float m) { auto b = __builtin_amdgcn_permlane32_swap(__float_as_uint(m), __float_as_uint(m), false, false); return fmaxf(__uint_as_float(b[0]), __uint_as_float(b[1])); }
DI float xsum32(float m) { auto b = __builtin_amdgcn_permlane32_swap(__float_as_uint(m), __float_as_uint(m), false, false); return __uint_as_float(b[0]) + __uint_as_float(b[1]); }
DI float rowmax16(const f32x16& s) {
  float a = fmaxf(fmaxf(s[0], s[1]), s[2]), b = fmaxf(fmaxf(s[3], s[4]), s[5]);
  a = fmaxf(fmaxf(a, s[6]), s[7]); b = fmaxf(fmaxf(b, s[8]), s[9]); a = fmaxf(fmaxf(a, s[10]), s[11]); b = fmaxf(fmaxf(b, s[12]), s[13]);
  a = fmaxf(fmaxf(a, s[14]), s[15]);
  return xmax32(fmaxf(a, b));
}
template <int OFF> DI void dma_nt(rsrc_t r, unsigned voff, unsigned soff, unsigned ldsaddr) {
  asm volatile("s_mov_b32 m0, %3\n\ts_nop 0\n\tbuffer_load_dwordx4 %0, %1, %2 offen offset:%4 nt lds" :: "v"(voff), "s"(r), "s"(soff), "s"(ldsaddr), "i"(OFF) : "memory");
}
DI void dma_df(rsrc_t r, unsigned voff, unsigned soff, unsigned ldsaddr) {
  asm volatile("s_mov_b32 m0, %3\n\ts_nop 0\n\tbuffer_load_dwordx4 %0, %1, %2 offen lds" :: "v"(voff), "s"(r), "s"(soff), "s"(ldsaddr) : "memory");
}
DI void dma_sc1(rsrc_t r, unsigned voff, unsigned soff, unsigned ldsaddr) {
  asm volatile("s_mov_b32 m0, %3\n\ts_nop 0\n\tbuffer_load_dwordx4 %0, %1, %2 offen sc1 lds" :: "v"(voff), "s"(r), "s"(soff), "s"(ldsaddr) : "memory");
}
#define SB() __builtin_amdgcn_sched_barrier(0)
#define WAITV(N) do { SB(); asm volatile("s_waitcnt vmcnt(" #N ") lgkmcnt(0)" ::: "memory"); SB(); } while (0)
#define WAITL() do { SB(); asm volatile("s_waitcnt lgkmcnt(0)" ::: "memory"); SB(); } while (0)
#define BAR_L() do { SB(); asm volatile("s_waitcnt lgkmcnt(0)\n\ts_barrier" ::: "memory"); SB(); } while (0)
#define ST() do {} while (0)
#define PST() do {} while (0)
#define PST2() do {} while (0)
#define PIN(x) asm volatile("" : "+v"(x))
#define NODMA 0
#define DPOS 2
#define DSTR 8
#define CAP 6
#define SAUX 16
#define PPRIO 0
#define ABL 0
#define TE0 2
#define ROT 1
#define QLD0 6
#define PFD 16
constexpr float PLIM = 1024.f;

template <int OFF> DI unsigned prog_read(unsigned pa) { return *(volatile LAS unsigned*)(lp(pa) + OFF); }
template <int OFF, int SLEEP> DI void prog_wait(unsigned pa, unsigned v, unsigned t) {
  unsigned n;
  asm volatile("s_mov_b32 %1, 0x1000000\n"
               "1:\n\tv_cmp_gt_u32 vcc, %3, %0\n\ts_cbranch_vccz 2f\n\ts_sleep %5\n\tds_read_b32 %0, %2 offset:%4\n\ts_sub_u32 %1, %1, 1\n\ts_waitcnt lgkmcnt(0)\n\ts_cbranch_scc0 1b\n2:"
               : "+v"(v), "=&s"(n) : "v"(pa), "s"(t), "i"(OFF), "i"(SLEEP) : "memory", "vcc", "scc");
}
template <int OFF, int SLEEP> DI void prog_wait(unsigned pa, unsigned t) { prog_wait<OFF, SLEEP>(pa, prog_read<OFF>(pa), t); }
DI void prog_post(lptr p, unsigned v) { *(volatile LAS unsigned*)p = v; }

DI void publish(gu64* flag, lptr cnt, unsigned long long tag, int lane) {
  asm volatile("s_waitcnt vmcnt(0)" ::: "memory");
  if (lane == 0) {
    const unsigned old = __hip_atomic_fetch_add((LAS unsigned*)cnt, 1u, __ATOMIC_RELAXED, __HIP_MEMORY_SCOPE_WORKGROUP);
    asm volatile("" : "+s"(tag));
    if (old == 3u) __hip_atomic_store(flag, tag, RLX_AGENT);
  }
}
DI void wait_flag(gu64* f, unsigned long long v, unsigned long long tag) {
#pragma nounroll
  for (unsigned spins = 0; spins < (1u << 15); ++spins) {
    if (__all(v == tag)) break;
    __builtin_amdgcn_s_sleep(2);
    v = __hip_atomic_load(f, RLX_AGENT);
  }
  __builtin_amdgcn_s_waitcnt(0x0F70);
  __builtin_amdgcn_fence(__ATOMIC_ACQUIRE, "wavefront");
  asm volatile("" ::: "memory");
}

struct Ctx {
  char* ws; int b, g;
  DI Ctx fresh() const { Ctx c = *this; asm volatile("" : "+s"(c.b), "+s"(c.g)); return c; }
  DI gu64* epoch() const { return (gu64*)ws; }
  DI gu64* tflags() const { return (gu64*)(ws + WS_TF + g * 256); }
  DI gu64* flags() const { return (gu64*)(ws + WS_OF + (b * 8 + g) * 512); }
  DI gu64* pflags() const { return (gu64*)(ws + WS_OF + (b * 8 + (g ^ 1)) * 512); }
  DI gu64* xflag() const { return (gu64*)(ws + WS_XF + (b * 2 + (g & 1)) * 512); }
  DI rsrc_t ro() const { return mkrsrc(ws + WS_O + (size_t)(b * 8 + g) * 114688, 114688); }
  DI rsrc_t rp() const { return mkrsrc(ws + WS_O + (size_t)(b * 8 + (g ^ 1)) * 114688, 114688); }
  DI rsrc_t rpx() const { return mkrsrc(ws + WS_PX + (size_t)(b * 2 + (g & 1)) * 573440, 573440); }
  DI rsrc_t rwt() const { return mkrsrc(ws + WS_WT + (size_t)g * 655360, 655360); }
  DI rsrc_t rqt() const { return mkrsrc(ws + WS_QT + (size_t)g * 327680, 327680); }
  DI rsrc_t rq() const { return mkrsrc(ws + WS_Q + (size_t)(b * 8 + g) * 28672, 28672); }
};
struct AttnState {
  u32x4 q[8], vf[8], p1[2];
  f32x16 o[4], negm;
  float l;
  unsigned ka, va, kvo, vvo, pa, pg;
  int slot;
};

DI void fixup(AttnState& a, const f32x16& sa, f32x16& sb, bool adjb, u32x4 (&p)[2], float& rs) {
  const float dl = fmaxf(rowmax16(sa), 0.f), sc = __builtin_amdgcn_exp2f(-dl);
  a.l *= sc; a.negm -= dl;
#pragma unroll
  for (int dt = 0; dt < 4; ++dt) a.o[dt] *= sc;
  float e[16]; rs = 0.f;
#pragma unroll
  for (int i = 0; i < 16; ++i) { e[i] = __builtin_amdgcn_exp2f(sa[i] - dl); rs += e[i]; }
#pragma unroll
  for (int i = 0; i < 8; ++i) p[i >> 2][i & 3] = cvtpk(e[2 * i], e[2 * i + 1]);
  if (adjb) sb -= dl;
}

template <bool FIRST>
DI void attn_step(AttnState& a, const int s, const int w, const int lane, lptr lds, const unsigned l0, rsrc_t rk, rsrc_t rv) {
  const unsigned kb = a.ka + a.slot, vb = a.va + a.slot;
  const unsigned pk = l0 + (a.slot == 0 ? 4 * SLOT : a.slot - 2 * SLOT) + w * 4096;
  const unsigned pv = pk + SLOT, gs = (s + 2) * SLOT;
  const f32x16 zero = {0.f, 0.f, 0.f, 0.f, 0.f, 0.f, 0.f, 0.f, 0.f, 0.f, 0.f, 0.f, 0.f, 0.f, 0.f, 0.f};
  u32x4 kf[8], kg[8];
  auto dma_at = [&](int gi) {
    if (NODMA) return;
    if (CAP < 16 && gi >= DPOS && (gi - DPOS) % (DSTR / 2) == 0 && (gi - DPOS) / (DSTR / 2) < 8) asm volatile("s_waitcnt vmcnt(%0)" :: "i"(CAP - 1) : "memory");
    if (gi == DPOS) dma_nt<0>(rk, a.kvo, gs, pk);
    if (gi == DPOS + DSTR / 2) dma_nt<1024>(rk, a.kvo ^ 64, gs, pk);
    if (gi == DPOS + 2 * DSTR / 2) dma_nt<2048>(rk, a.kvo ^ 128, gs, pk);
    if (gi == DPOS + 3 * DSTR / 2) dma_nt<3072>(rk, a.kvo ^ 192, gs, pk);
    if (gi == DPOS + 4 * DSTR / 2) dma_nt<0>(rv, a.vvo, gs, pv);
    if (gi == DPOS + 5 * DSTR / 2) dma_nt<1024>(rv, a.vvo, gs, pv);
    if (gi == DPOS + 6 * DSTR / 2) dma_nt<2048>(rv, a.vvo, gs, pv);
    if (gi == DPOS + 7 * DSTR / 2) dma_nt<3072>(rv, a.vvo, gs, pv);
  };
  SB();
  lptr qp = lds + ((s & 8) ? PARK : XS) + (w == 3 ? 6 : 2 * w + ((lane >> 4) & 1)) * 4096 + (lane >> 5) * 256 + (lane & 15) * 16;
#pragma unroll
  for (int i = 0; i < 8; ++i) {
    a.o[i & 3] = mfma32(a.vf[i], a.p1[i >> 2], a.o[i & 3]);
    if (i == 1) {
      PIN(a.o[0]); PIN(a.o[1]);
      prog_wait<0, 0>(a.pa, a.pg, s);
      if (FIRST) prog_wait<16, 1>(a.pa, s < 16 ? 0u : (s == 16 ? P_Q2 : P_Q3));
    }
    if (i >= 1 && i <= 4) { kf[2 * i - 2] = lds128(lp(kb ^ ((2 * i - 2) << 5))); kf[2 * i - 1] = lds128(lp(kb ^ ((2 * i - 1) << 5))); }
    if (FIRST && i >= 2 && i <= 5) { a.q[2 * i - 4] = lds128(qp + (2 * i - 4) * 512); a.q[2 * i - 3] = lds128(qp + (2 * i - 3) * 512); }
    dma_at(i);
    SB();
  }
  f32x16 s0, s1;
  float inv = 0.f; unsigned oad = 0;
  if (FIRST) { inv = __builtin_amdgcn_rcpf(xsum32(a.l));
    oad = l0 + ((s == 0 || (w == 3 && (lane & 16))) ? DUMMY : ((s & 8) ? PARK : XS) + (2 * w + ((lane >> 4) & 1)) * 4096) + (lane >> 5) * 256 + (lane & 15) * 16; }
#pragma unroll
  for (int i = 0; i < 8; ++i) {
    s0 = mfma32(kf[i], a.q[i], i ? s0 : (FIRST ? zero : a.negm));
    kg[i] = lds128(lp(kb ^ (i << 5)) + 8192);
    dma_at(8 + i);
    if (FIRST) {
      const int dt = i >> 1, e = 8 * (i & 1);
      const u32x4 f = {cvtpk(a.o[dt][e] * inv, a.o[dt][e + 1] * inv), cvtpk(a.o[dt][e + 2] * inv, a.o[dt][e + 3] * inv),
                       cvtpk(a.o[dt][e + 4] * inv, a.o[dt][e + 5] * inv), cvtpk(a.o[dt][e + 6] * inv, a.o[dt][e + 7] * inv)};
      *(LAS u32x4*)(lp(oad) + dt * 1024 + (i & 1) * 512) = f;
    }
    SB();
  }
  float m0 = 0.f;
  if (FIRST) {
    prog_post(lds + QPROG + w * 4, s + 1);
    m0 = rowmax16(s0);
#pragma unroll
    for (int i = 0; i < 16; ++i) a.negm[i] = -m0;
    PIN(a.negm);
    a.l = 0.f;
    SB();
  }
  u32x4 p0[2]; float ra, rb;
#pragma unroll
  for (int i = 0; i < 8; ++i) {
    s1 = mfma32(kg[i], a.q[i], i ? s1 : a.negm);
    { const u32x2 v0 = ldstr(lp(vb ^ ((i & 3) << 6)) + (i >> 2) * 4096), v1 = ldstr(lp(vb ^ ((i & 3) << 6)) + (i >> 2) * 4096 + 2048); a.vf[i] = u32x4{v0[0], v0[1], v1[0], v1[1]}; }
    const float e0 = __builtin_amdgcn_exp2f(FIRST ? s0[2 * i] - m0 : s0[2 * i]), e1 = __builtin_amdgcn_exp2f(FIRST ? s0[2 * i + 1] - m0 : s0[2 * i + 1]);
    ra = i ? ra + e0 : e0; rb = i ? rb + e1 : e1;
    p0[i >> 2][i & 3] = cvtpk(e0, e1);
    PIN(p0[i >> 2][i & 3]); PIN(ra); PIN(rb);
    dma_at(16 + i);
    SB();
  }
  ra += rb; PIN(ra);
  if (!FIRST) { if (__builtin_expect(__any(!(ra <= PLIM)), 0)) fixup(a, s0, s1, true, p0, ra); }
  PIN(ra); a.l += ra; PIN(a.l);
  SB();
#pragma unroll
  for (int i = 0; i < 8; ++i) {
    a.o[i & 3] = mfma32(a.vf[i], p0[i >> 2], (FIRST && i < 4) ? zero : a.o[i & 3]);
    const float e0 = __builtin_amdgcn_exp2f(s1[2 * i]), e1 = __builtin_amdgcn_exp2f(s1[2 * i + 1]);
    ra = i ? ra + e0 : e0; rb = i ? rb + e1 : e1;
    a.p1[i >> 2][i & 3] = cvtpk(e0, e1);
    PIN(a.p1[i >> 2][i & 3]); PIN(ra); PIN(rb);
    { const u32x2 v0 = ldstr(lp(vb ^ ((i & 3) << 6)) + (i >> 2) * 4096 + 8192), v1 = ldstr(lp(vb ^ ((i & 3) << 6)) + (i >> 2) * 4096 + 8192 + 2048); a.vf[i] = u32x4{v0[0], v0[1], v1[0], v1[1]}; }
    dma_at(24 + i);
    SB();
  }
  ra += rb; PIN(ra);
  if (__builtin_expect(__any(!(ra <= PLIM)), 0)) fixup(a, s1, s1, false, a.p1, ra);
  PIN(ra); a.l += ra; PIN(a.l);
  a.slot = a.slot == 4 * SLOT ? 0 : a.slot + 2 * SLOT;
  WAITV(8);
  prog_post(lds + APROG + w * 4, s + 1);
  a.pg = prog_read<0>(a.pa);
}

DI void attn_wave(const int w, const int lane, const int rot, lptr lds, const unsigned l0, rsrc_t rk, rsrc_t rv, rsrc_t rx, rsrc_t rwq, const char* wo, const Ctx& c0, int& nst) {
  const int lg = lane >> 4, lr = lane & 15, h = lane >> 5, r32 = lane & 31;
  ST();
  AttnState a;
  a.kvo = (16 * w + lg) * 256 + ((lr ^ lg) << 4);
  a.vvo = (16 * w + lg) * 256 + ((lr ^ (lg << 2)) << 4);
  {
    const unsigned wvo = h * 8192 + (((lane & 31) ^ (h | ((w & 1) << 3))) << 4) + c0.g * 1024;
    const unsigned xvo = (lane >> 3) * 1280 + (((lane & 7) ^ ((4 * (w & 1) + lg) & 7)) << 4);
    auto issue = [&](int c, int buf) {
      if (c < 10) {
        const int cr = c + rot >= 10 ? c + rot - 10 : c + rot;
        const unsigned xs = w * 10240 + cr * 128, xd = l0 + XS + buf * XCH + w * 1024, ws_ = (cr * 64 + 2 * w) * 8192, wd = l0 + buf * 2 * SLOT + w * 1024;
#pragma unroll
        for (int i = 0; i < 3; ++i) dma_df(rx, xvo, xs + i * 40960, xd + i * 4096);
        dma_df(rx, xvo, xs + 3 * 40960, w < 2 ? xd + 3 * 4096 : l0 + DUMMY);
#pragma unroll
        for (int i = 0; i < 8; ++i) dma_df(rwq, wvo, ws_ + i * 65536, wd + i * 4096);
      } else {
        const unsigned d = l0 + buf * 2 * SLOT + w * 4096, gsrc = (c - 10) * SLOT;
        dma_nt<0>(rk, a.kvo, gsrc, d); dma_nt<1024>(rk, a.kvo ^ 64, gsrc, d); dma_nt<2048>(rk, a.kvo ^ 128, gsrc, d); dma_nt<3072>(rk, a.kvo ^ 192, gsrc, d);
        dma_nt<0>(rv, a.vvo, gsrc, d + SLOT); dma_nt<1024>(rv, a.vvo, gsrc, d + SLOT); dma_nt<2048>(rv, a.vvo, gsrc, d + SLOT); dma_nt<3072>(rv, a.vvo, gsrc, d + SLOT);
      }
    };
    issue(0, 0); issue(1, 1);
    if (w == 0) { *(LAS u32x4*)(lds + CNT + (lane & 7) * 16) = u32x4{0u, 0u, 0u, 0u}; }
    u32x2 rw[8], rq2[8]; unsigned short r5[8]; unsigned long long e1 = 0;
#pragma unroll
    for (int i = 0; i < 8; ++i) { rw[i] = rq2[i] = u32x2{0u, 0u}; r5[i] = 0; }
    int buf = 2;
#pragma nounroll
    for (int c = 0; c < 10; ++c) {
      if (c < 9) WAITV(12); else WAITV(8);
      BAR_L();
      if (c == TE0) {
        const Ctx cc = c0.fresh(); const int b = cc.b, g = cc.g; const rsrc_t rwo = mkrsrc(wo, 4096 * 1280);
        e1 = __hip_atomic_load(cc.epoch(), RLX_AGENT);
        const unsigned rb = (16 * (lg >> 1) + 4 * (lg & 1)) * 1280 + (g & 1) * 640, wv0 = rb + w * 128 + lr * 8, wv1 = rb + 512 + w * 32 + lr * 2;
        const unsigned s2 = ((4 * (g ^ (b >> 4)) + ((b >> 2) & 3)) * 128 + (b & 3) * 32) * 1280;
        const unsigned qv = lg * 8 * 8192 + lr * 8 + w * 128 + g * 1024 + 512, sq = b < 20 ? b * 32 * 8192 : OOB;
#pragma unroll
        for (int jj = 0; jj < 8; ++jj) rq2[jj] = __builtin_amdgcn_raw_buffer_load_b64(rwq, qv, sq + jj * 8192, 0);
#pragma unroll
        for (int jj = 0; jj < 8; ++jj) rw[jj] = __builtin_amdgcn_raw_buffer_load_b64(rwo, wv0 + (jj & 3) * 1280, s2 + (jj >> 2) * 8 * 1280, 0);
#pragma unroll
        for (int jj = 0; jj < 8; ++jj) r5[jj] = __builtin_amdgcn_raw_buffer_load_b16(rwo, wv1 + (jj & 3) * 1280, s2 + (jj >> 2) * 8 * 1280, 0);
      }
      if (c == TE0 + 2) {
        const Ctx cc = c0.fresh(); const int b = cc.b;
#pragma unroll
        for (int e = 0; e < 4; ++e) { u32x4 f;
#pragma unroll
          for (int pp = 0; pp < 4; ++pp) f[pp] = __builtin_amdgcn_perm(rq2[2 * pp + 1][e >> 1], rq2[2 * pp][e >> 1], (e & 1) ? 0x07060302u : 0x05040100u);
          __builtin_amdgcn_raw_buffer_store_b128(f, cc.rqt(), (b * 4 + w) * 4096 + e * 1024 + lane * 16, 0, 16); }
#pragma unroll
        for (int e = 0; e < 5; ++e) { u32x4 f;
#pragma unroll
          for (int pp = 0; pp < 4; ++pp) f[pp] = e < 4 ? __builtin_amdgcn_perm(rw[2 * pp + 1][(e >> 1) & 1], rw[2 * pp][(e >> 1) & 1], (e & 1) ? 0x07060302u : 0x05040100u)
                                                       : __builtin_amdgcn_perm((unsigned)r5[2 * pp + 1], (unsigned)r5[2 * pp], 0x05040100u);
          __builtin_amdgcn_raw_buffer_store_b128(f, cc.rwt(), (b * 4 + w) * 5120 + e * 1024 + lane * 16, 0, 16); }
      }
      if (c == TE0 + 4) { const Ctx cc = c0.fresh(); const unsigned long long tag = (((unsigned long long)rfl((unsigned)(e1 >> 32)) << 32) | rfl((unsigned)e1)) + 1;
        *(LAS unsigned long long*)(lds + TAGW) = tag; publish(cc.tflags() + cc.b, lds + CNT + 16, tag, lane); }
      issue(c + 2, buf); buf = buf == 2 ? 0 : buf + 1;
    }
    BAR_L();
  }
  a.ka = l0 + r32 * 256 + ((h ^ lr) << 4);
  a.va = l0 + SLOT + (4 * h + (lr >> 2)) * 256 + ((lr >> 2) << 6) + (lg & 1) * 32 + (lane & 3) * 8;
  a.slot = 2 * SLOT; a.l = 0.f; a.pg = 0; a.pa = l0 + APROG + (lane & 3) * 4;
#pragma unroll
  for (int i = 0; i < 8; ++i) a.vf[i] = u32x4{0u, 0u, 0u, 0u};
  a.p1[0] = a.p1[1] = u32x4{0u, 0u, 0u, 0u};
#pragma unroll
  for (int i = 0; i < 16; ++i) { a.o[0][i] = a.o[1][i] = a.o[2][i] = a.o[3][i] = 0.f; }
  WAITV(8);
  BAR_L(); ST();
#pragma nounroll
  for (int r = 0; r < 4; ++r) {
    attn_step<true>(a, 8 * r, w, lane, lds, l0, rk, rv); ST();
#pragma nounroll
    for (int t = 1; t < 8; ++t) { attn_step<false>(a, 8 * r + t, w, lane, lds, l0, rk, rv); ST(); }
  }
#pragma unroll
  for (int i = 0; i < 8; ++i) a.o[i & 3] = mfma32(a.vf[i], a.p1[i >> 2], a.o[i & 3]);
  prog_wait<16, 1>(a.pa, P_XSFREE);
  const Ctx c = c0.fresh();
  const rsrc_t ro = c.ro(), rp = c.rp(); gu64* flags = c.flags(); gu64* pflags = c.pflags();
  {
    const float inv = __builtin_amdgcn_rcpf(xsum32(a.l));
    const bool pad = w == 3 && (lg & 1);
    const unsigned io = (2 * w + (lg & 1)) * 4096 + h * 256 + lr * 16, go = pad ? OOB : 3 * 28672 + io, lo = l0 + (pad ? DUMMY + h * 256 + lr * 16 : XS + io);
#pragma unroll
    for (int i = 0; i < 8; ++i) {
      const int dt = i >> 1, e = 8 * (i & 1);
      const u32x4 f = {cvtpk(a.o[dt][e] * inv, a.o[dt][e + 1] * inv), cvtpk(a.o[dt][e + 2] * inv, a.o[dt][e + 3] * inv),
                       cvtpk(a.o[dt][e + 4] * inv, a.o[dt][e + 5] * inv), cvtpk(a.o[dt][e + 6] * inv, a.o[dt][e + 7] * inv)};
      __builtin_amdgcn_raw_buffer_store_b128(f, ro, go + dt * 1024 + (i & 1) * 512, 0, 16);
      *(LAS u32x4*)(lp(lo) + dt * 1024 + (i & 1) * 512) = f;
    }
  }
  prog_post(lds + QPROG + w * 4, 33);
  ST();
  const unsigned long long tg = *(const LAS unsigned long long*)(lds + TAGW), tag = ((unsigned long long)rfl((unsigned)(tg >> 32)) << 32) | rfl((unsigned)tg);
  publish(flags + 3 * 16, lds + CNT + 12, tag, lane);
  wait_flag(pflags + 3 * 16, __hip_atomic_load(pflags + 3 * 16, RLX_AGENT), tag);
  {
    u32x4 f[7];
#pragma unroll
    for (int i = 0; i < 7; ++i) f[i] = __builtin_amdgcn_raw_buffer_load_b128(rp, 3 * 28672 + (w * 7 + i) * 1024 + lane * 16, 0, 16);
    prog_wait<0, 0>(a.pa, 32);
#pragma unroll
    for (int i = 0; i < 7; ++i) *(LAS u32x4*)(lds + PO3 + (w * 7 + i) * 1024 + lane * 16) = f[i];
  }
  WAITL(); prog_post(lds + APROG + w * 4, 34);
  ST();
}

constexpr int LAG = 3;
DI void parrive(unsigned& pc, const int j, lptr lds) { WAITL(); ++pc; prog_post(lds + PPROG + j * 4, pc); }
DI void oproj_wave(const int j, const Ctx& c0, unsigned pc, lptr lds, const unsigned l0, const unsigned long long tag, int& nst) {
  const int lane = fresh_lane();
  const Ctx c = c0.fresh();
  const int mrot = ROT ? c.b : 0;
  const rsrc_t rwt = c.rwt(), rp = c.rp();
  const unsigned pa = l0 + APROG + (lane & 3) * 4, lv = lane * 16;
  f32x4 acc[5][7]; u32x4 af[4][5], bf[4];
#pragma unroll
  for (int k = 0; k < 3; ++k)
#pragma unroll
    for (int e = 0; e < 5; ++e) { af[k][e] = __builtin_amdgcn_raw_buffer_load_b128(rwt, lv + e * 1024, (((k + mrot) & 3) * 4 + j) * 5120, 16); SB(); }
#pragma unroll
  for (int e = 0; e < 5; ++e) { af[3][e] = u32x4{0u, 0u, 0u, 0u};
#pragma unroll
    for (int t = 0; t < 7; ++t) acc[e][t] = f32x4{0.f, 0.f, 0.f, 0.f}; }
#pragma unroll
  for (int i = 0; i < 4; ++i) bf[i] = u32x4{0u, 0u, 0u, 0u};
  asm volatile("s_nop 1"); SB();
  __builtin_amdgcn_s_setprio(PPRIO);
#pragma nounroll
  for (int hh = 0; hh < 8; ++hh) {
    const int r = hh >> 1, par = hh & 1;
    const unsigned reg = hh == 7 ? PO3 : ((r & 1) ? XS : PARK);
    if (!par) {
      prog_wait<32, 2>(pa, 8 * r + 9);
      PST();
      if (r < 3) {
        const Ctx cc = c0.fresh(); const rsrc_t ro = cc.ro(); unsigned l2 = lv; PIN(l2);
#pragma unroll
        for (int t = 0; t < 7; ++t) { __builtin_amdgcn_raw_buffer_store_b128(lds128(lp(l0 + reg + j * 7168 + l2) + t * 1024), ro, ((ABL & 1) ? OOB : 0u) + r * 28672 + j * 7168 + l2 + t * 1024, 0, 16); SB(); }
      }
    } else if (hh == 7) prog_wait<0, 1>(pa, 34);
    PST();
#pragma unroll
    for (int k = 0; k < 4; ++k) {
      if (k == 1 && !par && r < 3) { const Ctx cc = c0.fresh(); publish(cc.flags() + r * 16, lds + CNT + r * 4, tag, lv); }
      if (k == 2 && !par && r < 3 && !(ABL & 16)) { const Ctx cc = c0.fresh(); wait_flag(cc.pflags() + r * 16, __hip_atomic_load(cc.pflags() + r * 16, RLX_AGENT), tag); }
      WAITV(12);
      ++pc; prog_post(lds + PPROG + j * 4, pc);
      const unsigned pg = prog_read<16>(pa);
      lptr bp = lp(l0 + reg + ((k + mrot) & 3) * 1024 + lv);
      unsigned so = 0;
      SB();
#pragma unroll
      for (int u = 0; u < 7; ++u) {
        const int t = (u + 7 - LAG) % 7, set = u < LAG ? (k + 3) & 3 : k, gi = 7 * k + u;
#pragma unroll
        for (int e = 0; e < 5; ++e) {
          mfma_ip(acc[e][t], af[set][e], bf[(gi + 28 - LAG) & 3]);
          if (e == 0 && u >= 1) bf[gi & 3] = lds128(bp + u * 4096);
          if (u >= LAG && (e == 2 || (u == 6 && e == 4))) { const int n = e == 2 ? u - LAG : 4;
            af[(k + 3) & 3][n] = __builtin_amdgcn_raw_buffer_load_b128(rwt, lv + (n & 3) * 1024, so + (n >> 2) * 4096, 16); }
          SB();
        }
        if (u == 0) { prog_wait<16, 0>(pa, pg, pc); bf[gi & 3] = lds128(bp); SB(); }
        if (u == 2) {
          int h2 = hh; asm volatile("" : "+s"(h2));
          const int i3 = 4 * h2 + k + 3;
          so = (ABL & 2) ? ((i3 & 3) * 4 + j) * 5120 : i3 < 32 ? ((((i3 >> 2) & 1) * 16 + (i3 >> 3) * 4 + ((i3 + mrot) & 3)) * 4 + j) * 5120 : OOB;
        }
        if (u == 1) {
          int h2 = hh; asm volatile("" : "+s"(h2));
          const bool on = !(ABL & 4) && h2 < 6 && (h2 & 1) == (k < 2);
          const unsigned bo = ((k + 2 + mrot) & 3) * 1024 + j * 4096, gsrc = on ? (h2 >> 1) * 28672 + bo : OOB, ld = l0 + (on ? reg + bo : DUMMY);
          dma_sc1(rp, lv, gsrc, ld);
          dma_sc1(rp, lv, j < 3 ? gsrc + 16384 : OOB, on && j < 3 ? ld + 16384 : l0 + DUMMY);
          SB();
        }
      }
      if (k == 0 && hh == 2) {
        const Ctx cc = c0.fresh(); const rsrc_t rq = cc.rq();
#pragma unroll
        for (int t = 0; t < 7; ++t) dma_df(rq, lv, ((ABL & 8) ? OOB : 0u) + j * 7168 + t * 1024, l0 + PARK + j * 7168 + t * 1024);
      }
    }
  }
#pragma unroll
  for (int u = 0; u < LAG; ++u)
#pragma unroll
    for (int e = 0; e < 5; ++e) mfma_ip(acc[e][7 - LAG + u], af[3][e], bf[(u + 28 - LAG) & 3]);
  MFMA_DONE(); SB();
  PST();
  const Ctx c2 = c0.fresh(); const int g = c2.g; const rsrc_t rpx = c2.rpx(); gu64* xflag = c2.xflag();
  unsigned l3 = lv; PIN(l3);
  if (j == (g >> 1)) {
#pragma unroll
    for (int t = 0; t < 7; ++t)
#pragma unroll
      for (int e = 0; e < 5; ++e) *(LAS f32x4*)(lp(l0 + OWNP + l3) + (t * 5 + e) * 1024) = acc[e][t];
  } else {
    const unsigned po = (j * 4 + (g >> 1)) * 35840 + l3;
#pragma unroll
    for (int t = 0; t < 7; ++t)
#pragma unroll
      for (int e = 0; e < 5; ++e) __builtin_amdgcn_raw_buffer_store_b128(__builtin_bit_cast(u32x4, acc[e][t]), rpx, po + (t * 5 + e) * 1024, 0, 16);
    asm volatile("s_waitcnt vmcnt(0)" ::: "memory");
    if (lv == 0) __hip_atomic_store(xflag + j * 16 + (g >> 1), tag, RLX_AGENT);
  }
  __builtin_amdgcn_s_waitcnt(0x0F70);
}

struct QpState { f32x4 acc[4][7]; u32x4 xf[7]; lptr xa0, xa1; };
DI void qp_init(QpState& q, lptr lds, int lane) {
  const int lg = lane >> 4, lr = lane & 15;
  q.xa0 = lds + XS + lr * 128 + ((lg ^ (lr >> 1)) << 4); q.xa1 = lds + XS + lr * 128 + (((4 + lg) ^ (lr >> 1)) << 4);
#pragma unroll
  for (int t = 0; t < 7; ++t) { q.xf[t] = u32x4{0u, 0u, 0u, 0u};
#pragma unroll
    for (int e = 0; e < 4; ++e) q.acc[e][t] = f32x4{0.f, 0.f, 0.f, 0.f}; }
}
template <bool TR> DI void qp_pack(const QpState& q, u32x4 (&qf)[7][2]) {
#pragma unroll
  for (int t = 0; t < 7; ++t)
#pragma unroll
    for (int hf = 0; hf < 2; ++hf) {
#pragma unroll
      for (int d = 0; d < 4; ++d) { const int i0 = 8 * hf + 2 * d, i1 = i0 + 1;
        qf[t][hf][d] = TR ? cvtpk(q.acc[i0 >> 2][t][i0 & 3] * QSCALE, q.acc[i1 >> 2][t][i1 & 3] * QSCALE) : cvtpk(q.acc[i0 & 3][t][i0 >> 2] * QSCALE, q.acc[i1 & 3][t][i1 >> 2] * QSCALE); }
      PIN(qf[t][hf]);
    }
}
template <int PAR>
DI void qp2_chunk(QpState& q, u32x4 (&af)[4][4], const int c, const int j, const int rot, unsigned& pc, const unsigned pa, const unsigned xvo, const unsigned lv, lptr lds, const unsigned l0,
                  rsrc_t rx, rsrc_t rqt) {
  WAITV(8);
  ++pc; prog_post(lds + PPROG + j * 4, pc);
  const unsigned pg = prog_read<16>(pa);
  const int c1 = c + 1 + rot >= 10 ? c + 1 + rot - 10 : c + 1 + rot, c2 = c + 2 + rot >= 10 ? c + 2 + rot - 10 : c + 2 + rot;
  const unsigned s1 = c < 9 ? ((2 * c1 + 1) * 4 + j) * 4096 : OOB, s2 = c < 8 ? ((2 * c2) * 4 + j) * 4096 : OOB;
  SB();
#pragma unroll
  for (int u = 0; u < 14; ++u) {
    const int v = (u + 14 - LAG) % 14, t = v % 7, set = u < LAG ? 2 * (PAR ^ 1) + 1 : 2 * PAR + v / 7;
#pragma unroll
    for (int e = 0; e < 4; ++e) {
      q.acc[e][t] = mfma(af[set][e], q.xf[t], q.acc[e][t]);
      if (e == 0 && u >= 2) q.xf[u % 7] = lds128((u >= 7 ? q.xa1 : q.xa0) + PAR * XCH + (u % 7) * 2048);
      if (e == 2 && u >= LAG && u < LAG + 4) af[2 * (PAR ^ 1) + 1][u - LAG] = __builtin_amdgcn_raw_buffer_load_b128(rqt, lv + (u - LAG) * 1024, s1, 16);
      if (e == 2 && u >= 10) af[2 * PAR][u - 10] = __builtin_amdgcn_raw_buffer_load_b128(rqt, lv + (u - 10) * 1024, s2, 16);
      SB();
    }
    if (u == 0) { _Pragma("unroll") for (int e = 0; e < 4; ++e) PIN(q.acc[e][t]); prog_wait<16, 0>(pa, pg, pc); q.xf[0] = lds128(q.xa0 + PAR * XCH); q.xf[1] = lds128(q.xa0 + PAR * XCH + 2048); SB(); }
    if (u == 1) {
      const unsigned xs = c < 9 ? j * 10240 + c1 * 128 : OOB, xd = l0 + (c < 9 ? XS + (PAR ^ 1) * XCH + j * 1024 : DUMMY), st = c < 9 ? 4096u : 0u;
#pragma unroll
      for (int i = 0; i < 3; ++i) dma_df(rx, xvo, xs + i * 40960, xd + i * st);
      dma_df(rx, xvo, xs + 3 * 40960, j < 2 ? xd + 3 * st : l0 + DUMMY);
      SB();
    }
  }
}

DI void proj_wave(const int j, const int lane, const Ctx& c0, const int rot, lptr lds, const unsigned l0, rsrc_t rx, rsrc_t rwq, const char* wo, int& nst) {
  unsigned long long tag;
  if (PPRIO) __builtin_amdgcn_s_setprio(PPRIO);
  const int lg = lane >> 4, lr = lane & 15;
  QpState q;
  u32x4 qf[7][2];
  const unsigned qo = (4 * (j & 1) + lg) * 512 + lr * 16;
  {
    qp_init(q, lds, lane);
    u32x4 af[2][4];
#pragma unroll
    for (int e = 0; e < 4; ++e) af[1][e] = u32x4{0u, 0u, 0u, 0u};
    const int q4 = lr >> 2;
    const unsigned wa = l0 + (8 * lg + q4) * 512 + ((j * 8 + 2 * (lane & 3)) << 4 ^ (((q4 & 1) | ((q4 >> 1) << 3)) << 4));
    int buf = 0;
#pragma nounroll
    for (int c = 0; c < 10; ++c) {
      const unsigned wb0 = wa + buf * 2 * SLOT, wb1 = wb0 ^ 16; const int xo = buf * XCH;
      BAR_L();
#pragma unroll
      for (int u = 0; u < 14; ++u) {
        const int v = (u + 14 - LAG) % 14, t = v % 7, set = u < LAG ? 1 : v / 7;
        auto ard = [&](int ks, int e) { lptr p = lp((e >> 1) ? wb1 : wb0) + ks * 16384 + (e & 1) * 8; const u32x2 lo = ldstr(p), hi = ldstr(p + 2048); af[ks][e] = u32x4{lo[0], lo[1], hi[0], hi[1]}; };
        if (u == 0) { ard(0, 0); q.xf[0] = lds128(q.xa0 + xo); ard(0, 1); SB(); }
#pragma unroll
        for (int e = 0; e < 4; ++e) {
          q.acc[e][t] = mfma(af[set][e], q.xf[t], q.acc[e][t]);
          if (e == 0 && u >= 1) q.xf[u % 7] = lds128((u >= 7 ? q.xa1 : q.xa0) + xo + (u % 7) * 2048);
          if (u == 0 && (e & 1)) ard(0, 2 + (e >> 1));
          if (e == 2 && u >= 4 && u < 8) ard(1, u - 4);
          SB();
        }
      }
      buf = buf == 2 ? 0 : buf + 1;
      PST();
    }
#pragma unroll
    for (int t = 7 - LAG; t < 7; ++t)
#pragma unroll
      for (int e = 0; e < 4; ++e) q.acc[e][t] = mfma(af[1][e], q.xf[t], q.acc[e][t]);
    BAR_L();
    qp_pack<true>(q, qf);
    lptr qp = lds + ((j >> 1) ? PARK : XS) + qo;
#pragma unroll
    for (int t = 0; t < 7; ++t)
#pragma unroll
      for (int hf = 0; hf < 2; ++hf) *(LAS u32x4*)(qp + t * 4096 + hf * 256) = qf[t][hf];
    { const unsigned long long tg = *(const LAS unsigned long long*)(lds + TAGW); tag = ((unsigned long long)rfl((unsigned)(tg >> 32)) << 32) | rfl((unsigned)tg); }
    BAR_L();
    PST();
  }
  unsigned pc = 0;
  {
    const Ctx c = c0.fresh();
    const rsrc_t rqt = c.rqt();
    const unsigned pa = l0 + APROG + (lane & 3) * 4, lv = lane * 16;
    const unsigned xvo = (lane >> 3) * 1280 + (((lane & 7) ^ ((4 * (j & 1) + lg) & 7)) << 4);
    wait_flag(c.tflags() + (lane & 31), __hip_atomic_load(c.tflags() + (lane & 31), RLX_AGENT), tag);
    u32x4 af[4][4];
    const int r1 = rot == 9 ? 0 : rot + 1;
#pragma unroll
    for (int k = 0; k < 3; ++k)
#pragma unroll
      for (int e = 0; e < 4; ++e) { af[k][e] = __builtin_amdgcn_raw_buffer_load_b128(rqt, lv + e * 1024, ((k < 2 ? 2 * rot + k : 2 * r1) * 4 + j) * 4096, 16); SB(); }
#pragma unroll
    for (int e = 0; e < 4; ++e) af[3][e] = u32x4{0u, 0u, 0u, 0u};
    qp_init(q, lds, lane);
    prog_wait<32, 1>(pa, 1);
#pragma unroll
    for (int i = 0; i < 3; ++i) dma_df(rx, xvo, j * 10240 + rot * 128 + i * 40960, l0 + XS + j * 1024 + i * 4096);
    dma_df(rx, xvo, j * 10240 + rot * 128 + 3 * 40960, j < 2 ? l0 + XS + j * 1024 + 3 * 4096 : l0 + DUMMY);
    WAITV(0);
    PST();
#pragma nounroll
    for (int cc = 0; cc < 10; cc += 2) {
      qp2_chunk<0>(q, af, cc, j, rot, pc, pa, xvo, lv, lds, l0, rx, rqt);
      qp2_chunk<1>(q, af, cc + 1, j, rot, pc, pa, xvo, lv, lds, l0, rx, rqt);
      PST();
    }
#pragma unroll
    for (int t = 7 - LAG; t < 7; ++t)
#pragma unroll
      for (int e = 0; e < 4; ++e) q.acc[e][t] = mfma(af[3][e], q.xf[t], q.acc[e][t]);
    parrive(pc, j, lds);
    __builtin_amdgcn_s_setprio(2);
    qp_pack<false>(q, qf);
    prog_wait<16, 0>(pa, pc);
    if (j >= 2) {
#pragma unroll
      for (int t = 0; t < 7; ++t)
#pragma unroll
        for (int hf = 0; hf < 2; ++hf) __builtin_amdgcn_raw_buffer_store_b128(qf[t][hf], c.rq(), ((ABL & 8) ? OOB : 0u) + qo + t * 4096 + hf * 256, 0, 0);
    } else {
#pragma unroll
      for (int t = 0; t < 7; ++t)
#pragma unroll
        for (int hf = 0; hf < 2; ++hf) *(LAS u32x4*)(lds + XS + qo + t * 4096 + hf * 256) = qf[t][hf];
    }
    parrive(pc, j, lds);
    PST();
  }
  oproj_wave(j, c0, pc, lds, l0, tag, nst);
}

DI void final_part(const int w, const Ctx& c0, lptr lds, char* out, int& nst) {
  const Ctx c = c0.fresh(); const int b = c.b, g = c.g; const rsrc_t rpx = c.rpx(); gu64* xflag = c.xflag(); gu64* epoch = c.epoch();
  const int lane = fresh_lane(), p = g >> 1;
  const unsigned long long tag = *(const LAS unsigned long long*)(lds + TAGW);
  {
    gu64* f = xflag + p * 16 + ((lane & 3) == p ? (p + 1) & 3 : lane & 3);
    wait_flag(f, __hip_atomic_load(f, RLX_AGENT), tag);
  }
  if (w < 7) {
    const int lg = lane >> 4, lr = lane & 15;
    f32x4 in[3][5], sacc[5];
#pragma unroll
    for (int k = 0; k < 3; ++k)
#pragma unroll
      for (int e = 0; e < 5; ++e) in[k][e] = __builtin_bit_cast(f32x4, __builtin_amdgcn_raw_buffer_load_b128(rpx, lane * 16 + (p * 4 + k + (k >= p)) * 35840 + w * 5120 + e * 1024, 0, 16));
#pragma unroll
    for (int e = 0; e < 5; ++e) sacc[e] = (*(LAS f32x4*)(lds + OWNP + (w * 5 + e) * 1024 + lane * 16) + in[0][e]) + (in[1][e] + in[2][e]);
    char* orow = out + ((size_t)b * 112 + w * 16 + lr) * 1280 + (g & 1) * 640;
    const u32x4 f0 = {cvtpk(sacc[0][0], sacc[1][0]), cvtpk(sacc[2][0], sacc[3][0]), cvtpk(sacc[0][1], sacc[1][1]), cvtpk(sacc[2][1], sacc[3][1])};
    const u32x4 f1 = {cvtpk(sacc[0][2], sacc[1][2]), cvtpk(sacc[2][2], sacc[3][2]), cvtpk(sacc[0][3], sacc[1][3]), cvtpk(sacc[2][3], sacc[3][3])};
    *(u32x4*)(orow + p * 128 + lg * 32) = f0;
    *(u32x4*)(orow + p * 128 + lg * 32 + 16) = f1;
    *(u32x2*)(orow + 512 + p * 32 + lg * 8) = u32x2{cvtpk(sacc[4][0], sacc[4][1]), cvtpk(sacc[4][2], sacc[4][3])};
  } else if (g == 0 && b == 0 && lane == 0) {
    __hip_atomic_store(epoch, tag, RLX_AGENT);
  }
}

__global__ __launch_bounds__(512, 2) void attn_fused(const char* __restrict__ x, const char* __restrict__ K, const char* __restrict__ V,
                                                     const char* __restrict__ wq, const char* __restrict__ wo, char* __restrict__ ws, char* __restrict__ out) {
  extern __shared__ __attribute__((aligned(1024))) char smem[];
  const int w = rfl(threadIdx.x >> 6);
  const int bid = blockIdx.x;
  const int b = ((bid >> 1) & 3) * 8 + (bid >> 5), g = (bid & 1) * 4 + ((bid >> 3) & 3);
  const int rot = ROT ? (((b & 7) * 5) / 4 + ((g & 3) * 5) / 2) % 10 : 0;
  lptr lds = (lptr)smem;
  const unsigned l0 = (unsigned)(uintptr_t)lds;
  const Ctx c = {ws, b, g};
  const rsrc_t rx = mkrsrc(x + (size_t)b * 143360, 143360), rwq = mkrsrc(wq, 640 * 8192);
  int nst = 0; (void)nst;
  if (w < 4) {
    const size_t kvb = ((size_t)b * 32 + g * 4) * 131072;
    attn_wave(w, fresh_lane(), rot, lds, l0, mkrsrc(K + kvb, 524288), mkrsrc(V + kvb, 524288), rx, rwq, wo, c, nst);
  } else {
    proj_wave(w - 4, fresh_lane(), c, rot, lds, l0, rx, rwq, wo, nst);
  }
  BAR_L();
  ST();
  final_part(w, c, lds, out, nst);
}

extern "C" void kernel_launch(void* const* d_in, const int* in_sizes, int n_in, void* d_out, int out_size, void* d_ws, size_t ws_size, hipStream_t stream) {
  (void)hipFuncSetAttribute((const void*)attn_fused, hipFuncAttributeMaxDynamicSharedMemorySize, LDS_BYTES);
  attn_fused<<<256, 512, LDS_BYTES, stream>>>((const char*)d_in[0], (const char*)d_in[1], (const char*)d_in[2], (const char*)d_in[3], (const char*)d_in[4], (char*)d_ws, (char*)d_out);
}
```

```cpp
#include <hip/hip_runtime.h>
#include <stdint.h>
typedef __attribute__((ext_vector_type(8))) __bf16 bf16x8;
typedef __attribute__((ext_vector_type(2))) __bf16 bf16x2;
typedef __attribute__((ext_vector_type(4))) float f32x4;
typedef __attribute__((ext_vector_type(16))) float f32x16;
typedef __attribute__((ext_vector_type(2))) float f32x2;
typedef __attribute__((ext_vector_type(4))) unsigned u32x4;
typedef __attribute__((ext_vector_type(2))) unsigned u32x2;
typedef __attribute__((ext_vector_type(4))) short s16x4;
#define LAS __attribute__((address_space(3)))
#define DI __device__ __forceinline__
typedef LAS char* lptr;
typedef __amdgpu_buffer_rsrc_t rsrc_t;
typedef __attribute__((address_space(1))) unsigned long long gu64;
#define RLX_AGENT __ATOMIC_RELAXED, __HIP_MEMORY_SCOPE_AGENT

constexpr int SLOT = 16384, VOFF = 2 * SLOT, XCH = 14336, RA = 4 * SLOT, RB = RA + 2 * XCH, RC = RB + 2 * XCH, DUMMY = RC + 2 * XCH, XP = 6 * SLOT,
              TAGW = DUMMY + 4096, CNT = TAGW + 16, APROG = CNT + 32, PPROG = APROG + 16, QPROG = PPROG + 16, KPROG = QPROG + 16, STMP = CNT + 128, ARR = STMP + 512, LDS_BYTES = ARR + 1024;
constexpr float QSCALE = 0.08838834764831845f * 1.4426950408889634f;
constexpr unsigned OOB = 0x40000000u;
constexpr size_t WS_TF = 8192, WS_OF = 16384, WS_XF = WS_OF + 131072, WS_O = 1u << 20, WS_Q = 40u << 20, WS_WT = 48u << 20, WS_QT = 56u << 20, WS_PX = 64u << 20, WS_ST = 128u << 20;
constexpr int OWNP = 0;
constexpr unsigned P_Q23 = 12, P_R1 = 18, A_END = 100;

DI unsigned rfl(unsigned x) { return __builtin_amdgcn_readfirstlane(x); }
DI unsigned cvtpk(float lo, float hi) { f32x2 v = {lo, hi}; bf16x2 b = __builtin_convertvector(v, bf16x2); return __builtin_bit_cast(unsigned, b); }
DI f32x4 mfma(u32x4 a, u32x4 b, f32x4 c) { return __builtin_amdgcn_mfma_f32_16x16x32_bf16(__builtin_bit_cast(bf16x8, a), __builtin_bit_cast(bf16x8, b), c, 0, 0, 0); }
DI void mfma_ip(f32x4& c, const u32x4& a, const u32x4& b) { asm volatile("v_mfma_f32_16x16x32_bf16 %0, %1, %2, %0" : "+v"(c) : "v"(a), "v"(b)); }
#define MFMA_DONE() asm volatile("s_nop 15")
DI f32x16 mfma32(u32x4 a, u32x4 b, f32x16 c) { return __builtin_amdgcn_mfma_f32_32x32x16_bf16(__builtin_bit_cast(bf16x8, a), __builtin_bit_cast(bf16x8, b), c, 0, 0, 0); }
DI u32x4 lds128(lptr p) { return *(const LAS u32x4*)p; }
DI int fresh_lane() { int l; asm volatile("v_mbcnt_lo_u32_b32 %0, -1, 0\n\tv_mbcnt_hi_u32_b32 %0, -1, %0" : "=v"(l)); return l; }
DI lptr lp(unsigned a) { return (lptr)(uintptr_t)a; }
DI u32x2 ldstr(lptr p) { return __builtin_bit_cast(u32x2, __builtin_amdgcn_ds_read_tr16_b64_v4i16((LAS s16x4*)p)); }
DI rsrc_t mkrsrc(const void* p, unsigned bytes) { return __builtin_amdgcn_make_buffer_rsrc((void*)p, 0, bytes, 0x00020000); }
DI float xmax32(float m) { auto b = __builtin_amdgcn_permlane32_swap(__float_as_uint(m), __float_as_uint(m), false, false); return fmaxf(__uint_as_float(b[0]), __uint_as_float(b[1])); }
DI float xsum32(float m) { auto b = __builtin_amdgcn_permlane32_swap(__float_as_uint(m), __float_as_uint(m), false, false); return __uint_as_float(b[0]) + __uint_as_float(b[1]); }
DI float rowmax16(const f32x16& s) {
  float a = fmaxf(fmaxf(s[0], s[1]), s[2]), b = fmaxf(fmaxf(s[3], s[4]), s[5]);
  a = fmaxf(fmaxf(a, s[6]), s[7]); b = fmaxf(fmaxf(b, s[8]), s[9]); a = fmaxf(fmaxf(a, s[10]), s[11]); b = fmaxf(fmaxf(b, s[12]), s[13]);
  a = fmaxf(fmaxf(a, s[14]), s[15]);
  return xmax32(fmaxf(a, b));
}
#define DFPOL ""
#define TAUX 0
template <int OFF> DI void dma_nt(rsrc_t r, unsigned voff, unsigned soff, unsigned ldsaddr) {
  asm volatile("s_mov_b32 m0, %3\n\ts_nop 0\n\tbuffer_load_dwordx4 %0, %1, %2 offen offset:%4 nt lds" :: "v"(voff), "s"(r), "s"(soff), "s"(ldsaddr), "i"(OFF) : "memory");
}
DI void dma_df(rsrc_t r, unsigned voff, unsigned soff, unsigned ldsaddr) {
  asm volatile("s_mov_b32 m0, %3\n\ts_nop 0\n\tbuffer_load_dwordx4 %0, %1, %2 offen " DFPOL "lds" :: "v"(voff), "s"(r), "s"(soff), "s"(ldsaddr) : "memory");
}
DI void dma_sc1(rsrc_t r, unsigned voff, unsigned soff, unsigned ldsaddr) {
  asm volatile("s_mov_b32 m0, %3\n\ts_nop 0\n\tbuffer_load_dwordx4 %0, %1, %2 offen sc1 lds" :: "v"(voff), "s"(r), "s"(soff), "s"(ldsaddr) : "memory");
}
DI void touch(rsrc_t r, unsigned voff, unsigned ldsaddr) {
  asm volatile("s_mov_b32 m0, %2\n\ts_nop 0\n\tbuffer_load_dword %0, %1, 0 offen lds" :: "v"(voff), "s"(r), "s"(ldsaddr) : "memory");
}
#define SB() __builtin_amdgcn_sched_barrier(0)
#define WAITV(N) do { SB(); asm volatile("s_waitcnt vmcnt(" #N ") lgkmcnt(0)" ::: "memory"); SB(); } while (0)
#define WAITVM(N) do { SB(); asm volatile("s_waitcnt vmcnt(" #N ")" ::: "memory"); SB(); } while (0)
#define WAITL() do { SB(); asm volatile("s_waitcnt lgkmcnt(0)" ::: "memory"); SB(); } while (0)
#define BAR_L() do { SB(); asm volatile("s_waitcnt lgkmcnt(0)\n\ts_barrier" ::: "memory"); SB(); } while (0)
#define ST() do {} while (0)
#define PST() do {} while (0)
#define PST2() do {} while (0)
#define PCAP 0
#define PC() do { if (PCAP) asm volatile("s_waitcnt vmcnt(%0)" :: "i"(PCAP - 1) : "memory"); } while (0)
#define PIN(x) asm volatile("" : "+v"(x))
#define NODMA 0
#define DPOS 2
#define DSTR 8
#define CAP 16
#define SAUX 16
#define PPRIO 0
#define ABL 0
#define TE0 2
#define XPRIO 0
#define PF 0
#define VP0 2
#define VST 4
#define KP0 18
#define KST 4
#define QPACE 0
#define OPACE 0
#define OP0 26
#define ROT 1
#define QLD0 6
#define PFD 16
constexpr float PLIM = 1024.f;

template <int OFF> DI unsigned prog_read(unsigned pa) { return *(volatile LAS unsigned*)(lp(pa) + OFF); }
template <int OFF, int SLEEP> DI void prog_wait(unsigned pa, unsigned v, unsigned t) {
  unsigned n;
  asm volatile("s_mov_b32 %1, 0x1000000\n"
               "1:\n\tv_cmp_gt_u32 vcc, %3, %0\n\ts_cbranch_vccz 2f\n\ts_sleep %5\n\tds_read_b32 %0, %2 offset:%4\n\ts_sub_u32 %1, %1, 1\n\ts_waitcnt lgkmcnt(0)\n\ts_cbranch_scc0 1b\n2:"
               : "+v"(v), "=&s"(n) : "v"(pa), "s"(t), "i"(OFF), "i"(SLEEP) : "memory", "vcc", "scc");
}
template <int OFF, int SLEEP> DI void prog_wait(unsigned pa, unsigned t) { prog_wait<OFF, SLEEP>(pa, prog_read<OFF>(pa), t); }
DI void prog_post(lptr p, unsigned v) { *(volatile LAS unsigned*)p = v; }

DI void publish(gu64* flag, lptr cnt, unsigned long long tag, int lane) {
  asm volatile("s_waitcnt vmcnt(0)" ::: "memory");
  if (lane == 0) {
    const unsigned old = __hip_atomic_fetch_add((LAS unsigned*)cnt, 1u, __ATOMIC_RELAXED, __HIP_MEMORY_SCOPE_WORKGROUP);
    asm volatile("" : "+s"(tag));
    if (old == 3u) __hip_atomic_store(flag, tag, RLX_AGENT);
  }
}
DI void wait_flag(gu64* f, unsigned long long v, unsigned long long tag) {
#pragma nounroll
  for (unsigned spins = 0; spins < (1u << 15); ++spins) {
    if (__all(v == tag)) break;
    __builtin_amdgcn_s_sleep(2);
    v = __hip_atomic_load(f, RLX_AGENT);
  }
  __builtin_amdgcn_s_waitcnt(0x0F70);
  __builtin_amdgcn_fence(__ATOMIC_ACQUIRE, "wavefront");
  asm volatile("" ::: "memory");
}

struct Ctx {
  char* ws; int b, g;
  DI Ctx fresh() const { Ctx c = *this; asm volatile("" : "+s"(c.b), "+s"(c.g)); return c; }
  DI gu64* epoch() const { return (gu64*)ws; }
  DI gu64* tflags() const { return (gu64*)(ws + WS_TF + g * 256); }
  DI gu64* flags() const { return (gu64*)(ws + WS_OF + (b * 8 + g) * 512); }
  DI gu64* pflags() const { return (gu64*)(ws + WS_OF + (b * 8 + (g ^ 1)) * 512); }
  DI gu64* xflag() const { return (gu64*)(ws + WS_XF + (b * 2 + (g & 1)) * 512); }
  DI rsrc_t ro() const { return mkrsrc(ws + WS_O + (size_t)(b * 8 + g) * 114688, 114688); }
  DI rsrc_t rp() const { return mkrsrc(ws + WS_O + (size_t)(b * 8 + (g ^ 1)) * 114688, 114688); }
  DI rsrc_t rpx() const { return mkrsrc(ws + WS_PX + (size_t)(b * 2 + (g & 1)) * 286720, 286720); }
  DI rsrc_t rwt() const { return mkrsrc(ws + WS_WT + (size_t)g * 655360, 655360); }
  DI rsrc_t rqt() const { return mkrsrc(ws + WS_QT + (size_t)g * 327680, 327680); }
  DI rsrc_t rq() const { return mkrsrc(ws + WS_Q + (size_t)(b * 8 + g) * 28672, 28672); }
};
struct AttnState {
  u32x4 q[8], fr[8], p[4];
  f32x16 o[4], negm, sc[2][2];
  float l, ra, rb, inv;
  unsigned ka[8], va[4], kvo, vvo, pa, pg, oad;
};
DI unsigned kvx(unsigned v, unsigned x) { if (x) { PIN(v); v ^= x; } return v; }
DI int qreg(int r) { return r == 3 ? RC : ((r & 1) ? RB : RA); }
constexpr int NB = 8;
DI void sm_pair(AttnState& a, const f32x16 (&x)[2], const int n, const bool start) {
  const float e0 = __builtin_amdgcn_exp2f(x[n >> 3][2 * (n & 7)]), e1 = __builtin_amdgcn_exp2f(x[n >> 3][2 * (n & 7) + 1]);
  a.ra = start ? e0 : a.ra + e0; a.rb = start ? e1 : a.rb + e1;
  a.p[n >> 2][n & 3] = cvtpk(e0, e1);
  PIN(a.p[n >> 2][n & 3]); PIN(a.ra); PIN(a.rb);
}
template <int N0, int N1, bool ADJ>
DI void fixup(AttnState& a, f32x16 (&x)[2], f32x16 (&y)[2], float& rs) {
  float mx = x[N0 >> 3][2 * (N0 & 7)];
#pragma unroll
  for (int i = 2 * N0; i < 32; ++i) mx = fmaxf(mx, x[i >> 4][i & 15]);
  const float dl = __builtin_ceilf(fmaxf(xmax32(mx), 0.f)), sc = __builtin_amdgcn_exp2f(-dl);
  a.l *= sc; a.negm -= dl;
#pragma unroll
  for (int dt = 0; dt < 4; ++dt) a.o[dt] *= sc;
#pragma unroll
  for (int n = 0; n < N0; ++n) { const unsigned v = a.p[n >> 2][n & 3]; a.p[n >> 2][n & 3] = cvtpk(__uint_as_float(v << 16) * sc, __uint_as_float(v & 0xffff0000u) * sc); }
#pragma unroll
  for (int i = 2 * N0; i < 32; ++i) x[i >> 4][i & 15] -= dl;
  rs = 0.f;
#pragma unroll
  for (int n = N0; n < N1; ++n) { const float e0 = __builtin_amdgcn_exp2f(x[n >> 3][2 * (n & 7)]), e1 = __builtin_amdgcn_exp2f(x[n >> 3][2 * (n & 7) + 1]);
    rs += e0 + e1; a.p[n >> 2][n & 3] = cvtpk(e0, e1); }
  if (ADJ) { y[0] -= dl; y[1] -= dl; }
}
DI void o_frag(const AttnState& a, const int i, u32x4& f) {
  const int dt = i >> 1, e = 8 * (i & 1);
  f = u32x4{cvtpk(a.o[dt][e] * a.inv, a.o[dt][e + 1] * a.inv), cvtpk(a.o[dt][e + 2] * a.inv, a.o[dt][e + 3] * a.inv),
            cvtpk(a.o[dt][e + 4] * a.inv, a.o[dt][e + 5] * a.inv), cvtpk(a.o[dt][e + 6] * a.inv, a.o[dt][e + 7] * a.inv)};
}
template <bool V> DI void frag_read(AttnState& a, const int m, const int par) {
  if (!V) a.fr[m & 7] = lds128(lp(a.ka[m >> 1]) + par * SLOT + (m & 1) * 8192);
  else { lptr p = lp(a.va[m & 3]) + par * SLOT + (m >> 2) * 4096; const u32x2 v0 = ldstr(p), v1 = ldstr(p + 2048); a.fr[m & 7] = u32x4{v0[0], v0[1], v1[0], v1[1]}; }
}
template <bool CV> DI void phase_io(AttnState& a, const int i, const int cp, const int np, const unsigned val, const int w, lptr lds) {
  if (i <= 10) frag_read<CV>(a, i + 5, cp);
  if (i == 10) {
    WAITVM(8);
    prog_post(lds + APROG + w * 4, val);
    a.pg = prog_read<0>(a.pa);
  }
  if (i == 12) prog_wait<0, 0>(a.pa, a.pg, val);
  if (i == 13 || i == 14) { frag_read<!CV>(a, 2 * (i - 13), np); frag_read<!CV>(a, 2 * (i - 13) + 1, np); }
  if (i == 15) frag_read<!CV>(a, 4, np);
}
template <int KIND, int PAR>
DI void attn_step(AttnState& a, const int s, const int w, const int lane, lptr lds, const unsigned l0, rsrc_t rk, rsrc_t rv) {
  const unsigned pv = l0 + VOFF + PAR * SLOT + w * 4096, pk = l0 + PAR * SLOT + w * 4096, gv = s * SLOT, gk = (s + 2) * SLOT;
  const f32x16 zero = {0.f, 0.f, 0.f, 0.f, 0.f, 0.f, 0.f, 0.f, 0.f, 0.f, 0.f, 0.f, 0.f, 0.f, 0.f, 0.f};
  f32x16 (&c)[2] = a.sc[PAR]; f32x16 (&d)[2] = a.sc[PAR ^ 1];
  SB();
  if (KIND == 1) {
    prog_wait<16, 1>(a.pa, s < 16 ? 0u : P_Q23);
    const unsigned qa = l0 + qreg(s >> 3) + (lane >> 5) * 256 + (lane & 15) * 16;
    lptr qp = lp(qa + (w == 3 ? 6 : 2 * w + ((lane >> 4) & 1)) * 4096);
#pragma unroll
    for (int i = 0; i < 8; ++i) a.q[i] = lds128(qp + i * 512);
    a.oad = (s == 0 || (w == 3 && (lane & 16))) ? l0 + DUMMY + (lane >> 5) * 256 + (lane & 15) * 16 : qa + (2 * w + ((lane >> 4) & 1)) * 4096;
    SB();
  }
#pragma unroll
  for (int i = 0; i < 16; ++i) {
    c[i & 1] = mfma32(a.fr[i & 7], a.q[i >> 1], i >= 2 ? c[i & 1] : (KIND == 1 ? zero : a.negm));
    if (i == 12) PIN(c[0]);
    phase_io<false>(a, i, PAR, PAR ^ 1, 2 * s + 1, w, lds);
    { constexpr int PA[16] = {-1, 0, 1, -1, 2, 3, -1, 4, 5, -1, -1, 6, 7, -1, -1, -1}; if (PA[i] >= 0) sm_pair(a, d, NB + PA[i], PA[i] == 0); }
    if (KIND == 2 && i >= 8) { u32x4 f; o_frag(a, i - 8, f); *(LAS u32x4*)(lp(a.oad) + ((i - 8) >> 1) * 1024 + (i & 1) * 512) = f; }
    if (!NODMA) { if (i == 0) dma_nt<0>(rv, a.vvo, gv, pv); if (i == 3) dma_nt<1024>(rv, a.vvo, gv, pv); if (i == 6) dma_nt<2048>(rv, a.vvo, gv, pv); if (i == 9) dma_nt<3072>(rv, a.vvo, gv, pv); }
    SB();
  }
  if (KIND == 2) prog_post(lds + QPROG + w * 4, s);
  { float rs = a.ra + a.rb; PIN(rs);
    if (KIND != 2) { if (__builtin_expect(__any(!(rs <= PLIM)), 0)) fixup<NB, 16, KIND == 0>(a, d, c, rs); }
    PIN(rs); a.l += rs; PIN(a.l); }
  float m0 = 0.f;
  if (KIND == 1) { a.inv = __builtin_amdgcn_rcpf(xsum32(a.l)); a.l = 0.f; }
  SB();
#pragma unroll
  for (int i = 0; i < 16; ++i) {
    a.o[i & 3] = mfma32(a.fr[i & 7], a.p[i >> 2], (KIND == 2 && i < 4) ? zero : a.o[i & 3]);
    if (i == 12) PIN(a.o[0]);
    phase_io<true>(a, i, PAR ^ 1, PAR ^ 1, 2 * s + 2, w, lds);
    if (KIND == 1) {
      if (i == 1) { m0 = fmaxf(rowmax16(c[0]), rowmax16(c[1])); PIN(m0); }
      if (i == 2) { c[0] -= m0; PIN(c[0]); }
      if (i == 3) { c[1] -= m0; PIN(c[1]); }
      if (i == 5) { _Pragma("unroll") for (int e = 0; e < 16; ++e) a.negm[e] = -m0; PIN(a.negm); }
    }
    { constexpr int PB[16] = {-1, -1, -1, -1, 0, -1, 1, -1, 2, -1, 3, 4, 5, -1, 6, 7}; if (PB[i] >= 0) sm_pair(a, c, PB[i], PB[i] == 0); }
    if (!NODMA) { if (i == 0) dma_nt<0>(rk, kvx(a.kvo, 0), gk, pk); if (i == 3) dma_nt<1024>(rk, kvx(a.kvo, 64), gk, pk); if (i == 7) dma_nt<2048>(rk, kvx(a.kvo, 128), gk, pk); if (i == 9) dma_nt<3072>(rk, kvx(a.kvo, 192), gk, pk); }
    SB();
  }
  { float rs = a.ra + a.rb; PIN(rs);
    if (KIND != 1) { if (__builtin_expect(__any(!(rs <= PLIM)), 0)) fixup<0, NB, false>(a, c, c, rs); }
    PIN(rs); a.l += rs; PIN(a.l); }
}

DI void final_part(const int w, const Ctx& c0, lptr lds, const unsigned l0, const unsigned long long tag, char* out, int& nst) {
  const Ctx c = c0.fresh(); const int b = c.b, g = c.g; const rsrc_t rpx = c.rpx();
  const int lane = fresh_lane(), p = g >> 1, lg = lane >> 4, lr = lane & 15;
  {
    gu64* f = c.xflag() + p * 16 + ((lane & 3) == p ? (p + 1) & 3 : lane & 3);
    wait_flag(f, __hip_atomic_load(f, RLX_AGENT), tag);
  }
  ST();
  u32x4 in[2][3][2]; u32x2 in2[2][3];
#pragma unroll
  for (int i = 0; i < 2; ++i)
#pragma unroll
    for (int k = 0; k < 3; ++k) {
      const unsigned o = (i == 1 && w == 3) ? OOB : (p * 4 + k + (k >= p)) * 17920 + (w + 4 * i) * 2560;
      in[i][k][0] = __builtin_amdgcn_raw_buffer_load_b128(rpx, lane * 16 + o, 0, 16);
      in[i][k][1] = __builtin_amdgcn_raw_buffer_load_b128(rpx, lane * 16 + o + 1024, 0, 16);
      in2[i][k] = __builtin_amdgcn_raw_buffer_load_b64(rpx, lane * 8 + o + 2048, 0, 16);
    }
  if (w == 0 && g == 0 && b == 0 && lane == 0) __hip_atomic_store(c.epoch(), tag, RLX_AGENT);
  prog_wait<0, 1>(l0 + KPROG, 1);
  const rsrc_t rout = mkrsrc(out + (size_t)b * 143360, 143360);
  auto lo = [](unsigned v) { return __uint_as_float(v << 16); };
  auto hi = [](unsigned v) { return __uint_as_float(v & 0xffff0000u); };
#pragma unroll
  for (int i = 0; i < 2; ++i) {
    const int t = (i == 1 && w == 3) ? 0 : w + 4 * i;
    f32x4 a[5];
#pragma unroll
    for (int e = 0; e < 5; ++e) a[e] = *(LAS f32x4*)(lds + OWNP + (t * 5 + e) * 1024 + lane * 16);
    const unsigned ob = (i == 1 && w == 3) ? OOB : (t * 16 + lr) * 1280 + (g & 1) * 640;
#pragma unroll
    for (int hf = 0; hf < 2; ++hf) {
      u32x4 f;
#pragma unroll
      for (int d = 0; d < 4; ++d) { const int reg = 2 * hf + (d >> 1), e = 2 * (d & 1);
        f[d] = cvtpk((a[e][reg] + lo(in[i][0][hf][d])) + (lo(in[i][1][hf][d]) + lo(in[i][2][hf][d])), (a[e + 1][reg] + hi(in[i][0][hf][d])) + (hi(in[i][1][hf][d]) + hi(in[i][2][hf][d]))); }
      __builtin_amdgcn_raw_buffer_store_b128(f, rout, ob + p * 128 + lg * 32 + hf * 16, 0, 0);
    }
    u32x2 f2;
#pragma unroll
    for (int d = 0; d < 2; ++d)
      f2[d] = cvtpk((a[4][2 * d] + lo(in2[i][0][d])) + (lo(in2[i][1][d]) + lo(in2[i][2][d])), (a[4][2 * d + 1] + hi(in2[i][0][d])) + (hi(in2[i][1][d]) + hi(in2[i][2][d])));
    __builtin_amdgcn_raw_buffer_store_b64(f2, rout, ob + 512 + p * 32 + lg * 8, 0, 0);
  }
}

DI void attn_wave(const int w, const int lane, const int rot, lptr lds, const unsigned l0, rsrc_t rk, rsrc_t rv, rsrc_t rx, rsrc_t rwq, const char* wo, const Ctx& c0, char* out, int& nst) {
  const int lg = lane >> 4, lr = lane & 15, h = lane >> 5, r32 = lane & 31;
  ST();
  AttnState a;
  {
    const unsigned wvo = h * 8192 + (((lane & 31) ^ (h | ((w & 1) << 3))) << 4) + c0.g * 1024;
    const unsigned xvo = (lane >> 3) * 1280 + (((lane & 7) ^ ((4 * (w & 1) + lg) & 7)) << 4);
    auto issue = [&](int c, int buf) {
      const int cr = c + rot >= 10 ? c + rot - 10 : c + rot;
      const unsigned xs = w * 10240 + cr * 128, xd = l0 + XP + buf * XCH + w * 1024, ws_ = (cr * 64 + 2 * w) * 8192, wd = l0 + buf * 2 * SLOT + w * 1024;
#pragma unroll
      for (int i = 0; i < 3; ++i) dma_df(rx, xvo, xs + i * 40960, xd + i * 4096);
      dma_df(rx, xvo, xs + 3 * 40960, w < 2 ? xd + 3 * 4096 : l0 + DUMMY);
#pragma unroll
      for (int i = 0; i < 8; ++i) dma_df(rwq, wvo, ws_ + i * 65536, wd + i * 4096);
    };
    issue(0, 2); issue(1, 0);
    if (w == 0) { *(LAS u32x4*)(lds + CNT + (lane & 7) * 16) = u32x4{0u, 0u, 0u, 0u}; }
    const int b = c0.b, g = c0.g; const rsrc_t rwo = mkrsrc(wo, 4096 * 1280);
    u32x2 rw[8], rq2[8]; unsigned short r5[8]; unsigned long long e1;
    const unsigned rb = (16 * (lg >> 1) + 4 * (lg & 1)) * 1280 + (g & 1) * 640, wv0 = rb + w * 128 + lr * 8, wv1 = rb + 512 + w * 32 + lr * 2;
    const unsigned s2 = ((4 * (g ^ (b >> 4)) + ((b >> 2) & 3)) * 128 + (b & 3) * 32) * 1280;
    const unsigned qv = lg * 8 * 8192 + lr * 8 + w * 128 + g * 1024 + 512, sq = b < 20 ? b * 32 * 8192 : OOB;
    constexpr int TN[10] = {0, 0, 0, 9, 8, 8, 4, 0, 5, 0};
#pragma unroll
    for (int c = 0; c < 8; ++c) {
      SB(); asm volatile("s_waitcnt vmcnt(%0) lgkmcnt(0)\n\ts_barrier" :: "i"(12 + TN[c] + (c ? TN[c - 1] : 0)) : "memory"); SB();
      if (c == 5) { _Pragma("unroll") for (int i = 0; i < 8; ++i) PIN(rq2[i]); }
      if (c == 7) { _Pragma("unroll") for (int i = 0; i < 8; ++i) { PIN(rw[i]); PIN(r5[i]); } }
      SB();
      issue(c + 2, (c + 1) % 3);
      SB();
      if (c == 2) {
        e1 = __hip_atomic_load(c0.epoch(), RLX_AGENT) + 1;
#pragma unroll
        for (int jj = 0; jj < 8; ++jj) rq2[jj] = __builtin_amdgcn_raw_buffer_load_b64(rwq, qv, sq + jj * 8192, TAUX);
      }
      if (c == 3) {
#pragma unroll
        for (int jj = 0; jj < 8; ++jj) rw[jj] = __builtin_amdgcn_raw_buffer_load_b64(rwo, wv0 + (jj & 3) * 1280, s2 + (jj >> 2) * 8 * 1280, TAUX);
      }
      if (c == 4) {
#pragma unroll
        for (int jj = 0; jj < 8; ++jj) r5[jj] = __builtin_amdgcn_raw_buffer_load_b16(rwo, wv1 + (jj & 3) * 1280, s2 + (jj >> 2) * 8 * 1280, TAUX);
      }
      if (c == 5) {
#pragma unroll
        for (int e = 0; e < 4; ++e) { u32x4 f;
#pragma unroll
          for (int pp = 0; pp < 4; ++pp) f[pp] = __builtin_amdgcn_perm(rq2[2 * pp + 1][e >> 1], rq2[2 * pp][e >> 1], (e & 1) ? 0x07060302u : 0x05040100u);
          __builtin_amdgcn_raw_buffer_store_b128(f, c0.rqt(), (b * 4 + w) * 4096 + e * 1024 + lane * 16, 0, 16); }
      }
      if (c == 7) {
#pragma unroll
        for (int e = 0; e < 5; ++e) { u32x4 f;
#pragma unroll
          for (int pp = 0; pp < 4; ++pp) f[pp] = e < 4 ? __builtin_amdgcn_perm(rw[2 * pp + 1][(e >> 1) & 1], rw[2 * pp][(e >> 1) & 1], (e & 1) ? 0x07060302u : 0x05040100u)
                                                       : __builtin_amdgcn_perm((unsigned)r5[2 * pp + 1], (unsigned)r5[2 * pp], 0x05040100u);
          __builtin_amdgcn_raw_buffer_store_b128(f, c0.rwt(), (b * 4 + w) * 5120 + e * 1024 + lane * 16, 0, 16); }
      }
    }
    a.kvo = (16 * w + lg) * 256 + ((lr ^ lg) << 4);
    a.vvo = (16 * w + lg) * 256 + ((lr ^ (lg << 2)) << 4);
    const unsigned d = l0 + w * 4096;
    WAITV(17); BAR_L();
    {
      const unsigned long long tag = ((unsigned long long)rfl((unsigned)(e1 >> 32)) << 32) | rfl((unsigned)e1);
      *(LAS unsigned long long*)(lds + TAGW) = tag;
      publish(c0.tflags() + b, lds + CNT + 16, tag, lane);
    }
    dma_nt<0>(rk, kvx(a.kvo, 0), 0, d); dma_nt<1024>(rk, kvx(a.kvo, 64), 0, d); dma_nt<2048>(rk, kvx(a.kvo, 128), 0, d); dma_nt<3072>(rk, kvx(a.kvo, 192), 0, d);
    dma_nt<0>(rk, kvx(a.kvo, 0), SLOT, d + SLOT); dma_nt<1024>(rk, kvx(a.kvo, 64), SLOT, d + SLOT); dma_nt<2048>(rk, kvx(a.kvo, 128), SLOT, d + SLOT); dma_nt<3072>(rk, kvx(a.kvo, 192), SLOT, d + SLOT);
    BAR_L();
    BAR_L();
  }
#pragma unroll
  for (int i = 0; i < 8; ++i) { a.ka[i] = l0 + r32 * 256 + (((2 * i + h) ^ lr) << 4); PIN(a.ka[i]); }
#pragma unroll
  for (int i = 0; i < 4; ++i) { a.va[i] = l0 + VOFF + (4 * h + (lr >> 2)) * 256 + ((i ^ (lr >> 2)) << 6) + (lg & 1) * 32 + (lane & 3) * 8; PIN(a.va[i]); }
  a.l = 0.f; a.pg = 0; a.pa = l0 + APROG + (lane & 3) * 4; a.inv = 0.f; a.oad = 0;
#pragma unroll
  for (int i = 0; i < 4; ++i) a.p[i] = u32x4{0u, 0u, 0u, 0u};
#pragma unroll
  for (int i = 0; i < 16; ++i) { a.o[0][i] = a.o[1][i] = a.o[2][i] = a.o[3][i] = 0.f; a.sc[1][0][i] = a.sc[1][1][i] = 0.f; a.negm[i] = 0.f; }
  WAITV(4);
  BAR_L(); ST();
#pragma unroll
  for (int m = 0; m < 5; ++m) frag_read<false>(a, m, 0);
#pragma nounroll
  for (int r = 0; r < 4; ++r) {
    attn_step<1, 0>(a, 8 * r, w, lane, lds, l0, rk, rv); ST();
    attn_step<2, 1>(a, 8 * r + 1, w, lane, lds, l0, rk, rv); ST();
#pragma nounroll
    for (int t = 2; t < 8; t += 2) {
      attn_step<0, 0>(a, 8 * r + t, w, lane, lds, l0, rk, rv); ST();
      attn_step<0, 1>(a, 8 * r + t + 1, w, lane, lds, l0, rk, rv); ST();
    }
  }
  {
    WAITVM(4);
    prog_post(lds + APROG + w * 4, 65);
#pragma unroll
    for (int i = 0; i < 16 - NB; ++i) sm_pair(a, a.sc[1], NB + i, i == 0);
    prog_wait<0, 0>(a.pa, 65);
#pragma unroll
    for (int m = 0; m < 8; ++m) frag_read<true>(a, m, 1);
    float rs = a.ra + a.rb; PIN(rs);
    if (__builtin_expect(__any(!(rs <= PLIM)), 0)) fixup<NB, 16, false>(a, a.sc[1], a.sc[1], rs);
    PIN(rs); a.l += rs;
    SB();
#pragma unroll
    for (int i = 0; i < 16; ++i) { a.o[i & 3] = mfma32(a.fr[i & 7], a.p[i >> 2], a.o[i & 3]); if (i < 8) frag_read<true>(a, i + 8, 1); SB(); }
    a.inv = __builtin_amdgcn_rcpf(xsum32(a.l));
  }
  prog_wait<16, 1>(a.pa, P_R1);
  const Ctx c = c0.fresh();
  const rsrc_t ro = c.ro(), rp = c.rp(); gu64* flags = c.flags(); gu64* pflags = c.pflags();
  {
    const bool pad = w == 3 && (lg & 1);
    const unsigned io = (2 * w + (lg & 1)) * 4096 + h * 256 + lr * 16, go = pad ? OOB : 3 * 28672 + io, lo = l0 + (pad ? DUMMY + h * 256 + lr * 16 : RB + io);
#pragma unroll
    for (int i = 0; i < 8; ++i) {
      u32x4 f; o_frag(a, i, f);
      __builtin_amdgcn_raw_buffer_store_b128(f, ro, go + (i >> 1) * 1024 + (i & 1) * 512, 0, 16);
      *(LAS u32x4*)(lp(lo) + (i >> 1) * 1024 + (i & 1) * 512) = f;
    }
  }
  prog_post(lds + QPROG + w * 4, 33);
  ST();
  const unsigned long long tg = *(const LAS unsigned long long*)(lds + TAGW), tag = ((unsigned long long)rfl((unsigned)(tg >> 32)) << 32) | rfl((unsigned)tg);
  publish(flags + 3 * 16, lds + CNT + 12, tag, lane);
  wait_flag(pflags + 3 * 16, __hip_atomic_load(pflags + 3 * 16, RLX_AGENT), tag);
  {
    u32x4 f[7];
#pragma unroll
    for (int i = 0; i < 7; ++i) f[i] = __builtin_amdgcn_raw_buffer_load_b128(rp, 3 * 28672 + (w * 7 + i) * 1024 + lane * 16, 0, 16);
#pragma unroll
    for (int i = 0; i < 7; ++i) *(LAS u32x4*)(lds + RA + (w * 7 + i) * 1024 + lane * 16) = f[i];
  }
  prog_post(lds + APROG + w * 4, A_END);
  ST();
  final_part(w, c0, lds, l0, tag, out, nst);
}

constexpr int LAG = 3;
DI void parrive(unsigned& pc, const int j, lptr lds) { WAITL(); ++pc; prog_post(lds + PPROG + j * 4, pc); }
DI void oproj_last(const bool KEEP, f32x4 (&acc)[5][7], u32x4 (&af)[4][5], u32x4 (&bf)[4], const int mrot, const unsigned lv, const unsigned l0, rsrc_t rpx, const unsigned po) {
  unsigned ba[4];
#pragma unroll
  for (int k = 0; k < 4; ++k) ba[k] = l0 + RA + ((k + mrot) & 3) * 1024 + lv;
  auto out = [&](int t, int part) {
    if (KEEP) { *(LAS f32x4*)(lp(l0 + OWNP + lv) + (t * 5 + part) * 1024) = acc[part][t]; if (part == 3) *(LAS f32x4*)(lp(l0 + OWNP + lv) + (t * 5 + 4) * 1024) = acc[4][t]; }
    else if (part < 2) {
      const u32x4 f = {cvtpk(acc[0][t][2 * part], acc[1][t][2 * part]), cvtpk(acc[2][t][2 * part], acc[3][t][2 * part]),
                       cvtpk(acc[0][t][2 * part + 1], acc[1][t][2 * part + 1]), cvtpk(acc[2][t][2 * part + 1], acc[3][t][2 * part + 1])};
      __builtin_amdgcn_raw_buffer_store_b128(f, rpx, po + lv + t * 2560 + part * 1024, 0, 16);
    } else if (part == 2) __builtin_amdgcn_raw_buffer_store_b64(u32x2{cvtpk(acc[4][t][0], acc[4][t][1]), cvtpk(acc[4][t][2], acc[4][t][3])}, rpx, po + (lv >> 1) + t * 2560 + 2048, 0, 16);
  };
#pragma unroll
  for (int gi = 0; gi < LAG; ++gi) bf[gi] = lds128(lp(ba[gi & 3]) + (gi >> 2) * 4096);
  SB();
#pragma unroll
  for (int gi = 0; gi < 28; ++gi) {
    const int t = gi >> 2, k = gi & 3;
#pragma unroll
    for (int e = 0; e < 5; ++e) {
      mfma_ip(acc[e][t], af[k][e], bf[gi & 3]);
      if (e == 0 && gi + LAG < 28) bf[(gi + LAG) & 3] = lds128(lp(ba[(gi + LAG) & 3]) + ((gi + LAG) >> 2) * 4096);
      if (e == 4 && k == 0 && t > 0) asm volatile("s_nop 3");
      if (e == 4 && t > 0) out(t - 1, k);
      SB();
    }
  }
  MFMA_DONE(); SB();
#pragma unroll
  for (int k = 0; k < 4; ++k) out(6, k);
}
DI void oproj_wave(const int j, const Ctx& c0, unsigned pc, lptr lds, const unsigned l0, const unsigned long long tag, int& nst) {
  const int lane = fresh_lane();
  const Ctx c = c0.fresh();
  const int mrot = ROT ? c.b : 0;
  const rsrc_t rwt = c.rwt();
  const unsigned pa = l0 + APROG + (lane & 3) * 4, lv = lane * 16;
  f32x4 acc[5][7]; u32x4 af[4][5], bf[4];
#pragma unroll
  for (int k = 0; k < 3; ++k)
#pragma unroll
    for (int e = 0; e < 5; ++e) { af[k][e] = __builtin_amdgcn_raw_buffer_load_b128(rwt, lv + e * 1024, (((k + mrot) & 3) * 4 + j) * 5120, 16); SB(); }
#pragma unroll
  for (int e = 0; e < 5; ++e)
#pragma unroll
    for (int t = 0; t < 7; ++t) acc[e][t] = f32x4{0.f, 0.f, 0.f, 0.f};
  asm volatile("s_nop 1"); SB();
  __builtin_amdgcn_s_setprio(PPRIO);
#pragma nounroll
  for (int hh = 0; hh < 7; ++hh) {
    const int r = hh >> 1, par = hh & 1;
    const unsigned reg = hh == 6 ? RB : qreg(r + 1);
    if (!par) {
      prog_wait<32, 2>(pa, 8 * r + 9);
      if (r < 3) {
        const rsrc_t ro = c0.fresh().ro(); unsigned l2 = lv; PIN(l2);
#pragma unroll
        for (int t = 0; t < 4; ++t) bf[t] = lds128(lp(l0 + reg + j * 7168 + l2) + t * 1024);
#pragma unroll
        for (int t = 0; t < 7; ++t) {
          PC(); __builtin_amdgcn_raw_buffer_store_b128(bf[t & 3], ro, ((ABL & 1) ? OOB : 0u) + r * 28672 + j * 7168 + l2 + t * 1024, 0, 16);
          if (t < 3) bf[t] = lds128(lp(l0 + reg + j * 7168 + l2) + (t + 4) * 1024);
          SB();
        }
      }
    } else {
      const Ctx cc = c0.fresh();
      parrive(pc, j, lds);
      if (!(ABL & 16)) wait_flag(cc.pflags() + r * 16, __hip_atomic_load(cc.pflags() + r * 16, RLX_AGENT), tag);
      prog_wait<16, 0>(pa, pc);
      const rsrc_t rp = cc.rp();
#pragma unroll
      for (int t = 0; t < 7; ++t) { PC(); dma_sc1(rp, lv, ((ABL & 4) ? OOB : 0u) + r * 28672 + j * 7168 + t * 1024, l0 + reg + j * 7168 + t * 1024); }
      WAITVM(0);
      parrive(pc, j, lds);
      prog_wait<16, 0>(pa, pc);
    }
    PST();
    auto baddr = [&](int gi) { return lp(l0 + reg + ((gi / 7 + mrot) & 3) * 1024 + lv) + (gi % 7) * 4096; };
#pragma unroll
    for (int gi = 0; gi < LAG; ++gi) bf[gi] = lds128(baddr(gi));
    SB();
#pragma unroll
    for (int k = 0; k < 4; ++k) {
      if (k == 2 && !par && r < 3) publish(c0.fresh().flags() + r * 16, lds + CNT + r * 4, tag, lv);
      int h2 = hh; asm volatile("" : "+s"(h2));
      if (OPACE) prog_wait<0, 2>(pa, h2 < 6 ? OP0 + (((4 * h2 + k) * OPACE) >> 2) : 0);
      const int i3 = 4 * h2 + k + 3;
      const unsigned so = (ABL & 2) ? ((i3 & 3) * 4 + j) * 5120 : ((((i3 >> 2) & 1) * 16 + (i3 >> 3) * 4 + ((i3 + mrot) & 3)) * 4 + j) * 5120;
#pragma unroll
      for (int t = 0; t < 7; ++t) {
        const int gi = 7 * k + t;
#pragma unroll
        for (int e = 0; e < 5; ++e) {
          mfma_ip(acc[e][t], af[k][e], bf[gi & 3]);
          if (e == 0 && gi + LAG < 28) bf[(gi + LAG) & 3] = lds128(baddr(gi + LAG));
          if (e == 2 && t >= 1 && t <= 5) PC();
          if (e == 2 && t >= 1 && t <= 5) af[(k + 3) & 3][t - 1] = __builtin_amdgcn_raw_buffer_load_b128(rwt, lv + ((t - 1) & 3) * 1024, so + ((t - 1) >> 2) * 4096, 16);
          SB();
        }
      }
    }
    if (par) parrive(pc, j, lds);
    PST();
  }
#pragma unroll
  for (int e = 0; e < 5; ++e) af[3][e] = __builtin_amdgcn_raw_buffer_load_b128(rwt, lv + (e & 3) * 1024, ((28 + ((3 + mrot) & 3)) * 4 + j) * 5120 + (e >> 2) * 4096, 16);
  prog_wait<0, 1>(pa, A_END);
  PST();
  const Ctx c2 = c0.fresh(); const int p = c2.g >> 1;
  oproj_last(j == p, acc, af, bf, mrot, lv, l0, c2.rpx(), (j * 4 + p) * 17920);
  if (j == p) prog_post(lds + KPROG, 1);
  else {
    asm volatile("s_waitcnt vmcnt(0)" ::: "memory");
    if (lv == 0) __hip_atomic_store(c2.xflag() + j * 16 + p, tag, RLX_AGENT);
  }
  PST();
  __builtin_amdgcn_s_waitcnt(0x0F70);
}

struct QpState { f32x4 acc[4][7]; u32x4 xf[7]; lptr xa0, xa1; };
DI void qp_init(QpState& q, lptr xb, int lane) {
  const int lg = lane >> 4, lr = lane & 15;
  q.xa0 = xb + lr * 128 + ((lg ^ (lr >> 1)) << 4); q.xa1 = xb + lr * 128 + (((4 + lg) ^ (lr >> 1)) << 4);
#pragma unroll
  for (int t = 0; t < 7; ++t) { q.xf[t] = u32x4{0u, 0u, 0u, 0u};
#pragma unroll
    for (int e = 0; e < 4; ++e) q.acc[e][t] = f32x4{0.f, 0.f, 0.f, 0.f}; }
}
template <bool TR> DI void qp_pack(const QpState& q, u32x4 (&qf)[7][2]) {
#pragma unroll
  for (int t = 0; t < 7; ++t)
#pragma unroll
    for (int hf = 0; hf < 2; ++hf) {
#pragma unroll
      for (int d = 0; d < 4; ++d) { const int i0 = 8 * hf + 2 * d, i1 = i0 + 1;
        qf[t][hf][d] = TR ? cvtpk(q.acc[i0 >> 2][t][i0 & 3] * QSCALE, q.acc[i1 >> 2][t][i1 & 3] * QSCALE) : cvtpk(q.acc[i0 & 3][t][i0 >> 2] * QSCALE, q.acc[i1 & 3][t][i1 >> 2] * QSCALE); }
      PIN(qf[t][hf]);
    }
}
template <int PAR>
DI void qp2_chunk(QpState& q, u32x4 (&af)[4][4], const int c, const int j, const int rot, unsigned& pc, const unsigned pa, const unsigned xvo, const unsigned lv, lptr lds, const unsigned l0,
                  rsrc_t rx, rsrc_t rqt) {
  if (QPACE) prog_wait<0, 2>(pa, (c * QPACE) >> 2);
  WAITV(8);
  ++pc; prog_post(lds + PPROG + j * 4, pc);
  const unsigned pg = prog_read<16>(pa);
  const int c1 = c + 1 + rot >= 10 ? c + 1 + rot - 10 : c + 1 + rot, c2 = c + 2 + rot >= 10 ? c + 2 + rot - 10 : c + 2 + rot;
  const unsigned s1 = c < 9 ? ((2 * c1 + 1) * 4 + j) * 4096 : OOB, s2 = c < 8 ? ((2 * c2) * 4 + j) * 4096 : OOB;
  SB();
#pragma unroll
  for (int u = 0; u < 14; ++u) {
    const int v = (u + 14 - LAG) % 14, t = v % 7, set = u < LAG ? 2 * (PAR ^ 1) + 1 : 2 * PAR + v / 7;
#pragma unroll
    for (int e = 0; e < 4; ++e) {
      mfma_ip(q.acc[e][t], af[set][e], q.xf[t]);
      if (e == 0 && u >= 2) q.xf[u % 7] = lds128((u >= 7 ? q.xa1 : q.xa0) + PAR * XCH + (u % 7) * 2048);
      if (e == 2 && ((u >= LAG && u < LAG + 4) || u >= 10)) PC();
      if (e == 2 && u >= LAG && u < LAG + 4) af[2 * (PAR ^ 1) + 1][u - LAG] = __builtin_amdgcn_raw_buffer_load_b128(rqt, lv + (u - LAG) * 1024, s1, 16);
      if (e == 2 && u >= 10) af[2 * PAR][u - 10] = __builtin_amdgcn_raw_buffer_load_b128(rqt, lv + (u - 10) * 1024, s2, 16);
      SB();
    }
    if (u == 0) { prog_wait<16, 0>(pa, pg, pc); q.xf[0] = lds128(q.xa0 + PAR * XCH); q.xf[1] = lds128(q.xa0 + PAR * XCH + 2048); SB(); }
    if (u == 1) {
      const unsigned xs = c < 9 ? j * 10240 + c1 * 128 : OOB, xd = l0 + (c < 9 ? RC + (PAR ^ 1) * XCH + j * 1024 : DUMMY), st = c < 9 ? 4096u : 0u;
#pragma unroll
      for (int i = 0; i < 3; ++i) { PC(); dma_df(rx, xvo, xs + i * 40960, xd + i * st); }
      PC(); dma_df(rx, xvo, xs + 3 * 40960, j < 2 ? xd + 3 * st : l0 + DUMMY);
      SB();
    }
  }
}

DI void proj_wave(const int j, const int lane, const Ctx& c0, const int rot, lptr lds, const unsigned l0, rsrc_t rx, rsrc_t rwq, const char* wo, int& nst) {
  unsigned long long tag, tv;
  if (PPRIO) __builtin_amdgcn_s_setprio(PPRIO);
  const int lg = lane >> 4, lr = lane & 15;
  QpState q;
  u32x4 qf[7][2];
  const unsigned qo = (4 * (j & 1) + lg) * 512 + lr * 16;
  {
    if (PF) {
      const unsigned n = j * 64 + lane, b = c0.b, g = c0.g;
      for (int i = 0; i < PF; ++i) __builtin_amdgcn_s_sleep(1);
      const unsigned iw = (b & 7) * 224 + n;
      touch(rwq, n < 224 ? (192 + (iw >> 2)) * 8192 + g * 1024 + (iw & 3) * 128 : OOB, l0 + DUMMY);
      touch(rx, n < 196 ? (28 * (g & 3) + n / 7) * 1280 + (3 + n % 7) * 128 : OOB, l0 + DUMMY);
      touch(rwq, n < 128 && b < 20 ? (32 * b + (n >> 2)) * 8192 + g * 1024 + 512 + (n & 3) * 128 : OOB, l0 + DUMMY);
      touch(mkrsrc(wo, 4096 * 1280), n < 160 ? ((4 * (g ^ (b >> 4)) + ((b >> 2) & 3)) * 128 + (b & 3) * 32 + n / 5) * 1280 + (g & 1) * 640 + (n % 5) * 128 : OOB, l0 + DUMMY);
    }
    qp_init(q, lds + XP, lane);
    u32x4 af[2][4];
#pragma unroll
    for (int e = 0; e < 4; ++e) af[1][e] = u32x4{0u, 0u, 0u, 0u};
    const int q4 = lr >> 2;
    const unsigned wa = l0 + (8 * lg + q4) * 512 + ((j * 8 + 2 * (lane & 3)) << 4 ^ (((q4 & 1) | ((q4 >> 1) << 3)) << 4));
    int buf = 2;
#pragma nounroll
    for (int c = 0; c < 10; ++c) {
      const unsigned wb0 = wa + buf * 2 * SLOT, wb1 = wb0 ^ 16; const int xo = buf * XCH;
      BAR_L();
#pragma unroll
      for (int u = 0; u < 14; ++u) {
        const int v = (u + 14 - LAG) % 14, t = v % 7, set = u < LAG ? 1 : v / 7;
        auto ard = [&](int ks, int e) { lptr p = lp((e >> 1) ? wb1 : wb0) + ks * 16384 + (e & 1) * 8; const u32x2 lo = ldstr(p), hi = ldstr(p + 2048); af[ks][e] = u32x4{lo[0], lo[1], hi[0], hi[1]}; };
        if (u == 0) { ard(0, 0); q.xf[0] = lds128(q.xa0 + xo); ard(0, 1); SB(); }
#pragma unroll
        for (int e = 0; e < 4; ++e) {
          mfma_ip(q.acc[e][t], af[set][e], q.xf[t]);
          if (e == 0 && u >= 1) q.xf[u % 7] = lds128((u >= 7 ? q.xa1 : q.xa0) + xo + (u % 7) * 2048);
          if (u == 0 && (e & 1)) ard(0, 2 + (e >> 1));
          if (e == 2 && u >= 4 && u < 8) ard(1, u - 4);
          SB();
        }
      }
      buf = buf == 2 ? 0 : buf + 1;
      PST();
    }
#pragma unroll
    for (int t = 7 - LAG; t < 7; ++t)
#pragma unroll
      for (int e = 0; e < 4; ++e) mfma_ip(q.acc[e][t], af[1][e], q.xf[t]);
    MFMA_DONE(); SB();
    BAR_L();
    qp_pack<true>(q, qf);
    lptr qp = lds + ((j >> 1) ? RB : RA) + qo;
#pragma unroll
    for (int t = 0; t < 7; ++t)
#pragma unroll
      for (int hf = 0; hf < 2; ++hf) *(LAS u32x4*)(qp + t * 4096 + hf * 256) = qf[t][hf];
    { const unsigned long long tg = *(const LAS unsigned long long*)(lds + TAGW); tag = ((unsigned long long)rfl((unsigned)(tg >> 32)) << 32) | rfl((unsigned)tg); }
    tv = __hip_atomic_load(c0.fresh().tflags() + (lane & 31), RLX_AGENT);
    BAR_L();
    PST();
  }
  unsigned pc = 0;
  {
    const Ctx c = c0.fresh();
    const rsrc_t rqt = c.rqt();
    const unsigned pa = l0 + APROG + (lane & 3) * 4, lv = lane * 16;
    const unsigned xvo = (lane >> 3) * 1280 + (((lane & 7) ^ ((4 * (j & 1) + lg) & 7)) << 4);
#pragma unroll
    for (int i = 0; i < 3; ++i) dma_df(rx, xvo, j * 10240 + rot * 128 + i * 40960, l0 + RC + j * 1024 + i * 4096);
    dma_df(rx, xvo, j * 10240 + rot * 128 + 3 * 40960, j < 2 ? l0 + RC + j * 1024 + 3 * 4096 : l0 + DUMMY);
    wait_flag(c.tflags() + (lane & 31), tv, tag);
    u32x4 af[4][4];
    const int r1 = rot == 9 ? 0 : rot + 1;
#pragma unroll
    for (int k = 0; k < 3; ++k)
#pragma unroll
      for (int e = 0; e < 4; ++e) { af[k][e] = __builtin_amdgcn_raw_buffer_load_b128(rqt, lv + e * 1024, ((k < 2 ? 2 * rot + k : 2 * r1) * 4 + j) * 4096, 16); SB(); }
#pragma unroll
    for (int e = 0; e < 4; ++e) af[3][e] = u32x4{0u, 0u, 0u, 0u};
    qp_init(q, lds + RC, lane);
    asm volatile("s_nop 1"); SB();
    PST();
#pragma nounroll
    for (int cc = 0; cc < 10; cc += 2) {
      qp2_chunk<0>(q, af, cc, j, rot, pc, pa, xvo, lv, lds, l0, rx, rqt);
      qp2_chunk<1>(q, af, cc + 1, j, rot, pc, pa, xvo, lv, lds, l0, rx, rqt);
      PST();
    }
#pragma unroll
    for (int t = 7 - LAG; t < 7; ++t)
#pragma unroll
      for (int e = 0; e < 4; ++e) mfma_ip(q.acc[e][t], af[3][e], q.xf[t]);
    MFMA_DONE(); SB();
    parrive(pc, j, lds);
    __builtin_amdgcn_s_setprio(XPRIO);
    qp_pack<false>(q, qf);
    prog_wait<16, 0>(pa, pc);
    prog_wait<32, 0>(pa, 1);
#pragma unroll
    for (int t = 0; t < 7; ++t)
#pragma unroll
      for (int hf = 0; hf < 2; ++hf) *(LAS u32x4*)(lds + ((j >> 1) ? RC : RA) + qo + t * 4096 + hf * 256) = qf[t][hf];
    parrive(pc, j, lds);
    PST();
  }
  oproj_wave(j, c0, pc, lds, l0, tag, nst);
}

__global__ __launch_bounds__(512, 2) void attn_fused(const char* __restrict__ x, const char* __restrict__ K, const char* __restrict__ V,
                                                     const char* __restrict__ wq, const char* __restrict__ wo, char* __restrict__ ws, char* __restrict__ out) {
  extern __shared__ __attribute__((aligned(1024))) char smem[];
  const int w = rfl(threadIdx.x >> 6);
  const int bid = blockIdx.x;
  const int b = ((bid >> 1) & 3) * 8 + (bid >> 5), g = (bid & 1) * 4 + ((bid >> 3) & 3);
  const int rot = ROT ? (((b & 7) * 5) / 4 + ((g & 3) * 5) / 2) % 10 : 0;
  lptr lds = (lptr)smem;
  const unsigned l0 = (unsigned)(uintptr_t)lds;
  const Ctx c = {ws, b, g};
  const rsrc_t rx = mkrsrc(x + (size_t)b * 143360, 143360), rwq = mkrsrc(wq, 640 * 8192);
  int nst = 0; (void)nst;
  if (w < 4) {
    const size_t kvb = ((size_t)b * 32 + g * 4) * 131072;
    attn_wave(w, fresh_lane(), rot, lds, l0, mkrsrc(K + kvb, 524288), mkrsrc(V + kvb, 524288), rx, rwq, wo, c, out, nst);
  } else {
    proj_wave(w - 4, fresh_lane(), c, rot, lds, l0, rx, rwq, wo, nst);
  }
}

extern "C" void kernel_launch(void* const* d_in, const int* in_sizes, int n_in, void* d_out, int out_size, void* d_ws, size_t ws_size, hipStream_t stream) {
  (void)hipFuncSetAttribute((const void*)attn_fused, hipFuncAttributeMaxDynamicSharedMemorySize, LDS_BYTES);
  attn_fused<<<256, 512, LDS_BYTES, stream>>>((const char*)d_in[0], (const char*)d_in[1], (const char*)d_in[2], (const char*)d_in[3], (const char*)d_in[4], (char*)d_ws, (char*)d_out);
}
```

```cpp
#include <hip/hip_runtime.h>
#include <stdint.h>
typedef __attribute__((ext_vector_type(8))) __bf16 bf16x8;
typedef __attribute__((ext_vector_type(2))) __bf16 bf16x2;
typedef __attribute__((ext_vector_type(4))) float f32x4;
typedef __attribute__((ext_vector_type(16))) float f32x16;
typedef __attribute__((ext_vector_type(2))) float f32x2;
typedef __attribute__((ext_vector_type(4))) unsigned u32x4;
typedef __attribute__((ext_vector_type(2))) unsigned u32x2;
typedef __attribute__((ext_vector_type(4))) short s16x4;
#define LAS __attribute__((address_space(3)))
#define DI __device__ __forceinline__
typedef LAS char* lptr;
typedef __amdgpu_buffer_rsrc_t rsrc_t;
typedef __attribute__((address_space(1))) unsigned long long gu64;
#define RLX_AGENT __ATOMIC_RELAXED, __HIP_MEMORY_SCOPE_AGENT

constexpr int SLOT = 16384, VOFF = 2 * SLOT, XCH = 14336, RA = 4 * SLOT, RB = RA + 2 * XCH, RC = RB + 2 * XCH, DUMMY = RC + 2 * XCH, XP = 6 * SLOT,
              TAGW = DUMMY + 4096, CNT = TAGW + 16, APROG = CNT + 32, PPROG = APROG + 16, QPROG = PPROG + 16, KPROG = QPROG + 16, STMP = CNT + 128, ARR = STMP + 512, LDS_BYTES = ARR + 1024;
constexpr float QSCALE = 0.08838834764831845f * 1.4426950408889634f;
constexpr unsigned OOB = 0x40000000u;
constexpr size_t WS_TF = 8192, WS_OF = 16384, WS_XF = WS_OF + 131072, WS_O = 1u << 20, WS_Q = 40u << 20, WS_WT = 48u << 20, WS_QT = 56u << 20, WS_PX = 64u << 20, WS_ST = 128u << 20;
constexpr int OWNP = 0;
constexpr unsigned P_Q23 = 12, P_R1 = 18, A_END = 100;

DI unsigned rfl(unsigned x) { return __builtin_amdgcn_readfirstlane(x); }
DI unsigned cvtpk(float lo, float hi) { f32x2 v = {lo, hi}; bf16x2 b = __builtin_convertvector(v, bf16x2); return __builtin_bit_cast(unsigned, b); }
DI f32x4 mfma(u32x4 a, u32x4 b, f32x4 c) { return __builtin_amdgcn_mfma_f32_16x16x32_bf16(__builtin_bit_cast(bf16x8, a), __builtin_bit_cast(bf16x8, b), c, 0, 0, 0); }
DI void mfma_ip(f32x4& c, const u32x4& a, const u32x4& b) { asm volatile("v_mfma_f32_16x16x32_bf16 %0, %1, %2, %0" : "+v"(c) : "v"(a), "v"(b)); }
#define MFMA_DONE() asm volatile("s_nop 15")
DI f32x16 mfma32(u32x4 a, u32x4 b, f32x16 c) { return __builtin_amdgcn_mfma_f32_32x32x16_bf16(__builtin_bit_cast(bf16x8, a), __builtin_bit_cast(bf16x8, b), c, 0, 0, 0); }
DI u32x4 lds128(lptr p) { return *(const LAS u32x4*)p; }
DI int fresh_lane() { int l; asm volatile("v_mbcnt_lo_u32_b32 %0, -1, 0\n\tv_mbcnt_hi_u32_b32 %0, -1, %0" : "=v"(l)); return l; }
DI lptr lp(unsigned a) { return (lptr)(uintptr_t)a; }
DI u32x2 ldstr(lptr p) { return __builtin_bit_cast(u32x2, __builtin_amdgcn_ds_read_tr16_b64_v4i16((LAS s16x4*)p)); }
DI rsrc_t mkrsrc(const void* p, unsigned bytes) { return __builtin_amdgcn_make_buffer_rsrc((void*)p, 0, bytes, 0x00020000); }
DI float xmax(float m) {
  auto a = __builtin_amdgcn_permlane16_swap(__float_as_uint(m), __float_as_uint(m), false, false); m = fmaxf(__uint_as_float(a[0]), __uint_as_float(a[1]));
  auto b = __builtin_amdgcn_permlane32_swap(__float_as_uint(m), __float_as_uint(m), false, false); return fmaxf(__uint_as_float(b[0]), __uint_as_float(b[1]));
}
DI float xsum(float m) {
  auto a = __builtin_amdgcn_permlane16_swap(__float_as_uint(m), __float_as_uint(m), false, false); m = __uint_as_float(a[0]) + __uint_as_float(a[1]);
  auto b = __builtin_amdgcn_permlane32_swap(__float_as_uint(m), __float_as_uint(m), false, false); return __uint_as_float(b[0]) + __uint_as_float(b[1]);
}
#define DFPOL ""
#define TAUX 0
template <int OFF> DI void dma_nt(rsrc_t r, unsigned voff, unsigned soff, unsigned ldsaddr) {
  asm volatile("s_mov_b32 m0, %3\n\ts_nop 0\n\tbuffer_load_dwordx4 %0, %1, %2 offen offset:%4 nt lds" :: "v"(voff), "s"(r), "s"(soff), "s"(ldsaddr), "i"(OFF) : "memory");
}
DI void dma_df(rsrc_t r, unsigned voff, unsigned soff, unsigned ldsaddr) {
  asm volatile("s_mov_b32 m0, %3\n\ts_nop 0\n\tbuffer_load_dwordx4 %0, %1, %2 offen " DFPOL "lds" :: "v"(voff), "s"(r), "s"(soff), "s"(ldsaddr) : "memory");
}
DI void dma_sc1(rsrc_t r, unsigned voff, unsigned soff, unsigned ldsaddr) {
  asm volatile("s_mov_b32 m0, %3\n\ts_nop 0\n\tbuffer_load_dwordx4 %0, %1, %2 offen sc1 lds" :: "v"(voff), "s"(r), "s"(soff), "s"(ldsaddr) : "memory");
}
DI void touch(rsrc_t r, unsigned voff, unsigned ldsaddr) {
  asm volatile("s_mov_b32 m0, %2\n\ts_nop 0\n\tbuffer_load_dword %0, %1, 0 offen lds" :: "v"(voff), "s"(r), "s"(ldsaddr) : "memory");
}
#define SB() __builtin_amdgcn_sched_barrier(0)
#define WAITV(N) do { SB(); asm volatile("s_waitcnt vmcnt(" #N ") lgkmcnt(0)" ::: "memory"); SB(); } while (0)
#define WAITVM(N) do { SB(); asm volatile("s_waitcnt vmcnt(" #N ")" ::: "memory"); SB(); } while (0)
#define WAITL() do { SB(); asm volatile("s_waitcnt lgkmcnt(0)" ::: "memory"); SB(); } while (0)
#define BAR_L() do { SB(); asm volatile("s_waitcnt lgkmcnt(0)\n\ts_barrier" ::: "memory"); SB(); } while (0)
#define ST() do {} while (0)
#define PST() do {} while (0)
#define PST2() do {} while (0)
#define PCAP 0
#define PC() do { if (PCAP) asm volatile("s_waitcnt vmcnt(%0)" :: "i"(PCAP - 1) : "memory"); } while (0)
#define PIN(x) asm volatile("" : "+v"(x))
#define NODMA 0
#define DPOS 2
#define DSTR 8
#define CAP 16
#define SAUX 16
#define PPRIO 0
#define ABL 0
#define TE0 2
#define XPRIO 0
#define PF 0
#define VP0 2
#define VST 4
#define KP0 18
#define KST 4
#define QPACE 0
#define OPACE 0
#define OP0 26
#define MAP 4
#define OSA 16
#define WSOFF 0
#define CODEPF 0
#define NH 2
#define SSL 0
#define DSL 0
#define ROT 1
#define QLD0 6
#define PFD 16
constexpr float PLIM = 1024.f;

template <int OFF> DI unsigned prog_read(unsigned pa) { return *(volatile LAS unsigned*)(lp(pa) + OFF); }
template <int OFF, int SLEEP> DI void prog_wait(unsigned pa, unsigned v, unsigned t) {
  unsigned n;
  asm volatile("s_mov_b32 %1, 0x1000000\n"
               "1:\n\tv_cmp_gt_u32 vcc, %3, %0\n\ts_cbranch_vccz 2f\n\ts_sleep %5\n\tds_read_b32 %0, %2 offset:%4\n\ts_sub_u32 %1, %1, 1\n\ts_waitcnt lgkmcnt(0)\n\ts_cbranch_scc0 1b\n2:"
               : "+v"(v), "=&s"(n) : "v"(pa), "s"(t), "i"(OFF), "i"(SLEEP) : "memory", "vcc", "scc");
}
template <int OFF, int SLEEP> DI void prog_wait(unsigned pa, unsigned t) { prog_wait<OFF, SLEEP>(pa, prog_read<OFF>(pa), t); }
DI void prog_post(lptr p, unsigned v) { *(volatile LAS unsigned*)p = v; }

DI void publish(gu64* flag, lptr cnt, unsigned long long tag, int lane) {
  asm volatile("s_waitcnt vmcnt(0)" ::: "memory");
  if (lane == 0) {
    const unsigned old = __hip_atomic_fetch_add((LAS unsigned*)cnt, 1u, __ATOMIC_RELAXED, __HIP_MEMORY_SCOPE_WORKGROUP);
    asm volatile("" : "+s"(tag));
    if (old == 3u) __hip_atomic_store(flag, tag, RLX_AGENT);
  }
}
DI void wait_flag(gu64* f, unsigned long long v, unsigned long long tag) {
#pragma nounroll
  for (unsigned spins = 0; spins < (1u << 15); ++spins) {
    if (__all(v == tag)) break;
    __builtin_amdgcn_s_sleep(2);
    v = __hip_atomic_load(f, RLX_AGENT);
  }
  __builtin_amdgcn_s_waitcnt(0x0F70);
  __builtin_amdgcn_fence(__ATOMIC_ACQUIRE, "wavefront");
  asm volatile("" ::: "memory");
}

struct Ctx {
  char* ws; int b, g;
  DI Ctx fresh() const { Ctx c = *this; asm volatile("" : "+s"(c.b), "+s"(c.g)); return c; }
  DI gu64* epoch() const { return (gu64*)ws; }
  DI gu64* tflags() const { return (gu64*)(ws + WS_TF + g * 256); }
  DI gu64* flags() const { return (gu64*)(ws + WS_OF + (b * 8 + g) * 512); }
  DI gu64* pflags() const { return (gu64*)(ws + WS_OF + (b * 8 + (g ^ 1)) * 512); }
  DI gu64* xflag() const { return (gu64*)(ws + WS_XF + (b * 2 + (g & 1)) * 512); }
  DI rsrc_t ro() const { return mkrsrc(ws + WS_O + (size_t)(b * 8 + g) * 114688, 114688); }
  DI rsrc_t rp() const { return mkrsrc(ws + WS_O + (size_t)(b * 8 + (g ^ 1)) * 114688, 114688); }
  DI rsrc_t rpx() const { return mkrsrc(ws + WS_PX + (size_t)(b * 2 + (g & 1)) * 286720, 286720); }
  DI rsrc_t rwt() const { return mkrsrc(ws + WS_WT + (size_t)g * 655360, 655360); }
  DI rsrc_t rqt() const { return mkrsrc(ws + WS_QT + (size_t)g * 327680, 327680); }
  DI rsrc_t rq() const { return mkrsrc(ws + WS_Q + (size_t)(b * 8 + g) * 28672, 28672); }
};
template <int NQ> struct AttnState {
  u32x4 q[NQ][4], fr[8], p[2][NQ];
  f32x4 o[8][NQ], negm[NQ], sc[2][4][NQ];
  float l[NQ], rs[NQ], inv[NQ];
  unsigned ka[4], va[8], kvo, vvo, pa, pg, oad;
};

DI int qreg(int r) { return r == 3 ? RC : ((r & 1) ? RB : RA); }
template <int NQ> DI void sm_pair(AttnState<NQ>& a, const f32x4 (&x)[4][NQ], u32x4 (&p)[NQ], const int hf, const int n) {
  const int t = n >> 2, d = n & 3;
  if (t >= NQ) return;
  const float e0 = __builtin_amdgcn_exp2f(x[2 * hf + (d >> 1)][t][2 * (d & 1)]), e1 = __builtin_amdgcn_exp2f(x[2 * hf + (d >> 1)][t][2 * (d & 1) + 1]);
  a.rs[t] = d ? (a.rs[t] + e0) + e1 : e0 + e1;
  p[t][d] = cvtpk(e0, e1);
  PIN(p[t][d]); PIN(a.rs[t]);
}
template <int NQ, int HF, bool ADJ>
DI void fixup(AttnState<NQ>& a, f32x4 (&x)[4][NQ], f32x4 (&y)[4][NQ]) {
#pragma unroll
  for (int t = 0; t < NQ; ++t) {
    float mx = x[3][t][3];
#pragma unroll
    for (int i = 8 * HF; i < 15; ++i) mx = fmaxf(mx, x[i >> 2][t][i & 3]);
    const float dl = __builtin_ceilf(fmaxf(xmax(mx), 0.f)), sc = __builtin_amdgcn_exp2f(-dl);
    a.l[t] *= sc; a.negm[t] -= dl;
#pragma unroll
    for (int dt = 0; dt < 8; ++dt) a.o[dt][t] *= sc;
    if (HF) {
#pragma unroll
      for (int d = 0; d < 4; ++d) { const unsigned v = a.p[0][t][d]; a.p[0][t][d] = cvtpk(__uint_as_float(v << 16) * sc, __uint_as_float(v & 0xffff0000u) * sc); }
    }
#pragma unroll
    for (int kt = 2 * HF; kt < 4; ++kt) x[kt][t] -= dl;
    float r = 0.f;
#pragma unroll
    for (int d = 0; d < 4; ++d) { const float e0 = __builtin_amdgcn_exp2f(x[2 * HF + (d >> 1)][t][2 * (d & 1)]), e1 = __builtin_amdgcn_exp2f(x[2 * HF + (d >> 1)][t][2 * (d & 1) + 1]);
      r += e0 + e1; a.p[HF][t][d] = cvtpk(e0, e1); }
    a.rs[t] = r;
    if (ADJ) {
#pragma unroll
      for (int kt = 0; kt < 4; ++kt) y[kt][t] -= dl;
    }
  }
}
template <int NQ, int HF, bool CHECK, bool ADJ>
DI void sm_close(AttnState<NQ>& a, f32x4 (&x)[4][NQ], f32x4 (&y)[4][NQ]) {
  if (CHECK) {
    if (NQ == 1) PIN(a.p[HF][0]);
    float m = NQ == 2 ? a.rs[0] + a.rs[NQ - 1] : a.rs[0]; PIN(m);
    if (__builtin_expect(__any(!(m <= PLIM)), 0)) fixup<NQ, HF, ADJ>(a, x, y);
  }
#pragma unroll
  for (int t = 0; t < NQ; ++t) { PIN(a.rs[t]); a.l[t] += a.rs[t]; PIN(a.l[t]); }
}
template <int NQ> DI u32x4 o_frag(const AttnState<NQ>& a, const int m, const int t) {
  const f32x4 x = a.o[2 * m][t] * a.inv[t], y = a.o[2 * m + 1][t] * a.inv[t];
  return u32x4{cvtpk(x[0], x[1]), cvtpk(x[2], x[3]), cvtpk(y[0], y[1]), cvtpk(y[2], y[3])};
}
template <bool V, int NQ> DI void frag_read(AttnState<NQ>& a, const int m, const int par) {
  if (!V) a.fr[m & 7] = lds128(lp(a.ka[m & 3]) + par * SLOT + (m >> 2) * 4096);
  else { lptr p = lp(a.va[m & 7]) + par * SLOT + (m >> 3) * 8192; const u32x2 v0 = ldstr(p), v1 = ldstr(p + 4096); a.fr[m & 7] = u32x4{v0[0], v0[1], v1[0], v1[1]}; }
}
template <int NQ> constexpr int npiece() { return NQ == 2 ? NH : 16 - 3 * NH; }
template <bool V, int NQ, int N> DI void tile_piece(const AttnState<NQ>& a, rsrc_t r, const unsigned gsrc, const unsigned dst) {
  constexpr int P = NQ == 2 ? N : 3 * NH + N;
  dma_nt<(P & 3) * 1024>(r, V ? a.vvo ^ ((P & 1) * 128) : a.kvo ^ ((P & 3) * 64), gsrc + (P >> 2) * 4096, dst + (P >> 2) * 4096);
}
template <bool V, int NQ, int I> DI void tile_issue(const AttnState<NQ>& a, rsrc_t r, const unsigned gsrc, const unsigned dst) {
  constexpr int NP = npiece<NQ>(), n0 = (I * NP + 9) / 10, n1 = ((I + 1) * NP + 9) / 10;
  if constexpr (n1 > n0) tile_piece<V, NQ, n0>(a, r, gsrc, dst);
  if constexpr (n1 > n0 + 1) tile_piece<V, NQ, n0 + 1>(a, r, gsrc, dst);
}
template <bool V, int NQ> DI void tile_issue_at(const AttnState<NQ>& a, const int i, rsrc_t r, const unsigned gsrc, const unsigned dst) {
  if (NODMA) return;
  if (i == 0) tile_issue<V, NQ, 0>(a, r, gsrc, dst); if (i == 1) tile_issue<V, NQ, 1>(a, r, gsrc, dst); if (i == 2) tile_issue<V, NQ, 2>(a, r, gsrc, dst);
  if (i == 3) tile_issue<V, NQ, 3>(a, r, gsrc, dst); if (i == 4) tile_issue<V, NQ, 4>(a, r, gsrc, dst); if (i == 5) tile_issue<V, NQ, 5>(a, r, gsrc, dst);
  if (i == 6) tile_issue<V, NQ, 6>(a, r, gsrc, dst); if (i == 7) tile_issue<V, NQ, 7>(a, r, gsrc, dst); if (i == 8) tile_issue<V, NQ, 8>(a, r, gsrc, dst);
  if (i == 9) tile_issue<V, NQ, 9>(a, r, gsrc, dst);
}
template <int N> DI void waitvm() { SB(); asm volatile("s_waitcnt vmcnt(%0)" :: "i"(N) : "memory"); SB(); }
template <bool CV, int NQ> DI void phase_io(AttnState<NQ>& a, const int i, const int cp, const int np, const unsigned val, const int w, lptr lds) {
  if (i <= 10) frag_read<CV>(a, i + 5, cp);
  if (i == 10) {
    waitvm<2 * npiece<NQ>()>();
    prog_post(lds + APROG + w * 4, val);
    a.pg = prog_read<0>(a.pa);
  }
  if (i == 12) prog_wait<0, NQ == 1>(a.pa, a.pg, val);
  if (i == 13 || i == 14) { frag_read<!CV>(a, 2 * (i - 13), np); frag_read<!CV>(a, 2 * (i - 13) + 1, np); }
  if (i == 15) frag_read<!CV>(a, 4, np);
}
template <int KIND, int PAR, int NQ>
DI void attn_step(AttnState<NQ>& a, const int s, const int w, const int lane, lptr lds, const unsigned l0, rsrc_t rk, rsrc_t rv) {
  const unsigned wb = NQ == 2 ? w * NH * 1024 : 0;
  const unsigned pv = l0 + VOFF + PAR * SLOT + wb, pk = l0 + PAR * SLOT + wb, gv = s * SLOT + wb, gk = (s + 2) * SLOT + wb;
  const f32x4 zero = {0.f, 0.f, 0.f, 0.f};
  f32x4 (&c)[4][NQ] = a.sc[PAR]; f32x4 (&d)[4][NQ] = a.sc[PAR ^ 1];
  SB();
  if (KIND == 1) {
    prog_wait<16, 1>(a.pa, s < 16 ? 0u : P_Q23);
    const unsigned qa = l0 + qreg(s >> 3) + 2 * w * 4096 + lane * 16;
#pragma unroll
    for (int i = 0; i < 4; ++i)
#pragma unroll
      for (int t = 0; t < NQ; ++t) a.q[t][i] = lds128(lp(qa) + t * 4096 + i * 1024);
    a.oad = qa;
    SB();
  }
#pragma unroll
  for (int i = 0; i < 16; ++i) {
    constexpr int PA[16] = {-1, 0, 4, -1, 1, 5, -1, 2, 6, -1, -1, 3, 7, -1, -1, -1};
#pragma unroll
    for (int t = 0; t < NQ; ++t) {
      c[i >> 2][t] = mfma(a.fr[i & 7], a.q[t][i & 3], (i & 3) ? c[i >> 2][t] : (KIND == 1 ? zero : a.negm[t]));
      if (t == 0) { if (PA[i] >= 0) sm_pair(a, d, a.p[1], 1, PA[i]); SB(); }
    }
    if (i == 12) PIN(c[3][NQ - 1]);
    phase_io<false>(a, i, PAR, PAR ^ 1, 2 * s + 1, w, lds);
    if (KIND == 2 && i >= 8 && ((i - 8) >> 2) < NQ) *(LAS u32x4*)(lp(a.oad) + ((i - 8) >> 2) * 4096 + (i & 3) * 1024) = o_frag(a, i & 3, ((i - 8) >> 2) % NQ);
    tile_issue_at<true>(a, i, rv, gv, pv);
    SB();
  }
  if (KIND == 2) prog_post(lds + QPROG + w * 4, s);
  sm_close<NQ, 1, KIND != 2, KIND == 0>(a, d, c);
  if (KIND == 1) {
#pragma unroll
    for (int t = 0; t < NQ; ++t) { a.inv[t] = __builtin_amdgcn_rcpf(xsum(a.l[t])); a.l[t] = 0.f; }
  }
  SB();
  u32x4 pn[NQ];
#pragma unroll
  for (int i = 0; i < 16; ++i) {
    constexpr int PB[16] = {-1, -1, -1, 0, -1, 4, -1, 1, -1, 5, -1, 2, 6, -1, 3, 7};
#pragma unroll
    for (int t = 0; t < NQ; ++t) {
      a.o[i & 7][t] = mfma(a.fr[i & 7], a.p[i >> 3][t], (KIND == 2 && i < 8) ? zero : a.o[i & 7][t]);
      if (t == 0) { if (PB[i] >= 0) sm_pair(a, c, pn, 0, PB[i]); SB(); }
    }
    if (i == 12) PIN(a.o[4][NQ - 1]);
    phase_io<true>(a, i, PAR ^ 1, PAR ^ 1, 2 * s + 2, w, lds);
    if (KIND == 1 && (i == 1 || i == 2) && i - 1 < NQ) {
      const int t = (i - 1) % NQ;
      float m0 = c[3][t][3];
#pragma unroll
      for (int e = 0; e < 15; ++e) m0 = fmaxf(m0, c[e >> 2][t][e & 3]);
      m0 = xmax(m0);
#pragma unroll
      for (int kt = 0; kt < 4; ++kt) { c[kt][t] -= m0; PIN(c[kt][t]); }
      a.negm[t] = f32x4{-m0, -m0, -m0, -m0}; PIN(a.negm[t]);
    }
    tile_issue_at<false>(a, i, rk, gk, pk);
    SB();
  }
#pragma unroll
  for (int t = 0; t < NQ; ++t) a.p[0][t] = pn[t];
  sm_close<NQ, 0, KIND != 1, false>(a, c, c);
}

DI void final_part(const int w, const Ctx& c0, lptr lds, const unsigned l0, const unsigned long long tag, char* out, int& nst) {
  const Ctx c = c0.fresh(); const int b = c.b, g = c.g; const rsrc_t rpx = c.rpx();
  const int lane = fresh_lane(), p = g >> 1, lg = lane >> 4, lr = lane & 15;
  {
    gu64* f = c.xflag() + p * 16 + ((lane & 3) == p ? (p + 1) & 3 : lane & 3);
    wait_flag(f, __hip_atomic_load(f, RLX_AGENT), tag);
  }
  ST();
  u32x4 in[2][3][2]; u32x2 in2[2][3];
#pragma unroll
  for (int i = 0; i < 2; ++i)
#pragma unroll
    for (int k = 0; k < 3; ++k) {
      const unsigned o = (i == 1 && w == 3) ? OOB : (p * 4 + k + (k >= p)) * 17920 + (w + 4 * i) * 2560;
      in[i][k][0] = __builtin_amdgcn_raw_buffer_load_b128(rpx, lane * 16 + o, 0, 16);
      in[i][k][1] = __builtin_amdgcn_raw_buffer_load_b128(rpx, lane * 16 + o + 1024, 0, 16);
      in2[i][k] = __builtin_amdgcn_raw_buffer_load_b64(rpx, lane * 8 + o + 2048, 0, 16);
    }
  if (w == 0 && g == 0 && b == 0 && lane == 0) __hip_atomic_store(c.epoch(), tag, RLX_AGENT);
  prog_wait<0, 1>(l0 + KPROG, 1);
  const rsrc_t rout = mkrsrc(out + (size_t)b * 143360, 143360);
  auto lo = [](unsigned v) { return __uint_as_float(v << 16); };
  auto hi = [](unsigned v) { return __uint_as_float(v & 0xffff0000u); };
#pragma unroll
  for (int i = 0; i < 2; ++i) {
    const int t = (i == 1 && w == 3) ? 0 : w + 4 * i;
    f32x4 a[5];
#pragma unroll
    for (int e = 0; e < 5; ++e) a[e] = *(LAS f32x4*)(lds + OWNP + (t * 5 + e) * 1024 + lane * 16);
    const unsigned ob = (i == 1 && w == 3) ? OOB : (t * 16 + lr) * 1280 + (g & 1) * 640;
#pragma unroll
    for (int hf = 0; hf < 2; ++hf) {
      u32x4 f;
#pragma unroll
      for (int d = 0; d < 4; ++d) { const int reg = 2 * hf + (d >> 1), e = 2 * (d & 1);
        f[d] = cvtpk((a[e][reg] + lo(in[i][0][hf][d])) + (lo(in[i][1][hf][d]) + lo(in[i][2][hf][d])), (a[e + 1][reg] + hi(in[i][0][hf][d])) + (hi(in[i][1][hf][d]) + hi(in[i][2][hf][d]))); }
      __builtin_amdgcn_raw_buffer_store_b128(f, rout, ob + p * 128 + lg * 32 + hf * 16, 0, 0);
    }
    u32x2 f2;
#pragma unroll
    for (int d = 0; d < 2; ++d)
      f2[d] = cvtpk((a[4][2 * d] + lo(in2[i][0][d])) + (lo(in2[i][1][d]) + lo(in2[i][2][d])), (a[4][2 * d + 1] + hi(in2[i][0][d])) + (hi(in2[i][1][d]) + hi(in2[i][2][d])));
    __builtin_amdgcn_raw_buffer_store_b64(f2, rout, ob + 512 + p * 32 + lg * 8, 0, 0);
  }
}

template <int NQ>
DI void attn_main(const int w, lptr lds, const unsigned l0, rsrc_t rk, rsrc_t rv, const Ctx& c0, int& nst) {
  const int lane = fresh_lane(), lg = lane >> 4, lr = lane & 15;
  AttnState<NQ> a;
  a.kvo = lg * 256 + ((lr ^ lg ^ (NQ == 2 ? 4 * ((w * NH) & 3) : 0)) << 4);
  a.vvo = lg * 256 + ((lr ^ (lg << 1) ^ (NQ == 2 ? 8 * ((w * NH) & 1) : 0)) << 4);
  {
    const unsigned wb = NQ == 2 ? w * NH * 1024 : 0;
#pragma unroll
    for (int i = 0; i < 10; ++i) tile_issue_at<false>(a, i, rk, wb, l0 + wb);
#pragma unroll
    for (int i = 0; i < 10; ++i) tile_issue_at<false>(a, i, rk, SLOT + wb, l0 + SLOT + wb);
  }
  BAR_L();
  BAR_L();
#pragma unroll
  for (int i = 0; i < 4; ++i) { a.ka[i] = l0 + lr * 256 + (((4 * i + lg) ^ lr) << 4); PIN(a.ka[i]); }
#pragma unroll
  for (int i = 0; i < 8; ++i) { a.va[i] = l0 + VOFF + (lane >> 2) * 256 + ((i ^ ((lane >> 2) & 7)) << 5) + (lane & 3) * 8; PIN(a.va[i]); }
  a.pg = 0; a.pa = l0 + APROG + (lane & 3) * 4; a.oad = 0;
#pragma unroll
  for (int t = 0; t < NQ; ++t) {
    a.l[t] = 0.f; a.inv[t] = 0.f; a.rs[t] = 0.f; a.p[0][t] = a.p[1][t] = u32x4{0u, 0u, 0u, 0u}; a.negm[t] = f32x4{0.f, 0.f, 0.f, 0.f};
#pragma unroll
    for (int i = 0; i < 8; ++i) { a.o[i][t] = f32x4{0.f, 0.f, 0.f, 0.f}; a.sc[1][i & 3][t] = f32x4{0.f, 0.f, 0.f, 0.f}; }
  }
  waitvm<npiece<NQ>()>();
  BAR_L(); ST();
#pragma unroll
  for (int m = 0; m < 5; ++m) frag_read<false>(a, m, 0);
#pragma nounroll
  for (int r = 0; r < 4; ++r) {
    attn_step<1, 0>(a, 8 * r, w, lane, lds, l0, rk, rv); ST();
    attn_step<2, 1>(a, 8 * r + 1, w, lane, lds, l0, rk, rv); ST();
#pragma nounroll
    for (int t = 2; t < 8; t += 2) {
      attn_step<0, 0>(a, 8 * r + t, w, lane, lds, l0, rk, rv); ST();
      attn_step<0, 1>(a, 8 * r + t + 1, w, lane, lds, l0, rk, rv); ST();
    }
  }
  {
    waitvm<npiece<NQ>()>();
    prog_post(lds + APROG + w * 4, 65);
#pragma unroll
    for (int i = 0; i < 8; ++i) sm_pair(a, a.sc[1], a.p[1], 1, i);
    prog_wait<0, 0>(a.pa, 65);
#pragma unroll
    for (int m = 0; m < 8; ++m) frag_read<true>(a, m, 1);
    sm_close<NQ, 1, true, false>(a, a.sc[1], a.sc[1]);
    SB();
#pragma unroll
    for (int i = 0; i < 16; ++i) {
#pragma unroll
      for (int t = 0; t < NQ; ++t) a.o[i & 7][t] = mfma(a.fr[i & 7], a.p[i >> 3][t], a.o[i & 7][t]);
      if (i < 8) frag_read<true>(a, i + 8, 1);
      SB();
    }
#pragma unroll
    for (int t = 0; t < NQ; ++t) a.inv[t] = __builtin_amdgcn_rcpf(xsum(a.l[t]));
  }
  prog_wait<16, 1>(a.pa, P_R1);
  const rsrc_t ro = c0.fresh().ro();
#pragma unroll
  for (int t = 0; t < NQ; ++t)
#pragma unroll
    for (int m = 0; m < 4; ++m) {
      const u32x4 f = o_frag(a, m, t);
      __builtin_amdgcn_raw_buffer_store_b128(f, ro, 3 * 28672 + (2 * w + t) * 4096 + m * 1024 + lane * 16, 0, 16);
      *(LAS u32x4*)(lds + RB + (2 * w + t) * 4096 + m * 1024 + lane * 16) = f;
    }
  prog_post(lds + QPROG + w * 4, 33);
  ST();
}

DI void attn_wave(const int w, const int lane, const int rot, lptr lds, const unsigned l0, rsrc_t rk, rsrc_t rv, rsrc_t rx, rsrc_t rwq, const char* wo, const Ctx& c0, char* out, int& nst) {
  const int lg = lane >> 4, lr = lane & 15, h = lane >> 5;
  ST();
  {
    const unsigned wvo = h * 8192 + (((lane & 31) ^ (h | ((w & 1) << 3))) << 4) + c0.g * 1024;
    const unsigned xvo = (lane >> 3) * 1280 + (((lane & 7) ^ ((4 * (w & 1) + lg) & 7)) << 4);
    auto issue = [&](int c, int buf) {
      const int cr = c + rot >= 10 ? c + rot - 10 : c + rot;
      const unsigned xs = w * 10240 + cr * 128, xd = l0 + XP + buf * XCH + w * 1024, ws_ = (cr * 64 + 2 * w) * 8192, wd = l0 + buf * 2 * SLOT + w * 1024;
#pragma unroll
      for (int i = 0; i < 3; ++i) dma_df(rx, xvo, xs + i * 40960, xd + i * 4096);
      dma_df(rx, xvo, xs + 3 * 40960, w < 2 ? xd + 3 * 4096 : l0 + DUMMY);
#pragma unroll
      for (int i = 0; i < 8; ++i) dma_df(rwq, wvo, ws_ + i * 65536, wd + i * 4096);
    };
    issue(0, 2); issue(1, 0);
    if (w == 0) { *(LAS u32x4*)(lds + CNT + (lane & 7) * 16) = u32x4{0u, 0u, 0u, 0u}; }
    const int b = c0.b, g = c0.g; const rsrc_t rwo = mkrsrc(wo, 4096 * 1280);
    u32x2 rw[8], rq2[8]; unsigned short r5[8]; unsigned long long e1;
    const unsigned rb = 4 * lg * 1280 + (g & 1) * 640, wv0 = rb + w * 128 + lr * 8, wv1 = rb + 512 + w * 32 + lr * 2;
    const unsigned s2 = ((4 * (g ^ (b >> 4)) + ((b >> 2) & 3)) * 128 + (b & 3) * 32) * 1280;
    const unsigned qv = lg * 8 * 8192 + lr * 8 + w * 128 + g * 1024 + 512, sq = b < 20 ? b * 32 * 8192 : OOB;
    constexpr int TN[10] = {0, 0, 0, 9, 8, 8, 4, 0, 5, 0};
#pragma unroll
    for (int c = 0; c < 8; ++c) {
      SB(); asm volatile("s_waitcnt vmcnt(%0) lgkmcnt(0)\n\ts_barrier" :: "i"(12 + TN[c] + (c ? TN[c - 1] : 0)) : "memory"); SB();
      if (c == 5) { _Pragma("unroll") for (int i = 0; i < 8; ++i) PIN(rq2[i]); }
      if (c == 7) { _Pragma("unroll") for (int i = 0; i < 8; ++i) { PIN(rw[i]); PIN(r5[i]); } }
      SB();
      issue(c + 2, (c + 1) % 3);
      SB();
      if (c == 2) {
        e1 = __hip_atomic_load(c0.epoch(), RLX_AGENT) + 1;
#pragma unroll
        for (int jj = 0; jj < 8; ++jj) rq2[jj] = __builtin_amdgcn_raw_buffer_load_b64(rwq, qv, sq + jj * 8192, TAUX);
      }
      if (c == 3) {
#pragma unroll
        for (int jj = 0; jj < 8; ++jj) rw[jj] = __builtin_amdgcn_raw_buffer_load_b64(rwo, wv0 + (jj & 3) * 1280, s2 + (jj >> 2) * 16 * 1280, TAUX);
      }
      if (c == 4) {
#pragma unroll
        for (int jj = 0; jj < 8; ++jj) r5[jj] = __builtin_amdgcn_raw_buffer_load_b16(rwo, wv1 + (jj & 3) * 1280, s2 + (jj >> 2) * 16 * 1280, TAUX);
      }
      if (c == 5) {
#pragma unroll
        for (int e = 0; e < 4; ++e) { u32x4 f;
#pragma unroll
          for (int pp = 0; pp < 4; ++pp) f[pp] = __builtin_amdgcn_perm(rq2[2 * pp + 1][e >> 1], rq2[2 * pp][e >> 1], (e & 1) ? 0x07060302u : 0x05040100u);
          __builtin_amdgcn_raw_buffer_store_b128(f, c0.rqt(), (b * 4 + w) * 4096 + e * 1024 + lane * 16, 0, 16); }
      }
      if (c == 7) {
#pragma unroll
        for (int e = 0; e < 5; ++e) { u32x4 f;
#pragma unroll
          for (int pp = 0; pp < 4; ++pp) f[pp] = e < 4 ? __builtin_amdgcn_perm(rw[2 * pp + 1][(e >> 1) & 1], rw[2 * pp][(e >> 1) & 1], (e & 1) ? 0x07060302u : 0x05040100u)
                                                       : __builtin_amdgcn_perm((unsigned)r5[2 * pp + 1], (unsigned)r5[2 * pp], 0x05040100u);
          __builtin_amdgcn_raw_buffer_store_b128(f, c0.rwt(), (b * 4 + w) * 5120 + e * 1024 + lane * 16, 0, 16); }
      }
    }
    WAITV(17); BAR_L();
    {
      const unsigned long long tag = ((unsigned long long)rfl((unsigned)(e1 >> 32)) << 32) | rfl((unsigned)e1);
      *(LAS unsigned long long*)(lds + TAGW) = tag;
      publish(c0.tflags() + b, lds + CNT + 16, tag, lane);
    }
  }
  if (w < 3) attn_main<2>(w, lds, l0, rk, rv, c0, nst); else attn_main<1>(w, lds, l0, rk, rv, c0, nst);
  const int ln = fresh_lane();
  const Ctx c = c0.fresh();
  const unsigned long long tg = *(const LAS unsigned long long*)(lds + TAGW), tag = ((unsigned long long)rfl((unsigned)(tg >> 32)) << 32) | rfl((unsigned)tg);
  publish(c.flags() + 3 * 16, lds + CNT + 12, tag, ln);
  wait_flag(c.pflags() + 3 * 16, __hip_atomic_load(c.pflags() + 3 * 16, RLX_AGENT), tag);
  {
    u32x4 f[7];
#pragma unroll
    for (int i = 0; i < 7; ++i) f[i] = __builtin_amdgcn_raw_buffer_load_b128(c.rp(), 3 * 28672 + (w * 7 + i) * 1024 + ln * 16, 0, 16);
#pragma unroll
    for (int i = 0; i < 7; ++i) *(LAS u32x4*)(lds + RA + (w * 7 + i) * 1024 + ln * 16) = f[i];
  }
  prog_post(lds + APROG + w * 4, A_END);
  ST();
  final_part(w, c0, lds, l0, tag, out, nst);
}

constexpr int LAG = 3;
DI void parrive(unsigned& pc, const int j, lptr lds) { WAITL(); ++pc; prog_post(lds + PPROG + j * 4, pc); }
DI void oproj_last(const bool KEEP, f32x4 (&acc)[5][7], u32x4 (&af)[4][5], u32x4 (&bf)[4], const int mrot, const unsigned lv, const unsigned l0, rsrc_t rpx, const unsigned po) {
  unsigned ba[4];
#pragma unroll
  for (int k = 0; k < 4; ++k) ba[k] = l0 + RA + ((k + mrot) & 3) * 1024 + lv;
  auto out = [&](int t, int part) {
    if (KEEP) { *(LAS f32x4*)(lp(l0 + OWNP + lv) + (t * 5 + part) * 1024) = acc[part][t]; if (part == 3) *(LAS f32x4*)(lp(l0 + OWNP + lv) + (t * 5 + 4) * 1024) = acc[4][t]; }
    else if (part < 2) {
      const u32x4 f = {cvtpk(acc[0][t][2 * part], acc[1][t][2 * part]), cvtpk(acc[2][t][2 * part], acc[3][t][2 * part]),
                       cvtpk(acc[0][t][2 * part + 1], acc[1][t][2 * part + 1]), cvtpk(acc[2][t][2 * part + 1], acc[3][t][2 * part + 1])};
      __builtin_amdgcn_raw_buffer_store_b128(f, rpx, po + lv + t * 2560 + part * 1024, 0, 16);
    } else if (part == 2) __builtin_amdgcn_raw_buffer_store_b64(u32x2{cvtpk(acc[4][t][0], acc[4][t][1]), cvtpk(acc[4][t][2], acc[4][t][3])}, rpx, po + (lv >> 1) + t * 2560 + 2048, 0, 16);
  };
#pragma unroll
  for (int gi = 0; gi < LAG; ++gi) bf[gi] = lds128(lp(ba[gi & 3]) + (gi >> 2) * 4096);
  SB();
#pragma unroll
  for (int gi = 0; gi < 28; ++gi) {
    const int t = gi >> 2, k = gi & 3;
#pragma unroll
    for (int e = 0; e < 5; ++e) {
      mfma_ip(acc[e][t], af[k][e], bf[gi & 3]);
      if (e == 0 && gi + LAG < 28) bf[(gi + LAG) & 3] = lds128(lp(ba[(gi + LAG) & 3]) + ((gi + LAG) >> 2) * 4096);
      if (e == 4 && k == 0 && t > 0) asm volatile("s_nop 3");
      if (e == 4 && t > 0) out(t - 1, k);
      SB();
    }
  }
  MFMA_DONE(); SB();
#pragma unroll
  for (int k = 0; k < 4; ++k) out(6, k);
}
DI void oproj_wave(const int j, const Ctx& c0, unsigned pc, lptr lds, const unsigned l0, const unsigned long long tag, int& nst) {
  const int lane = fresh_lane();
  const Ctx c = c0.fresh();
  const int mrot = ROT ? c.b : 0;
  const rsrc_t rwt = c.rwt();
  const unsigned pa = l0 + APROG + (lane & 3) * 4, lv = lane * 16;
  f32x4 acc[5][7]; u32x4 af[4][5], bf[4];
#pragma unroll
  for (int k = 0; k < 3; ++k)
#pragma unroll
    for (int e = 0; e < 5; ++e) { af[k][e] = __builtin_amdgcn_raw_buffer_load_b128(rwt, lv + e * 1024, (((k + mrot) & 3) * 4 + j) * 5120, 16); SB(); }
#pragma unroll
  for (int e = 0; e < 5; ++e)
#pragma unroll
    for (int t = 0; t < 7; ++t) acc[e][t] = f32x4{0.f, 0.f, 0.f, 0.f};
  asm volatile("s_nop 1"); SB();
  __builtin_amdgcn_s_setprio(PPRIO);
#pragma nounroll
  for (int hh = 0; hh < 7; ++hh) {
    const int r = hh >> 1, par = hh & 1;
    const unsigned reg = hh == 6 ? RB : qreg(r + 1);
    if (!par) {
      prog_wait<32, 2>(pa, 8 * r + 9);
      if (r < 3) {
        const rsrc_t ro = c0.fresh().ro(); unsigned l2 = lv; PIN(l2);
#pragma unroll
        for (int t = 0; t < 4; ++t) bf[t] = lds128(lp(l0 + reg + j * 7168 + l2) + t * 1024);
#pragma unroll
        for (int t = 0; t < 7; ++t) {
          if (SSL) __builtin_amdgcn_s_sleep(SSL);
          __builtin_amdgcn_raw_buffer_store_b128(bf[t & 3], ro, ((ABL & 1) ? OOB : 0u) + r * 28672 + j * 7168 + l2 + t * 1024, 0, OSA);
          if (t < 3) bf[t] = lds128(lp(l0 + reg + j * 7168 + l2) + (t + 4) * 1024);
          SB();
        }
      }
    } else {
      const Ctx cc = c0.fresh();
      parrive(pc, j, lds);
      if (!(ABL & 16)) wait_flag(cc.pflags() + r * 16, __hip_atomic_load(cc.pflags() + r * 16, RLX_AGENT), tag);
      prog_wait<16, 0>(pa, pc);
      const rsrc_t rp = cc.rp();
#pragma unroll
      for (int t = 0; t < 7; ++t) { if (DSL) __builtin_amdgcn_s_sleep(DSL); dma_sc1(rp, lv, ((ABL & 4) ? OOB : 0u) + r * 28672 + j * 7168 + t * 1024, l0 + reg + j * 7168 + t * 1024); }
      WAITVM(0);
      parrive(pc, j, lds);
      prog_wait<16, 0>(pa, pc);
    }
    PST();
    auto baddr = [&](int gi) { return lp(l0 + reg + ((gi / 7 + mrot) & 3) * 1024 + lv) + (gi % 7) * 4096; };
#pragma unroll
    for (int gi = 0; gi < LAG; ++gi) bf[gi] = lds128(baddr(gi));
    SB();
#pragma unroll
    for (int k = 0; k < 4; ++k) {
      if (k == 2 && !par && r < 3) publish(c0.fresh().flags() + r * 16, lds + CNT + r * 4, tag, lv);
      int h2 = hh; asm volatile("" : "+s"(h2));
      if (OPACE) prog_wait<0, 2>(pa, h2 < 6 ? OP0 + (((4 * h2 + k) * OPACE) >> 2) : 0);
      const int i3 = 4 * h2 + k + 3;
      const unsigned so = (ABL & 2) ? ((i3 & 3) * 4 + j) * 5120 : ((((i3 >> 2) & 1) * 16 + (i3 >> 3) * 4 + ((i3 + mrot) & 3)) * 4 + j) * 5120;
#pragma unroll
      for (int t = 0; t < 7; ++t) {
        const int gi = 7 * k + t;
#pragma unroll
        for (int e = 0; e < 5; ++e) {
          mfma_ip(acc[e][t], af[k][e], bf[gi & 3]);
          if (e == 0 && gi + LAG < 28) bf[(gi + LAG) & 3] = lds128(baddr(gi + LAG));
          if (e == 2 && t >= 1 && t <= 5) PC();
          if (e == 2 && t >= 1 && t <= 5) af[(k + 3) & 3][t - 1] = __builtin_amdgcn_raw_buffer_load_b128(rwt, lv + ((t - 1) & 3) * 1024, so + ((t - 1) >> 2) * 4096, 16);
          SB();
        }
      }
    }
    if (par) parrive(pc, j, lds);
    PST();
  }
#pragma unroll
  for (int e = 0; e < 5; ++e) af[3][e] = __builtin_amdgcn_raw_buffer_load_b128(rwt, lv + (e & 3) * 1024, ((28 + ((3 + mrot) & 3)) * 4 + j) * 5120 + (e >> 2) * 4096, 16);
  prog_wait<0, 1>(pa, A_END);
  PST();
  const Ctx c2 = c0.fresh(); const int p = c2.g >> 1;
  oproj_last(j == p, acc, af, bf, mrot, lv, l0, c2.rpx(), (j * 4 + p) * 17920);
  if (j == p) prog_post(lds + KPROG, 1);
  else {
    asm volatile("s_waitcnt vmcnt(0)" ::: "memory");
    if (lv == 0) __hip_atomic_store(c2.xflag() + j * 16 + p, tag, RLX_AGENT);
  }
  PST();
  __builtin_amdgcn_s_waitcnt(0x0F70);
}

struct QpState { f32x4 acc[4][7]; u32x4 xf[7]; lptr xa0, xa1; };
DI void qp_init(QpState& q, lptr xb, int lane) {
  const int lg = lane >> 4, lr = lane & 15;
  q.xa0 = xb + lr * 128 + ((lg ^ (lr >> 1)) << 4); q.xa1 = xb + lr * 128 + (((4 + lg) ^ (lr >> 1)) << 4);
#pragma unroll
  for (int t = 0; t < 7; ++t) { q.xf[t] = u32x4{0u, 0u, 0u, 0u};
#pragma unroll
    for (int e = 0; e < 4; ++e) q.acc[e][t] = f32x4{0.f, 0.f, 0.f, 0.f}; }
}
template <bool TR> DI void qp_pack(const QpState& q, u32x4 (&qf)[7][2]) {
#pragma unroll
  for (int t = 0; t < 7; ++t)
#pragma unroll
    for (int hf = 0; hf < 2; ++hf) {
#pragma unroll
      for (int d = 0; d < 4; ++d) { const int i0 = 8 * hf + 2 * d, i1 = i0 + 1;
        qf[t][hf][d] = TR ? cvtpk(q.acc[i0 >> 2][t][i0 & 3] * QSCALE, q.acc[i1 >> 2][t][i1 & 3] * QSCALE) : cvtpk(q.acc[i0 & 3][t][i0 >> 2] * QSCALE, q.acc[i1 & 3][t][i1 >> 2] * QSCALE); }
      PIN(qf[t][hf]);
    }
}
template <int PAR>
DI void qp2_chunk(QpState& q, u32x4 (&af)[4][4], const int c, const int j, const int rot, unsigned& pc, const unsigned pa, const unsigned xvo, const unsigned lv, lptr lds, const unsigned l0,
                  rsrc_t rx, rsrc_t rqt) {
  if (QPACE) prog_wait<0, 2>(pa, (c * QPACE) >> 2);
  WAITV(8);
  ++pc; prog_post(lds + PPROG + j * 4, pc);
  const unsigned pg = prog_read<16>(pa);
  const int c1 = c + 1 + rot >= 10 ? c + 1 + rot - 10 : c + 1 + rot, c2 = c + 2 + rot >= 10 ? c + 2 + rot - 10 : c + 2 + rot;
  const unsigned s1 = c < 9 ? ((2 * c1 + 1) * 4 + j) * 4096 : OOB, s2 = c < 8 ? ((2 * c2) * 4 + j) * 4096 : OOB;
  SB();
#pragma unroll
  for (int u = 0; u < 14; ++u) {
    const int v = (u + 14 - LAG) % 14, t = v % 7, set = u < LAG ? 2 * (PAR ^ 1) + 1 : 2 * PAR + v / 7;
#pragma unroll
    for (int e = 0; e < 4; ++e) {
      mfma_ip(q.acc[e][t], af[set][e], q.xf[t]);
      if (e == 0 && u >= 2) q.xf[u % 7] = lds128((u >= 7 ? q.xa1 : q.xa0) + PAR * XCH + (u % 7) * 2048);
      if (e == 2 && ((u >= LAG && u < LAG + 4) || u >= 10)) PC();
      if (e == 2 && u >= LAG && u < LAG + 4) af[2 * (PAR ^ 1) + 1][u - LAG] = __builtin_amdgcn_raw_buffer_load_b128(rqt, lv + (u - LAG) * 1024, s1, 16);
      if (e == 2 && u >= 10) af[2 * PAR][u - 10] = __builtin_amdgcn_raw_buffer_load_b128(rqt, lv + (u - 10) * 1024, s2, 16);
      SB();
    }
    if (u == 0) { prog_wait<16, 0>(pa, pg, pc); q.xf[0] = lds128(q.xa0 + PAR * XCH); q.xf[1] = lds128(q.xa0 + PAR * XCH + 2048); SB(); }
    if (u == 1) {
      const unsigned xs = c < 9 ? j * 10240 + c1 * 128 : OOB, xd = l0 + (c < 9 ? RC + (PAR ^ 1) * XCH + j * 1024 : DUMMY), st = c < 9 ? 4096u : 0u;
#pragma unroll
      for (int i = 0; i < 3; ++i) { PC(); dma_df(rx, xvo, xs + i * 40960, xd + i * st); }
      PC(); dma_df(rx, xvo, xs + 3 * 40960, j < 2 ? xd + 3 * st : l0 + DUMMY);
      SB();
    }
  }
}

DI void proj_wave(const int j, const int lane, const Ctx& c0, const int rot, lptr lds, const unsigned l0, rsrc_t rx, rsrc_t rwq, const char* wo, int& nst) {
  unsigned long long tag, tv;
  if (PPRIO) __builtin_amdgcn_s_setprio(PPRIO);
  const int lg = lane >> 4, lr = lane & 15;
  QpState q;
  u32x4 qf[7][2];
  const unsigned qo = (4 * (j & 1) + lg) * 512 + lr * 16;
  {
    if (PF) {
      const unsigned n = j * 64 + lane, b = c0.b, g = c0.g;
      for (int i = 0; i < PF; ++i) __builtin_amdgcn_s_sleep(1);
      const unsigned iw = (b & 7) * 224 + n;
      touch(rwq, n < 224 ? (192 + (iw >> 2)) * 8192 + g * 1024 + (iw & 3) * 128 : OOB, l0 + DUMMY);
      touch(rx, n < 196 ? (28 * (g & 3) + n / 7) * 1280 + (3 + n % 7) * 128 : OOB, l0 + DUMMY);
      touch(rwq, n < 128 && b < 20 ? (32 * b + (n >> 2)) * 8192 + g * 1024 + 512 + (n & 3) * 128 : OOB, l0 + DUMMY);
      touch(mkrsrc(wo, 4096 * 1280), n < 160 ? ((4 * (g ^ (b >> 4)) + ((b >> 2) & 3)) * 128 + (b & 3) * 32 + n / 5) * 1280 + (g & 1) * 640 + (n % 5) * 128 : OOB, l0 + DUMMY);
    }
    qp_init(q, lds + XP, lane);
    u32x4 af[2][4];
#pragma unroll
    for (int e = 0; e < 4; ++e) af[1][e] = u32x4{0u, 0u, 0u, 0u};
    const int q4 = lr >> 2;
    const unsigned wa = l0 + (8 * lg + q4) * 512 + ((j * 8 + 2 * (lane & 3)) << 4 ^ (((q4 & 1) | ((q4 >> 1) << 3)) << 4));
    int buf = 2;
#pragma nounroll
    for (int c = 0; c < 10; ++c) {
      const unsigned wb0 = wa + buf * 2 * SLOT, wb1 = wb0 ^ 16; const int xo = buf * XCH;
      BAR_L();
#pragma unroll
      for (int u = 0; u < 14; ++u) {
        const int v = (u + 14 - LAG) % 14, t = v % 7, set = u < LAG ? 1 : v / 7;
        auto ard = [&](int ks, int e) { lptr p = lp((e >> 1) ? wb1 : wb0) + ks * 16384 + (e & 1) * 8; const u32x2 lo = ldstr(p), hi = ldstr(p + 2048); af[ks][e] = u32x4{lo[0], lo[1], hi[0], hi[1]}; };
        if (u == 0) { ard(0, 0); q.xf[0] = lds128(q.xa0 + xo); ard(0, 1); SB(); }
#pragma unroll
        for (int e = 0; e < 4; ++e) {
          mfma_ip(q.acc[e][t], af[set][e], q.xf[t]);
          if (e == 0 && u >= 1) q.xf[u % 7] = lds128((u >= 7 ? q.xa1 : q.xa0) + xo + (u % 7) * 2048);
          if (u == 0 && (e & 1)) ard(0, 2 + (e >> 1));
          if (e == 2 && u >= 4 && u < 8) ard(1, u - 4);
          SB();
        }
      }
      buf = buf == 2 ? 0 : buf + 1;
      PST();
    }
#pragma unroll
    for (int t = 7 - LAG; t < 7; ++t)
#pragma unroll
      for (int e = 0; e < 4; ++e) mfma_ip(q.acc[e][t], af[1][e], q.xf[t]);
    MFMA_DONE(); SB();
    BAR_L();
    qp_pack<true>(q, qf);
    lptr qp = lds + ((j >> 1) ? RB : RA) + qo;
#pragma unroll
    for (int t = 0; t < 7; ++t)
#pragma unroll
      for (int hf = 0; hf < 2; ++hf) *(LAS u32x4*)(qp + t * 4096 + hf * 256) = qf[t][hf];
    { const unsigned long long tg = *(const LAS unsigned long long*)(lds + TAGW); tag = ((unsigned long long)rfl((unsigned)(tg >> 32)) << 32) | rfl((unsigned)tg); }
    tv = __hip_atomic_load(c0.fresh().tflags() + (lane & 31), RLX_AGENT);
    BAR_L();
    PST();
  }
  unsigned pc = 0;
  {
    const Ctx c = c0.fresh();
    const rsrc_t rqt = c.rqt();
    const unsigned pa = l0 + APROG + (lane & 3) * 4, lv = lane * 16;
    const unsigned xvo = (lane >> 3) * 1280 + (((lane & 7) ^ ((4 * (j & 1) + lg) & 7)) << 4);
#pragma unroll
    for (int i = 0; i < 3; ++i) dma_df(rx, xvo, j * 10240 + rot * 128 + i * 40960, l0 + RC + j * 1024 + i * 4096);
    dma_df(rx, xvo, j * 10240 + rot * 128 + 3 * 40960, j < 2 ? l0 + RC + j * 1024 + 3 * 4096 : l0 + DUMMY);
    wait_flag(c.tflags() + (lane & 31), tv, tag);
    u32x4 af[4][4];
    const int r1 = rot == 9 ? 0 : rot + 1;
#pragma unroll
    for (int k = 0; k < 3; ++k)
#pragma unroll
      for (int e = 0; e < 4; ++e) { af[k][e] = __builtin_amdgcn_raw_buffer_load_b128(rqt, lv + e * 1024, ((k < 2 ? 2 * rot + k : 2 * r1) * 4 + j) * 4096, 16); SB(); }
#pragma unroll
    for (int e = 0; e < 4; ++e) af[3][e] = u32x4{0u, 0u, 0u, 0u};
    qp_init(q, lds + RC, lane);
    asm volatile("s_nop 1"); SB();
    PST();
#pragma nounroll
    for (int cc = 0; cc < 10; cc += 2) {
      qp2_chunk<0>(q, af, cc, j, rot, pc, pa, xvo, lv, lds, l0, rx, rqt);
      qp2_chunk<1>(q, af, cc + 1, j, rot, pc, pa, xvo, lv, lds, l0, rx, rqt);
      PST();
    }
#pragma unroll
    for (int t = 7 - LAG; t < 7; ++t)
#pragma unroll
      for (int e = 0; e < 4; ++e) mfma_ip(q.acc[e][t], af[3][e], q.xf[t]);
    MFMA_DONE(); SB();
    parrive(pc, j, lds);
    __builtin_amdgcn_s_setprio(XPRIO);
    qp_pack<false>(q, qf);
    prog_wait<16, 0>(pa, pc);
    prog_wait<32, 0>(pa, 1);
#pragma unroll
    for (int t = 0; t < 7; ++t)
#pragma unroll
      for (int hf = 0; hf < 2; ++hf) *(LAS u32x4*)(lds + ((j >> 1) ? RC : RA) + qo + t * 4096 + hf * 256) = qf[t][hf];
    parrive(pc, j, lds);
    PST();
  }
  oproj_wave(j, c0, pc, lds, l0, tag, nst);
}

__global__ __launch_bounds__(512, 2) void attn_fused(const char* __restrict__ x, const char* __restrict__ K, const char* __restrict__ V,
                                                     const char* __restrict__ wq, const char* __restrict__ wo, char* __restrict__ ws, char* __restrict__ out) {
  extern __shared__ __attribute__((aligned(1024))) char smem[];
  const int w = rfl(threadIdx.x >> 6);
  unsigned long long pc0 = 0;
  if (CODEPF) asm volatile("s_getpc_b64 %0" : "=s"(pc0));
  const int bid = blockIdx.x;
  const int b = ((bid >> 1) & 3) * 8 + (bid >> 5), g = (bid & 1) * 4 + ((bid >> 3) & 3);
  const int rot = ROT ? (((b & 7) * 5) / 4 + ((g & 3) * 5) / 2) % 10 : 0;
  lptr lds = (lptr)smem;
  const unsigned l0 = (unsigned)(uintptr_t)lds;
  const Ctx c = {ws, b, g};
  const rsrc_t rx = mkrsrc(x + (size_t)b * 143360, 143360), rwq = mkrsrc(wq, 640 * 8192);
  int nst = 0; (void)nst;
  if (w < 4) {
    const size_t kvb = ((size_t)b * 32 + g * 4) * 131072;
    attn_wave(w, fresh_lane(), rot, lds, l0, mkrsrc(K + kvb, 524288), mkrsrc(V + kvb, 524288), rx, rwq, wo, c, out, nst);
  } else {
    if (CODEPF && w < 6) dma_df(mkrsrc((const void*)(pc0 & ~255ull), CODEPF), fresh_lane() * 16, ((blockIdx.x >> 3) * 2 + (w - 4)) * 1024, l0 + DUMMY + 1024);
    proj_wave(w - 4, fresh_lane(), c, rot, lds, l0, rx, rwq, wo, nst);
  }
}

extern "C" void kernel_launch(void* const* d_in, const int* in_sizes, int n_in, void* d_out, int out_size, void* d_ws, size_t ws_size, hipStream_t stream) {
  (void)hipFuncSetAttribute((const void*)attn_fused, hipFuncAttributeMaxDynamicSharedMemorySize, LDS_BYTES);
  attn_fused<<<256, 512, LDS_BYTES, stream>>>((const char*)d_in[0], (const char*)d_in[1], (const char*)d_in[2], (const char*)d_in[3], (const char*)d_in[4], (char*)d_ws + WSOFF, (char*)d_out);
}
```

```cpp
#include <hip/hip_runtime.h>
#include <stdint.h>
typedef __attribute__((ext_vector_type(8))) __bf16 bf16x8;
typedef __attribute__((ext_vector_type(2))) __bf16 bf16x2;
typedef __attribute__((ext_vector_type(4))) float f32x4;
typedef __attribute__((ext_vector_type(16))) float f32x16;
typedef __attribute__((ext_vector_type(2))) float f32x2;
typedef __attribute__((ext_vector_type(4))) unsigned u32x4;
typedef __attribute__((ext_vector_type(2))) unsigned u32x2;
typedef __attribute__((ext_vector_type(4))) short s16x4;
#define LAS __attribute__((address_space(3)))
#define DI __device__ __forceinline__
typedef LAS char* lptr;
typedef __amdgpu_buffer_rsrc_t rsrc_t;
typedef __attribute__((address_space(1))) unsigned long long gu64;
#define RLX_AGENT __ATOMIC_RELAXED, __HIP_MEMORY_SCOPE_AGENT

constexpr int SLOT = 16384, VOFF = 2 * SLOT, XCH = 14336, RA = 4 * SLOT, RB = RA + 2 * XCH, RC = RB + 2 * XCH, PBUF = SLOT + XCH, DUMMY = VOFF + 4 * PBUF,
              TAGW = DUMMY + 4096, CNT = TAGW + 16, APROG = CNT + 32, PPROG = APROG + 16, QPROG = PPROG + 16, KPROG = QPROG + 16, STMP = CNT + 128, ARR = STMP + 512, LDS_BYTES = ARR + 1024;
constexpr float QSCALE = 0.08838834764831845f * 1.4426950408889634f;
constexpr unsigned OOB = 0x40000000u;
constexpr size_t WS_TF = 8192, WS_OF = 16384, WS_XF = WS_OF + 131072, WS_O = 1u << 20, WS_Q = 40u << 20, WS_WT = 48u << 20, WS_QT = 56u << 20, WS_PX = 64u << 20, WS_ST = 128u << 20;
constexpr int OWN3 = 0, OWNP = 2 * XCH;
constexpr unsigned P_Q1 = 12, P_Q23 = 24;

DI unsigned rfl(unsigned x) { return __builtin_amdgcn_readfirstlane(x); }
DI unsigned cvtpk(float lo, float hi) { f32x2 v = {lo, hi}; bf16x2 b = __builtin_convertvector(v, bf16x2); return __builtin_bit_cast(unsigned, b); }
DI f32x4 mfma(u32x4 a, u32x4 b, f32x4 c) { return __builtin_amdgcn_mfma_f32_16x16x32_bf16(__builtin_bit_cast(bf16x8, a), __builtin_bit_cast(bf16x8, b), c, 0, 0, 0); }
DI void mfma_ip(f32x4& c, const u32x4& a, const u32x4& b) { asm volatile("v_mfma_f32_16x16x32_bf16 %0, %1, %2, %0" : "+v"(c) : "v"(a), "v"(b)); }
#define MFMA_DONE() asm volatile("s_nop 15")
DI f32x16 mfma32(u32x4 a, u32x4 b, f32x16 c) { return __builtin_amdgcn_mfma_f32_32x32x16_bf16(__builtin_bit_cast(bf16x8, a), __builtin_bit_cast(bf16x8, b), c, 0, 0, 0); }
DI u32x4 lds128(lptr p) { return *(const LAS u32x4*)p; }
DI int fresh_lane() { int l; asm volatile("v_mbcnt_lo_u32_b32 %0, -1, 0\n\tv_mbcnt_hi_u32_b32 %0, -1, %0" : "=v"(l)); return l; }
DI lptr lp(unsigned a) { return (lptr)(uintptr_t)a; }
DI u32x2 ldstr(lptr p) { return __builtin_bit_cast(u32x2, __builtin_amdgcn_ds_read_tr16_b64_v4i16((LAS s16x4*)p)); }
DI rsrc_t mkrsrc(const void* p, unsigned bytes) { return __builtin_amdgcn_make_buffer_rsrc((void*)p, 0, bytes, 0x00020000); }
DI float xmax(float m) {
  auto a = __builtin_amdgcn_permlane16_swap(__float_as_uint(m), __float_as_uint(m), false, false); m = fmaxf(__uint_as_float(a[0]), __uint_as_float(a[1]));
  auto b = __builtin_amdgcn_permlane32_swap(__float_as_uint(m), __float_as_uint(m), false, false); return fmaxf(__uint_as_float(b[0]), __uint_as_float(b[1]));
}
DI float xsum(float m) {
  auto a = __builtin_amdgcn_permlane16_swap(__float_as_uint(m), __float_as_uint(m), false, false); m = __uint_as_float(a[0]) + __uint_as_float(a[1]);
  auto b = __builtin_amdgcn_permlane32_swap(__float_as_uint(m), __float_as_uint(m), false, false); return __uint_as_float(b[0]) + __uint_as_float(b[1]);
}
#define DFPOL ""
#define TAUX 0
template <int OFF> DI void dma_nt(rsrc_t r, unsigned voff, unsigned soff, unsigned ldsaddr) {
  asm volatile("s_mov_b32 m0, %3\n\ts_nop 0\n\tbuffer_load_dwordx4 %0, %1, %2 offen offset:%4 nt lds" :: "v"(voff), "s"(r), "s"(soff), "s"(ldsaddr), "i"(OFF) : "memory");
}
DI void dma_df(rsrc_t r, unsigned voff, unsigned soff, unsigned ldsaddr) {
  asm volatile("s_mov_b32 m0, %3\n\ts_nop 0\n\tbuffer_load_dwordx4 %0, %1, %2 offen " DFPOL "lds" :: "v"(voff), "s"(r), "s"(soff), "s"(ldsaddr) : "memory");
}
DI void dma_sc1(rsrc_t r, unsigned voff, unsigned soff, unsigned ldsaddr) {
  asm volatile("s_mov_b32 m0, %3\n\ts_nop 0\n\tbuffer_load_dwordx4 %0, %1, %2 offen sc1 lds" :: "v"(voff), "s"(r), "s"(soff), "s"(ldsaddr) : "memory");
}
DI void touch(rsrc_t r, unsigned voff, unsigned ldsaddr) {
  asm volatile("s_mov_b32 m0, %2\n\ts_nop 0\n\tbuffer_load_dword %0, %1, 0 offen lds" :: "v"(voff), "s"(r), "s"(ldsaddr) : "memory");
}
#define SB() __builtin_amdgcn_sched_barrier(0)
#define WAITV(N) do { SB(); asm volatile("s_waitcnt vmcnt(" #N ") lgkmcnt(0)" ::: "memory"); SB(); } while (0)
#define WAITVM(N) do { SB(); asm volatile("s_waitcnt vmcnt(" #N ")" ::: "memory"); SB(); } while (0)
#define WAITL() do { SB(); asm volatile("s_waitcnt lgkmcnt(0)" ::: "memory"); SB(); } while (0)
#define BAR_L() do { SB(); asm volatile("s_waitcnt lgkmcnt(0)\n\ts_barrier" ::: "memory"); SB(); } while (0)
#define ST() do {} while (0)
#define PST() do {} while (0)
#define PST2() do {} while (0)
#define PCAP 0
#define PC() do { if (PCAP) asm volatile("s_waitcnt vmcnt(%0)" :: "i"(PCAP - 1) : "memory"); } while (0)
#define PIN(x) asm volatile("" : "+v"(x))
#define NODMA 0
#define DPOS 2
#define DSTR 8
#define CAP 16
#define SAUX 16
#define PPRIO 0
#define ABL 0
#define TE0 2
#define XPRIO 0
#define PF 0
#define VP0 2
#define VST 4
#define KP0 18
#define KST 4
#define QPACE 0
#define OPACE 0
#define OP0 26
#define MAP 4
#define OSA 16
#define WSOFF 0
#define CODEPF 0
#define NH 2
#define SSL 0
#define DSL 0
#define LPRIO 3
#define ILV 0
#define TC 1
#define TN0 0, 9, 8, 8, 0, 4, 0, 5, 0, 0
#define LCAP 0
#define ROT 1
#define QLD0 6
#define PFD 16
constexpr float PLIM = 1024.f;

template <int OFF> DI unsigned prog_read(unsigned pa) { return *(volatile LAS unsigned*)(lp(pa) + OFF); }
template <int OFF, int SLEEP> DI void prog_wait(unsigned pa, unsigned v, unsigned t) {
  unsigned n;
  asm volatile("s_mov_b32 %1, 0x1000000\n"
               "1:\n\tv_cmp_gt_u32 vcc, %3, %0\n\ts_cbranch_vccz 2f\n\ts_sleep %5\n\tds_read_b32 %0, %2 offset:%4\n\ts_sub_u32 %1, %1, 1\n\ts_waitcnt lgkmcnt(0)\n\ts_cbranch_scc0 1b\n2:"
               : "+v"(v), "=&s"(n) : "v"(pa), "s"(t), "i"(OFF), "i"(SLEEP) : "memory", "vcc", "scc");
}
template <int OFF, int SLEEP> DI void prog_wait(unsigned pa, unsigned t) { prog_wait<OFF, SLEEP>(pa, prog_read<OFF>(pa), t); }
DI void prog_post(lptr p, unsigned v) { asm volatile("" ::: "memory"); *(volatile LAS unsigned*)p = v; }

DI void publish(gu64* flag, lptr cnt, unsigned long long tag, int lane) {
  asm volatile("s_waitcnt vmcnt(0)" ::: "memory");
  if (lane == 0) {
    const unsigned old = __hip_atomic_fetch_add((LAS unsigned*)cnt, 1u, __ATOMIC_RELAXED, __HIP_MEMORY_SCOPE_WORKGROUP);
    asm volatile("" : "+s"(tag));
    if (old == 3u) __hip_atomic_store(flag, tag, RLX_AGENT);
  }
}
DI void wait_flag(gu64* f, unsigned long long v, unsigned long long tag) {
#pragma nounroll
  for (unsigned spins = 0; spins < (1u << 15); ++spins) {
    if (__all(v == tag)) break;
    __builtin_amdgcn_s_sleep(2);
    v = __hip_atomic_load(f, RLX_AGENT);
  }
  __builtin_amdgcn_s_waitcnt(0x0F70);
  __builtin_amdgcn_fence(__ATOMIC_ACQUIRE, "wavefront");
  asm volatile("" ::: "memory");
}

struct Ctx {
  char* ws; int b, g;
  DI Ctx fresh() const { Ctx c = *this; asm volatile("" : "+s"(c.b), "+s"(c.g)); return c; }
  DI gu64* epoch() const { return (gu64*)ws; }
  DI gu64* tflags() const { return (gu64*)(ws + WS_TF + g * 256); }
  DI gu64* xflag(int dst) const { return (gu64*)(ws + WS_XF + (b * 8 + dst) * 128); }
  DI rsrc_t rpx(int dst) const { return mkrsrc(ws + WS_PX + (size_t)(b * 8 + dst) * 143360, 143360); }
  DI rsrc_t rwt() const { return mkrsrc(ws + WS_WT + (size_t)g * 655360, 655360); }
  DI rsrc_t rqt() const { return mkrsrc(ws + WS_QT + (size_t)g * 491520, 491520); }
};
template <int NQ> struct AttnState {
  u32x4 q[NQ][4], fr[8], p[2][NQ];
  f32x4 o[8][NQ], negm[NQ], sc[2][4][NQ];
  float l[NQ], rs[NQ], inv[NQ];
  unsigned ka[4], va[8], kvo, vvo, pa, pb, pg, oad;
};

DI int qreg(int r) { return r == 3 ? RC : ((r & 1) ? RB : RA); }
template <int NQ> DI void sm_pair(AttnState<NQ>& a, const f32x4 (&x)[4][NQ], u32x4 (&p)[NQ], const int hf, const int n) {
  const int t = n >> 2, d = n & 3;
  if (t >= NQ) return;
  const float e0 = __builtin_amdgcn_exp2f(x[2 * hf + (d >> 1)][t][2 * (d & 1)]), e1 = __builtin_amdgcn_exp2f(x[2 * hf + (d >> 1)][t][2 * (d & 1) + 1]);
  a.rs[t] = d ? (a.rs[t] + e0) + e1 : e0 + e1;
  p[t][d] = cvtpk(e0, e1);
  PIN(p[t][d]); PIN(a.rs[t]);
}
template <int NQ, int HF, bool ADJ>
DI void fixup(AttnState<NQ>& a, f32x4 (&x)[4][NQ], f32x4 (&y)[4][NQ]) {
#pragma unroll
  for (int t = 0; t < NQ; ++t) {
    float mx = x[3][t][3];
#pragma unroll
    for (int i = 8 * HF; i < 15; ++i) mx = fmaxf(mx, x[i >> 2][t][i & 3]);
    const float dl = __builtin_ceilf(fmaxf(xmax(mx), 0.f)), sc = __builtin_amdgcn_exp2f(-dl);
    a.l[t] *= sc; a.negm[t] -= dl;
#pragma unroll
    for (int dt = 0; dt < 8; ++dt) a.o[dt][t] *= sc;
    if (HF) {
#pragma unroll
      for (int d = 0; d < 4; ++d) { const unsigned v = a.p[0][t][d]; a.p[0][t][d] = cvtpk(__uint_as_float(v << 16) * sc, __uint_as_float(v & 0xffff0000u) * sc); }
    }
#pragma unroll
    for (int kt = 2 * HF; kt < 4; ++kt) x[kt][t] -= dl;
    float r = 0.f;
#pragma unroll
    for (int d = 0; d < 4; ++d) { const float e0 = __builtin_amdgcn_exp2f(x[2 * HF + (d >> 1)][t][2 * (d & 1)]), e1 = __builtin_amdgcn_exp2f(x[2 * HF + (d >> 1)][t][2 * (d & 1) + 1]);
      r += e0 + e1; a.p[HF][t][d] = cvtpk(e0, e1); }
    a.rs[t] = r;
    if (ADJ) {
#pragma unroll
      for (int kt = 0; kt < 4; ++kt) y[kt][t] -= dl;
    }
  }
}
template <int NQ, int HF, bool CHECK, bool ADJ>
DI void sm_close(AttnState<NQ>& a, f32x4 (&x)[4][NQ], f32x4 (&y)[4][NQ]) {
  if (CHECK) {
    if (NQ == 1) PIN(a.p[HF][0]);
    float m = NQ == 2 ? a.rs[0] + a.rs[NQ - 1] : a.rs[0]; PIN(m);
    if (__builtin_expect(__any(!(m <= PLIM)), 0)) fixup<NQ, HF, ADJ>(a, x, y);
  }
#pragma unroll
  for (int t = 0; t < NQ; ++t) { PIN(a.rs[t]); a.l[t] += a.rs[t]; PIN(a.l[t]); }
}
template <int NQ> DI u32x4 o_frag(const AttnState<NQ>& a, const int m, const int t) {
  const f32x4 x = a.o[2 * m][t] * a.inv[t], y = a.o[2 * m + 1][t] * a.inv[t];
  return u32x4{cvtpk(x[0], x[1]), cvtpk(x[2], x[3]), cvtpk(y[0], y[1]), cvtpk(y[2], y[3])};
}
template <bool V, int NQ> DI void frag_read(AttnState<NQ>& a, const int m, const int par) {
  if (!V) a.fr[m & 7] = lds128(lp(a.ka[m & 3]) + par * SLOT + (m >> 2) * 4096);
  else { lptr p = lp(a.va[m & 7]) + par * SLOT + (m >> 3) * 8192; const u32x2 v0 = ldstr(p), v1 = ldstr(p + 4096); a.fr[m & 7] = u32x4{v0[0], v0[1], v1[0], v1[1]}; }
}
template <bool V, int NQ> DI void tile_issue(const AttnState<NQ>& a, rsrc_t r, const unsigned gsrc, const unsigned dst, const unsigned st = 4096) {
  if (NODMA) return;
#pragma unroll
  for (int i = 0; i < 4; ++i) {
    if (LCAP) asm volatile("s_waitcnt vmcnt(%0)" :: "i"(LCAP - 4) : "memory");
    dma_nt<0>(r, V ? a.vvo : a.kvo, gsrc + i * 4096, dst + i * st);          dma_nt<1024>(r, V ? a.vvo ^ 128 : a.kvo ^ 64, gsrc + i * 4096, dst + i * st);
    dma_nt<2048>(r, V ? a.vvo : a.kvo ^ 128, gsrc + i * 4096, dst + i * st); dma_nt<3072>(r, V ? a.vvo ^ 128 : a.kvo ^ 192, gsrc + i * 4096, dst + i * st);
  }
}
template <bool CV, int NQ> DI void phase_io(AttnState<NQ>& a, const int i, const int cp, const int np, const unsigned val, const int w, lptr lds) {
  if (i <= 10) frag_read<CV>(a, i + 5, cp);
  if (NQ == 2 && i == 10) {
    SB();
    prog_post(lds + APROG + w * 4, val);
    a.pg = prog_read<0>(a.pb);
  }
  if (NQ == 2 && i == 12) prog_wait<0, 0>(a.pb, a.pg, val);
  if (i == 13 || i == 14) { frag_read<!CV>(a, 2 * (i - 13), np); frag_read<!CV>(a, 2 * (i - 13) + 1, np); }
  if (i == 15) frag_read<!CV>(a, 4, np);
}
template <bool V, int NQ> DI void loader_turn(AttnState<NQ>& a, const unsigned val, lptr lds, rsrc_t r, const unsigned gsrc, const unsigned dst) {
  if (NQ == 1) {
    WAITVM(16);
    prog_post(lds + APROG + 12, val + 1);
    prog_wait<0, 1>(a.pb, val);
    tile_issue<V>(a, r, gsrc, gsrc < 32 * SLOT ? dst : (unsigned)(uintptr_t)lds + DUMMY, gsrc < 32 * SLOT ? 4096u : 0u);
    SB();
  }
}
template <int KIND, int PAR, int NQ>
DI void attn_step(AttnState<NQ>& a, const int s, const int w, const int lane, lptr lds, const unsigned l0, rsrc_t rk, rsrc_t rv) {
  const f32x4 zero = {0.f, 0.f, 0.f, 0.f};
  f32x4 (&c)[4][NQ] = a.sc[PAR]; f32x4 (&d)[4][NQ] = a.sc[PAR ^ 1];
  SB();
  if (KIND == 1) {
    prog_wait<16, 1>(a.pa, s == 0 ? 0u : (s == 8 ? P_Q1 : P_Q23));
    const unsigned qa = l0 + qreg(s >> 3) + 2 * w * 4096 + lane * 16;
#pragma unroll
    for (int i = 0; i < 4; ++i)
#pragma unroll
      for (int t = 0; t < NQ; ++t) a.q[t][i] = lds128(lp(qa) + t * 4096 + i * 1024);
    a.oad = qa;
    SB();
  }
#pragma unroll
  for (int i = 0; i < 16; ++i) {
    constexpr int PA[16] = {-1, 0, 4, -1, 1, 5, -1, 2, 6, -1, -1, 3, 7, -1, -1, -1};
#pragma unroll
    for (int t = 0; t < NQ; ++t) {
      c[i >> 2][t] = mfma(a.fr[i & 7], a.q[t][i & 3], (i & 3) ? c[i >> 2][t] : (KIND == 1 ? zero : a.negm[t]));
      if (t == 0) { if (PA[i] >= 0) sm_pair(a, d, a.p[1], 1, PA[i]); SB(); }
    }
    if (i == 12) PIN(c[3][NQ - 1]);
    phase_io<false>(a, i, PAR, PAR ^ 1, 2 * s + 1, w, lds);
    if (KIND == 2 && i >= 8 && ((i - 8) >> 2) < NQ) *(LAS u32x4*)(lp(a.oad) + ((i - 8) >> 2) * 4096 + (i & 3) * 1024) = o_frag(a, i & 3, ((i - 8) >> 2) % NQ);
    SB();
  }
  if (KIND == 2) prog_post(lds + QPROG + w * 4, s);
  loader_turn<false>(a, 2 * s + 1, lds, rk, (s + 2) * SLOT, l0 + PAR * SLOT);
  sm_close<NQ, 1, KIND != 2, KIND == 0>(a, d, c);
  if (KIND == 1) {
#pragma unroll
    for (int t = 0; t < NQ; ++t) { a.inv[t] = __builtin_amdgcn_rcpf(xsum(a.l[t])); a.l[t] = 0.f; }
  }
  SB();
  u32x4 pn[NQ];
#pragma unroll
  for (int i = 0; i < 16; ++i) {
    constexpr int PB[16] = {-1, -1, -1, 0, -1, 4, -1, 1, -1, 5, -1, 2, 6, -1, 3, 7};
#pragma unroll
    for (int t = 0; t < NQ; ++t) {
      a.o[i & 7][t] = mfma(a.fr[i & 7], a.p[i >> 3][t], (KIND == 2 && i < 8) ? zero : a.o[i & 7][t]);
      if (t == 0) { if (PB[i] >= 0) sm_pair(a, c, pn, 0, PB[i]); SB(); }
    }
    if (i == 12) PIN(a.o[4][NQ - 1]);
    phase_io<true>(a, i, PAR ^ 1, PAR ^ 1, 2 * s + 2, w, lds);
    if (KIND == 1 && (i == 1 || i == 2) && i - 1 < NQ) {
      const int t = (i - 1) % NQ;
      float m0 = c[3][t][3];
#pragma unroll
      for (int e = 0; e < 15; ++e) m0 = fmaxf(m0, c[e >> 2][t][e & 3]);
      m0 = xmax(m0);
#pragma unroll
      for (int kt = 0; kt < 4; ++kt) { c[kt][t] -= m0; PIN(c[kt][t]); }
      a.negm[t] = f32x4{-m0, -m0, -m0, -m0}; PIN(a.negm[t]);
    }
    SB();
  }
#pragma unroll
  for (int t = 0; t < NQ; ++t) a.p[0][t] = pn[t];
  loader_turn<true>(a, 2 * s + 2, lds, rv, (s + 1) * SLOT, l0 + VOFF + (PAR ^ 1) * SLOT);
  sm_close<NQ, 0, KIND != 1, false>(a, c, c);
}

template <int NQ>
DI void attn_main(const int w, lptr lds, const unsigned l0, rsrc_t rk, rsrc_t rv, const Ctx& c0, int& nst) {
  const int lane = fresh_lane(), lg = lane >> 4, lr = lane & 15;
  AttnState<NQ> a;
  a.kvo = lg * 256 + ((lr ^ lg) << 4);
  a.vvo = lg * 256 + ((lr ^ (lg << 1)) << 4);
  if (NQ == 1) { tile_issue<false>(a, rk, 0, l0); }
  BAR_L();
  if (NQ == 1) { tile_issue<false>(a, rk, SLOT, l0 + SLOT); }
  BAR_L();
  if (NQ == 1) { tile_issue<true>(a, rv, 0, l0 + VOFF); prog_post(lds + APROG + 12, 1); __builtin_amdgcn_s_setprio(LPRIO); }
#pragma unroll
  for (int i = 0; i < 4; ++i) { a.ka[i] = l0 + lr * 256 + (((4 * i + lg) ^ lr) << 4); PIN(a.ka[i]); }
#pragma unroll
  for (int i = 0; i < 8; ++i) { a.va[i] = l0 + VOFF + (lane >> 2) * 256 + ((i ^ ((lane >> 2) & 7)) << 5) + (lane & 3) * 8; PIN(a.va[i]); }
  a.pg = 0; a.pa = l0 + APROG + (lane & 3) * 4; a.pb = l0 + APROG + (NQ == 2 ? 12 : ((lane & 3) == 3 ? 0 : (lane & 3) * 4)); a.oad = 0;
#pragma unroll
  for (int t = 0; t < NQ; ++t) {
    a.l[t] = 0.f; a.inv[t] = 0.f; a.rs[t] = 0.f; a.p[0][t] = a.p[1][t] = u32x4{0u, 0u, 0u, 0u}; a.negm[t] = f32x4{0.f, 0.f, 0.f, 0.f};
#pragma unroll
    for (int i = 0; i < 8; ++i) { a.o[i][t] = f32x4{0.f, 0.f, 0.f, 0.f}; a.sc[1][i & 3][t] = f32x4{0.f, 0.f, 0.f, 0.f}; }
  }
  WAITVM(32);
  BAR_L(); ST();
#pragma unroll
  for (int m = 0; m < 5; ++m) frag_read<false>(a, m, 0);
#pragma nounroll
  for (int r = 0; r < 4; ++r) {
    attn_step<1, 0>(a, 8 * r, w, lane, lds, l0, rk, rv); ST();
    attn_step<2, 1>(a, 8 * r + 1, w, lane, lds, l0, rk, rv); ST();
#pragma nounroll
    for (int t = 2; t < 8; t += 2) {
      attn_step<0, 0>(a, 8 * r + t, w, lane, lds, l0, rk, rv); ST();
      attn_step<0, 1>(a, 8 * r + t + 1, w, lane, lds, l0, rk, rv); ST();
    }
  }
  {
#pragma unroll
    for (int i = 0; i < 8; ++i) sm_pair(a, a.sc[1], a.p[1], 1, i);
    if (NQ == 2) prog_wait<0, 0>(a.pb, 65);
#pragma unroll
    for (int m = 0; m < 8; ++m) frag_read<true>(a, m, 1);
    sm_close<NQ, 1, true, false>(a, a.sc[1], a.sc[1]);
    SB();
#pragma unroll
    for (int i = 0; i < 16; ++i) {
#pragma unroll
      for (int t = 0; t < NQ; ++t) a.o[i & 7][t] = mfma(a.fr[i & 7], a.p[i >> 3][t], a.o[i & 7][t]);
      if (i < 8) frag_read<true>(a, i + 8, 1);
      SB();
    }
#pragma unroll
    for (int t = 0; t < NQ; ++t) a.inv[t] = __builtin_amdgcn_rcpf(xsum(a.l[t]));
  }
#pragma unroll
  for (int t = 0; t < NQ; ++t)
#pragma unroll
    for (int m = 0; m < 4; ++m) *(LAS u32x4*)(lds + OWN3 + (2 * w + t) * 4096 + m * 1024 + lane * 16) = o_frag(a, m, t);
  prog_post(lds + QPROG + w * 4, 33);
  __builtin_amdgcn_s_setprio(0);
  ST();
}

DI void attn_wave(const int w, const int lane, const int rot, lptr lds, const unsigned l0, rsrc_t rk, rsrc_t rv, rsrc_t rx, rsrc_t rwq, const char* wo, const Ctx& c0, int& nst) {
  const int lg = lane >> 4, lr = lane & 15;
  ST();
  {
    const unsigned wvo = lg * 8192 + ((lr ^ (lg << 2)) << 4) + c0.g * 1024;
    const unsigned xvo = (lane >> 3) * 1280 + (((lane & 7) ^ ((4 * (w & 1) + lg) & 7)) << 4);
    auto issue = [&](int c) {
      const int cr = c + rot >= 10 ? c + rot - 10 : c + rot;
      const unsigned wd = l0 + VOFF + (c & 3) * PBUF + w * 1024, xd = wd + SLOT, xs = w * 10240 + cr * 128, ws_ = (cr * 64 + 4 * w) * 8192;
#pragma unroll
      for (int i = 0; i < 3; ++i) dma_df(rx, xvo, xs + i * 40960, xd + i * 4096);
      dma_df(rx, xvo, xs + 3 * 40960, w < 2 ? xd + 3 * 4096 : l0 + DUMMY);
#pragma unroll
      for (int i = 0; i < 4; ++i) dma_df(rwq, wvo, ws_ + i * 131072, wd + i * 4096);
    };
    issue(0); issue(1); issue(2);
    if (w == 0) { *(LAS u32x4*)(lds + CNT + (lane & 7) * 16) = u32x4{0u, 0u, 0u, 0u}; }
    const int b = c0.b, g = c0.g; const rsrc_t rwo = mkrsrc(wo, 4096 * 1280);
    u32x2 rw[8], rq2[8]; unsigned short r5[8]; unsigned long long e1;
    const unsigned rb = 4 * lg * 1280 + (b >> 4) * 640, wv0 = rb + w * 128 + lr * 8, wv1 = rb + 512 + w * 32 + lr * 2;
    const unsigned s2 = ((4 * g + ((b >> 2) & 3)) * 128 + (b & 3) * 32) * 1280;
    const int ti = (b - 20) * 4 + w, tk = b < 20 ? b : ti >> 1, tu = b < 20 ? w + 2 : ti & 1;
    const unsigned qv = lg * 8 * 8192 + lr * 8 + g * 1024 + 256, sq = tk * 32 * 8192 + tu * 128;
    constexpr int TN[13] = {0, 0, 0, TN0};
#pragma unroll
    for (int c = 0; c < 9; ++c) {
      SB(); asm volatile("s_waitcnt vmcnt(%0) lgkmcnt(0)\n\ts_barrier" :: "i"((c < 8 ? 16 : 8) + TN[c] + TN[c + 1] + TN[c + 2]) : "memory"); SB();
      if (c == TC + 4) { _Pragma("unroll") for (int i = 0; i < 8; ++i) PIN(rq2[i]); }
      if (c == TC + 6) { _Pragma("unroll") for (int i = 0; i < 8; ++i) { PIN(rw[i]); PIN(r5[i]); } }
      SB();
      if (c < 7) issue(c + 3);
      SB();
      if (c == TC) {
        e1 = __hip_atomic_load(c0.epoch(), RLX_AGENT) + 1;
#pragma unroll
        for (int jj = 0; jj < 8; ++jj) rq2[jj] = __builtin_amdgcn_raw_buffer_load_b64(rwq, qv, sq + jj * 8192, 0);
      }
      if (c == TC + 1) {
#pragma unroll
        for (int jj = 0; jj < 8; ++jj) rw[jj] = __builtin_amdgcn_raw_buffer_load_b64(rwo, wv0 + (jj & 3) * 1280, s2 + (jj >> 2) * 16 * 1280, 0);
      }
      if (c == TC + 2) {
#pragma unroll
        for (int jj = 0; jj < 8; ++jj) r5[jj] = __builtin_amdgcn_raw_buffer_load_b16(rwo, wv1 + (jj & 3) * 1280, s2 + (jj >> 2) * 16 * 1280, 0);
      }
      if (c == TC + 4) {
#pragma unroll
        for (int e = 0; e < 4; ++e) { u32x4 f;
#pragma unroll
          for (int pp = 0; pp < 4; ++pp) f[pp] = __builtin_amdgcn_perm(rq2[2 * pp + 1][e >> 1], rq2[2 * pp][e >> 1], (e & 1) ? 0x07060302u : 0x05040100u);
          __builtin_amdgcn_raw_buffer_store_b128(f, c0.rqt(), (tk * 6 + tu) * 4096 + e * 1024 + lane * 16, 0, 16); }
      }
      if (c == TC + 6) {
#pragma unroll
        for (int e = 0; e < 5; ++e) { u32x4 f;
#pragma unroll
          for (int pp = 0; pp < 4; ++pp) f[pp] = e < 4 ? __builtin_amdgcn_perm(rw[2 * pp + 1][(e >> 1) & 1], rw[2 * pp][(e >> 1) & 1], (e & 1) ? 0x07060302u : 0x05040100u)
                                                       : __builtin_amdgcn_perm((unsigned)r5[2 * pp + 1], (unsigned)r5[2 * pp], 0x05040100u);
          __builtin_amdgcn_raw_buffer_store_b128(f, c0.rwt(), (b * 4 + w) * 5120 + e * 1024 + lane * 16, 0, 16); }
      }
    }
    {
      const unsigned long long tag = ((unsigned long long)rfl((unsigned)(e1 >> 32)) << 32) | rfl((unsigned)e1);
      *(LAS unsigned long long*)(lds + TAGW) = tag;
      publish(c0.tflags() + b, lds + CNT + 16, tag, lane);
    }
  }
  if (w < 3) attn_main<2>(w, lds, l0, rk, rv, c0, nst); else attn_main<1>(w, lds, l0, rk, rv, c0, nst);
}

constexpr int LAG = 3;
DI void parrive(unsigned& pc, const int j, lptr lds) { WAITL(); ++pc; prog_post(lds + PPROG + j * 4, pc); }
DI void oproj_last(const bool KEEP, f32x4 (&acc)[5][7], u32x4 (&af)[4][5], u32x4 (&bf)[4], const int mrot, const unsigned lv, const unsigned l0, rsrc_t rpx, const unsigned po) {
  unsigned ba[4];
#pragma unroll
  for (int k = 0; k < 4; ++k) ba[k] = l0 + OWN3 + ((k + mrot) & 3) * 1024 + lv;
  auto out = [&](int t, int part) {
    if (KEEP) { *(LAS f32x4*)(lp(l0 + OWNP + lv) + (t * 5 + part) * 1024) = acc[part][t]; if (part == 3) *(LAS f32x4*)(lp(l0 + OWNP + lv) + (t * 5 + 4) * 1024) = acc[4][t]; }
    else if (part < 2) {
      const u32x4 f = {cvtpk(acc[0][t][2 * part], acc[1][t][2 * part]), cvtpk(acc[2][t][2 * part], acc[3][t][2 * part]),
                       cvtpk(acc[0][t][2 * part + 1], acc[1][t][2 * part + 1]), cvtpk(acc[2][t][2 * part + 1], acc[3][t][2 * part + 1])};
      __builtin_amdgcn_raw_buffer_store_b128(f, rpx, po + lv + t * 2560 + part * 1024, 0, 16);
    } else if (part == 2) __builtin_amdgcn_raw_buffer_store_b64(u32x2{cvtpk(acc[4][t][0], acc[4][t][1]), cvtpk(acc[4][t][2], acc[4][t][3])}, rpx, po + (lv >> 1) + t * 2560 + 2048, 0, 16);
  };
#pragma unroll
  for (int gi = 0; gi < LAG; ++gi) bf[gi] = lds128(lp(ba[gi & 3]) + (gi >> 2) * 4096);
  SB();
#pragma unroll
  for (int gi = 0; gi < 28; ++gi) {
    const int t = gi >> 2, k = gi & 3;
#pragma unroll
    for (int e = 0; e < 5; ++e) {
      mfma_ip(acc[e][t], af[k][e], bf[gi & 3]);
      if (e == 0 && gi + LAG < 28) bf[(gi + LAG) & 3] = lds128(lp(ba[(gi + LAG) & 3]) + ((gi + LAG) >> 2) * 4096);
      if (e == 4 && k == 0 && t > 0) asm volatile("s_nop 3");
      if (e == 4 && t > 0) out(t - 1, k);
      SB();
    }
  }
  MFMA_DONE(); SB();
#pragma unroll
  for (int k = 0; k < 4; ++k) out(6, k);
}
DI void oproj_wave(const int w, const Ctx& c0, lptr lds, const unsigned l0, int& nst) {
  const int lane = fresh_lane(), j = w & 3;
  const bool pstv = w == 4; (void)pstv;
  const Ctx c = c0.fresh();
  const int mrot = ROT ? c.b : 0, side = (c.g ^ (w < 4)) & 1;
  const rsrc_t rwt = c.rwt();
  const unsigned pa = l0 + APROG + (lane & 3) * 4, lv = lane * 16, tb = (side * 64 + j) * 5120;
  f32x4 acc[5][7]; u32x4 af[4][5], bf[4];
#pragma unroll
  for (int k = 0; k < 3; ++k)
#pragma unroll
    for (int e = 0; e < 5; ++e) { af[k][e] = __builtin_amdgcn_raw_buffer_load_b128(rwt, lv + (e & 3) * 1024, tb + ((k + mrot) & 3) * 20480 + (e >> 2) * 4096, 16); SB(); }
#pragma unroll
  for (int e = 0; e < 5; ++e)
#pragma unroll
    for (int t = 0; t < 7; ++t) acc[e][t] = f32x4{0.f, 0.f, 0.f, 0.f};
  asm volatile("s_nop 1"); SB();
#pragma nounroll
  for (int r = 0; r < 3; ++r) {
    const unsigned reg = qreg(r + 1);
    prog_wait<32, 2>(pa, 8 * r + 9);
    PST();
    auto baddr = [&](int gi) { return lp(l0 + reg + ((gi / 7 + mrot) & 3) * 1024 + lv) + (gi % 7) * 4096; };
#pragma unroll
    for (int gi = 0; gi < LAG; ++gi) bf[gi] = lds128(baddr(gi));
    SB();
#pragma unroll
    for (int k = 0; k < 4; ++k) {
      int r2 = r; asm volatile("" : "+s"(r2));
      const unsigned so = tb + ((k ? r2 + 1 : r2) * 4 + ((k + 3 + mrot) & 3)) * 20480;
#pragma unroll
      for (int t = 0; t < 7; ++t) {
        const int gi = 7 * k + t;
#pragma unroll
        for (int e = 0; e < 5; ++e) {
          mfma_ip(acc[e][t], af[k][e], bf[gi & 3]);
          if (e == 0 && gi + LAG < 28) bf[(gi + LAG) & 3] = lds128(baddr(gi + LAG));
          if (e == 2 && t >= 1 && t <= 5) af[(k + 3) & 3][t - 1] = __builtin_amdgcn_raw_buffer_load_b128(rwt, lv + ((t - 1) & 3) * 1024, so + ((t - 1) >> 2) * 4096, 16);
          SB();
        }
      }
    }
    PST();
  }
#pragma unroll
  for (int e = 0; e < 5; ++e) af[3][e] = __builtin_amdgcn_raw_buffer_load_b128(rwt, lv + (e & 3) * 1024, tb + (12 + ((3 + mrot) & 3)) * 20480 + (e >> 2) * 4096, 16);
  prog_wait<32, 1>(pa, 33);
  if (w >= 4) __builtin_amdgcn_s_setprio(2);
  PST(); ST();
  const Ctx c2 = c0.fresh(); const int dst = 2 * j + side;
  oproj_last(dst == c2.g, acc, af, bf, mrot, lv, l0, c2.rpx(dst), c2.g * 17920);
  if (dst == c2.g) prog_post(lds + KPROG, 1);
  else {
    const unsigned long long tag = *(const LAS unsigned long long*)(lds + TAGW);
    asm volatile("s_waitcnt vmcnt(0)" ::: "memory");
    if (lv == 0) __hip_atomic_store(c2.xflag(dst) + c2.g, tag, RLX_AGENT);
  }
  PST(); ST();
}

DI void final_part(const int j, const Ctx& c0, lptr lds, const unsigned l0, char* out, int& nst) {
  const Ctx c = c0.fresh(); const int b = c.b, g = c.g; const rsrc_t rpx = c.rpx(g);
  const int lane = fresh_lane(), p = g >> 1, lg = lane >> 4, lr = lane & 15;
  const bool pstv = j == 0; (void)pstv;
  const unsigned long long tg = *(const LAS unsigned long long*)(lds + TAGW), tag = ((unsigned long long)rfl((unsigned)(tg >> 32)) << 32) | rfl((unsigned)tg);
  auto lo = [](unsigned v) { return __uint_as_float(v << 16); };
  auto hi = [](unsigned v) { return __uint_as_float(v & 0xffff0000u); };
  f32x4 a[2][5];
#pragma unroll
  for (int late = 0; late < 2; ++late) {
    {
      const int k = lane & 3;
      gu64* f = c.xflag(g) + (late ? 2 * k + 1 - (g & 1) : 2 * (k == p ? (p + 1) & 3 : k) + (g & 1));
      wait_flag(f, __hip_atomic_load(f, RLX_AGENT), tag);
    }
    u32x4 in[2][4][2]; u32x2 in2[2][4];
#pragma unroll
    for (int i = 0; i < 2; ++i)
#pragma unroll
      for (int k = 0; k < 3 + late; ++k) {
        const int src = late ? 2 * k + 1 - (g & 1) : 2 * (k + (k >= p)) + (g & 1);
        const unsigned o = (i == 1 && j == 3) ? OOB : src * 17920 + (j + 4 * i) * 2560;
        in[i][k][0] = __builtin_amdgcn_raw_buffer_load_b128(rpx, lane * 16 + o, 0, 16);
        in[i][k][1] = __builtin_amdgcn_raw_buffer_load_b128(rpx, lane * 16 + o + 1024, 0, 16);
        in2[i][k] = __builtin_amdgcn_raw_buffer_load_b64(rpx, lane * 8 + o + 2048, 0, 16);
      }
    if (!late) {
      prog_wait<0, 1>(l0 + KPROG, 1);
#pragma unroll
      for (int i = 0; i < 2; ++i)
#pragma unroll
        for (int e = 0; e < 5; ++e) a[i][e] = *(LAS f32x4*)(lds + OWNP + (((i == 1 && j == 3) ? 0 : j + 4 * i) * 5 + e) * 1024 + lane * 16);
    }
#pragma unroll
    for (int i = 0; i < 2; ++i)
#pragma unroll
      for (int k = 0; k < 3 + late; ++k) {
#pragma unroll
        for (int d = 0; d < 8; ++d) { const int reg = d >> 1, e = 2 * (d & 1); a[i][e][reg] += lo(in[i][k][d >> 2][d & 3]); a[i][e + 1][reg] += hi(in[i][k][d >> 2][d & 3]); }
#pragma unroll
        for (int d = 0; d < 2; ++d) { a[i][4][2 * d] += lo(in2[i][k][d]); a[i][4][2 * d + 1] += hi(in2[i][k][d]); }
      }
    PST();
  }
  if (j == 0 && g == 0 && b == 0 && lane == 0) __hip_atomic_store(c.epoch(), tag, RLX_AGENT);
  const rsrc_t rout = mkrsrc(out + (size_t)b * 143360, 143360);
#pragma unroll
  for (int i = 0; i < 2; ++i) {
    const unsigned ob = (i == 1 && j == 3) ? OOB : ((j + 4 * i) * 16 + lr) * 1280 + (g & 1) * 640;
#pragma unroll
    for (int hf = 0; hf < 2; ++hf)
      __builtin_amdgcn_raw_buffer_store_b128(u32x4{cvtpk(a[i][0][2 * hf], a[i][1][2 * hf]), cvtpk(a[i][2][2 * hf], a[i][3][2 * hf]), cvtpk(a[i][0][2 * hf + 1], a[i][1][2 * hf + 1]),
                                                   cvtpk(a[i][2][2 * hf + 1], a[i][3][2 * hf + 1])}, rout, ob + p * 128 + lg * 32 + hf * 16, 0, 0);
    __builtin_amdgcn_raw_buffer_store_b64(u32x2{cvtpk(a[i][4][0], a[i][4][1]), cvtpk(a[i][4][2], a[i][4][3])}, rout, ob + 512 + p * 32 + lg * 8, 0, 0);
  }
}

template <int NE> struct QpState { f32x4 acc[NE][7]; u32x4 xf[7]; lptr xa0, xa1; };
template <int NE> DI void qp_init(QpState<NE>& q, lptr xb, int lane) {
  const int lg = lane >> 4, lr = lane & 15;
  q.xa0 = xb + lr * 128 + ((lg ^ (lr >> 1)) << 4); q.xa1 = xb + lr * 128 + (((4 + lg) ^ (lr >> 1)) << 4);
#pragma unroll
  for (int t = 0; t < 7; ++t) { q.xf[t] = u32x4{0u, 0u, 0u, 0u};
#pragma unroll
    for (int e = 0; e < NE; ++e) q.acc[e][t] = f32x4{0.f, 0.f, 0.f, 0.f}; }
  asm volatile("s_nop 1"); SB();
}
template <int PAR, int NE, bool DEEP>
DI void qp2_chunk(QpState<NE>& q, u32x4 (&af)[4][NE], const int c, const int j, const int rot, unsigned& pc, const unsigned pa, const unsigned xvo, const unsigned lv, const unsigned tb,
                  lptr lds, const unsigned l0, rsrc_t rx, rsrc_t rqt) {
  SB(); asm volatile("s_waitcnt vmcnt(%0) lgkmcnt(0)" :: "i"(DEEP ? 6 * NE + 8 : 2 * NE) : "memory"); SB();
  ++pc; prog_post(lds + PPROG + j * 4, pc);
  const unsigned pg = prog_read<16>(pa);
  const int c1 = c + 1 + rot >= 10 ? c + 1 + rot - 10 : c + 1 + rot, c2 = c + 2 + rot >= 10 ? c + 2 + rot - 10 : c + 2 + rot;
  const unsigned s1 = c < 9 ? (2 * c1 + 1) * 24576 + tb : OOB, s2 = c < 8 ? (2 * c2) * 24576 + tb : OOB;
  lptr x0 = q.xa0 + (DEEP ? (c & 2) * XCH : 0), x1 = q.xa1 + (DEEP ? (c & 2) * XCH : 0);
  SB();
#pragma unroll
  for (int u = 0; u < 14; ++u) {
    const int v = (u + 14 - LAG) % 14, t = v % 7, set = u < LAG ? 2 * (PAR ^ 1) + 1 : 2 * PAR + v / 7;
#pragma unroll
    for (int e = 0; e < NE; ++e) {
      mfma_ip(q.acc[e][t], af[set][e], q.xf[t]);
      if (e == 0 && u >= 2) q.xf[u % 7] = lds128((u >= 7 ? x1 : x0) + PAR * XCH + (u % 7) * 2048);
      if (e == NE / 2 && u >= LAG && u < LAG + NE) af[2 * (PAR ^ 1) + 1][u - LAG] = __builtin_amdgcn_raw_buffer_load_b128(rqt, lv + (u - LAG) * 1024, s1, 16);
      if (e == NE / 2 && u >= 10 && u < 10 + NE) af[2 * PAR][u - 10] = __builtin_amdgcn_raw_buffer_load_b128(rqt, lv + (u - 10) * 1024, s2, 16);
      SB();
    }
    if (u == 0) { prog_wait<16, 0>(pa, pg, pc); q.xf[0] = lds128(x0 + PAR * XCH); q.xf[1] = lds128(x0 + PAR * XCH + 2048); SB(); }
    if (u == 1) {
      const bool on = DEEP ? c < 8 : c < 9;
      const int cx = !DEEP ? c1 : (c + 3 + rot >= 10 ? c + 3 + rot - 10 : c + 3 + rot);
      const unsigned xs = on ? j * 10240 + cx * 128 : OOB, xd = l0 + (on ? (DEEP ? RB + ((c + 3) & 3) * XCH : RC + (PAR ^ 1) * XCH) + j * 1024 : DUMMY), st = on ? 4096u : 0u;
#pragma unroll
      for (int i = 0; i < 3; ++i) dma_df(rx, xvo, xs + i * 40960, xd + i * st);
      dma_df(rx, xvo, xs + 3 * 40960, j < 2 ? xd + 3 * st : l0 + DUMMY);
      SB();
    }
  }
}
template <int I>
DI void qp1_chunk(QpState<2>& q, u32x4 (&af)[10][2], const int c, const int j, const int rot, unsigned& pc, const unsigned pa, const unsigned xvo, const unsigned lv,
                  lptr lds, const unsigned l0, rsrc_t rx, rsrc_t rqt) {
  WAITV(20);
  ++pc; prog_post(lds + PPROG + j * 4, pc);
  const unsigned pg = prog_read<16>(pa);
  const int c4 = c + 4 + rot >= 10 ? c + 4 + rot - 10 : c + 4 + rot, c5 = c + 5 + rot >= 10 ? c + 5 + rot - 10 : c + 5 + rot;
  const unsigned s1 = c < 6 ? (2 * c4 + 1) * 24576 + j * 2048 : OOB, s2 = c < 5 ? (2 * c5) * 24576 + j * 2048 : OOB;
  lptr x0 = q.xa0 + (c & 3) * XCH, x1 = q.xa1 + (c & 3) * XCH;
  constexpr int SP = (2 * I + 9) % 10;
  SB();
#pragma unroll
  for (int u = 0; u < 14; ++u) {
    const int v = (u + 14 - LAG) % 14, t = v % 7, set = u < LAG ? SP : 2 * I + v / 7;
#pragma unroll
    for (int e = 0; e < 2; ++e) {
      mfma_ip(q.acc[e][t], af[set][e], q.xf[t]);
      if (e == 0 && u >= 2) q.xf[u % 7] = lds128((u >= 7 ? x1 : x0) + (u % 7) * 2048);
      if (e == 1 && (u == 4 || u == 6)) af[SP][(u - 4) >> 1] = __builtin_amdgcn_raw_buffer_load_b128(rqt, lv + ((u - 4) >> 1) * 1024, s1, 16);
      if (e == 1 && (u == 10 || u == 12)) af[2 * I][(u - 10) >> 1] = __builtin_amdgcn_raw_buffer_load_b128(rqt, lv + ((u - 10) >> 1) * 1024, s2, 16);
      SB();
    }
    if (u == 0) { prog_wait<16, 0>(pa, pg, pc); q.xf[0] = lds128(x0); q.xf[1] = lds128(x0 + 2048); SB(); }
    if (u == 1) {
      const bool on = c < 8;
      const int cx = c + 3 + rot >= 10 ? c + 3 + rot - 10 : c + 3 + rot;
      const unsigned xs = on ? j * 10240 + cx * 128 : OOB, xd = l0 + (on ? RB + ((c + 3) & 3) * XCH + j * 1024 : DUMMY), st = on ? 4096u : 0u;
#pragma unroll
      for (int i = 0; i < 3; ++i) dma_df(rx, xvo, xs + i * 40960, xd + i * st);
      dma_df(rx, xvo, xs + 3 * 40960, j < 2 ? xd + 3 * st : l0 + DUMMY);
      SB();
    }
  }
}
template <int NE> DI void qp2_first(u32x4 (&af)[4][NE], const int rot, const unsigned lv, const unsigned tb, rsrc_t rqt) {
  const int r1 = rot == 9 ? 0 : rot + 1;
#pragma unroll
  for (int k = 0; k < 3; ++k)
#pragma unroll
    for (int e = 0; e < NE; ++e) { af[k][e] = __builtin_amdgcn_raw_buffer_load_b128(rqt, lv + e * 1024, (k < 2 ? 2 * rot + k : 2 * r1) * 24576 + tb, 16); SB(); }
#pragma unroll
  for (int e = 0; e < NE; ++e) af[3][e] = u32x4{0u, 0u, 0u, 0u};
}
template <int NE> DI void qp_tail(QpState<NE>& q, const u32x4 (&a)[NE]) {
#pragma unroll
  for (int t = 7 - LAG; t < 7; ++t)
#pragma unroll
    for (int e = 0; e < NE; ++e) mfma_ip(q.acc[e][t], a[e], q.xf[t]);
  MFMA_DONE(); SB();
}

DI void proj_wave(const int j, const int lane, const Ctx& c0, const int rot, lptr lds, const unsigned l0, rsrc_t rx, int& nst) {
  unsigned long long tag;
  const bool pstv = j == 0; (void)pstv;
  const int lg = lane >> 4, lr = lane & 15;
  {
    QpState<2> q;
    u32x4 af[2][2];
    af[1][0] = af[1][1] = u32x4{0u, 0u, 0u, 0u};
    qp_init(q, lds + VOFF + SLOT, lane);
    const int q4 = lr >> 2;
    const unsigned wa = l0 + VOFF + (8 * lg + q4) * 256 + (((j ^ q4) * 4 + (lane & 3)) << 4);
#pragma nounroll
    for (int c = 0; c < 10; ++c) {
      const int bo = (c & 3) * PBUF;
      BAR_L();
#pragma unroll
      for (int u = 0; u < 14; ++u) {
        const int v = (u + 14 - LAG) % 14, t = v % 7, set = u < LAG ? 1 : v / 7;
        auto ard = [&](int ks, int e) { lptr p = lp(wa + bo) + ks * 8192 + e * 8; const u32x2 lo = ldstr(p), hi = ldstr(p + 1024); af[ks][e] = u32x4{lo[0], lo[1], hi[0], hi[1]}; };
        if (u == 0) { ard(0, 0); q.xf[0] = lds128(q.xa0 + bo); ard(0, 1); SB(); }
#pragma unroll
        for (int e = 0; e < 2; ++e) {
          mfma_ip(q.acc[e][t], af[set][e], q.xf[t]);
          if (e == 0 && u >= 1) q.xf[u % 7] = lds128((u >= 7 ? q.xa1 : q.xa0) + bo + (u % 7) * 2048);
          if (e == 1 && (u == 4 || u == 5)) ard(1, u - 4);
          SB();
        }
      }
      PST();
    }
    qp_tail(q, af[1]);
    BAR_L();
#pragma unroll
    for (int t = 0; t < 7; ++t)
      *(LAS u32x4*)(lds + RA + t * 4096 + j * 1024 + lane * 16) = u32x4{cvtpk(q.acc[0][t][0] * QSCALE, q.acc[0][t][1] * QSCALE), cvtpk(q.acc[0][t][2] * QSCALE, q.acc[0][t][3] * QSCALE),
                                                                        cvtpk(q.acc[1][t][0] * QSCALE, q.acc[1][t][1] * QSCALE), cvtpk(q.acc[1][t][2] * QSCALE, q.acc[1][t][3] * QSCALE)};
    { const unsigned long long tg = *(const LAS unsigned long long*)(lds + TAGW); tag = ((unsigned long long)rfl((unsigned)(tg >> 32)) << 32) | rfl((unsigned)tg); }
    BAR_L();
    PST();
  }
  unsigned pc = 0;
  {
    const Ctx c = c0.fresh();
    const rsrc_t rqt = c.rqt();
    const unsigned pa = l0 + APROG + (lane & 3) * 4, lv = lane * 16;
    const unsigned xvo = (lane >> 3) * 1280 + (((lane & 7) ^ ((4 * (j & 1) + lg) & 7)) << 4);
#pragma unroll
    for (int k = 0; k < 3; ++k) {
      const unsigned xs = j * 10240 + (k + rot >= 10 ? k + rot - 10 : k + rot) * 128, xd = l0 + RB + k * XCH + j * 1024;
#pragma unroll
      for (int i = 0; i < 3; ++i) dma_df(rx, xvo, xs + i * 40960, xd + i * 4096);
      dma_df(rx, xvo, xs + 3 * 40960, j < 2 ? xd + 3 * 4096 : l0 + DUMMY);
    }
    wait_flag(c.tflags() + (lane & 31), __hip_atomic_load(c.tflags() + (lane & 31), RLX_AGENT), tag);
    u32x4 af[4][4];
    {
      QpState<2> q; u32x4 a2[10][2];
#pragma unroll
      for (int k = 0; k < 9; ++k) {
        const int ck = (k >> 1) + rot >= 10 ? (k >> 1) + rot - 10 : (k >> 1) + rot;
#pragma unroll
        for (int e = 0; e < 2; ++e) { a2[k][e] = __builtin_amdgcn_raw_buffer_load_b128(rqt, lv + e * 1024, (2 * ck + (k & 1)) * 24576 + j * 2048, 16); SB(); }
      }
      a2[9][0] = a2[9][1] = u32x4{0u, 0u, 0u, 0u};
      qp_init(q, lds + RB, lane);
      PST();
#pragma nounroll
      for (int cc = 0; cc < 10; cc += 5) {
        qp1_chunk<0>(q, a2, cc, j, rot, pc, pa, xvo, lv, lds, l0, rx, rqt); qp1_chunk<1>(q, a2, cc + 1, j, rot, pc, pa, xvo, lv, lds, l0, rx, rqt);
        qp1_chunk<2>(q, a2, cc + 2, j, rot, pc, pa, xvo, lv, lds, l0, rx, rqt); qp1_chunk<3>(q, a2, cc + 3, j, rot, pc, pa, xvo, lv, lds, l0, rx, rqt);
        qp1_chunk<4>(q, a2, cc + 4, j, rot, pc, pa, xvo, lv, lds, l0, rx, rqt);
      }
      qp_tail(q, a2[9]);
      parrive(pc, j, lds);
      qp2_first(af, rot, lv, (j + 2) * 4096, rqt);
      prog_wait<16, 0>(pa, pc);
      lptr qp = lds + RB + (8 * (j >> 1) + 2 * lg) * 256 + lr * 16 + (j & 1) * 4;
#pragma unroll
      for (int t = 0; t < 7; ++t)
#pragma unroll
        for (int r = 0; r < 4; ++r) *(LAS unsigned*)(qp + t * 4096 + (r >> 1) * 256 + (r & 1) * 8) = cvtpk(q.acc[0][t][r] * QSCALE, q.acc[1][t][r] * QSCALE);
      parrive(pc, j, lds);
      PST();
    }
    QpState<4> q;
    qp_init(q, lds + RC, lane);
#pragma nounroll
    for (int cc = 0; cc < 10; cc += 2) {
      qp2_chunk<0, 4, false>(q, af, cc, j, rot, pc, pa, xvo, lv, (j + 2) * 4096, lds, l0, rx, rqt);
      qp2_chunk<1, 4, false>(q, af, cc + 1, j, rot, pc, pa, xvo, lv, (j + 2) * 4096, lds, l0, rx, rqt);
      PST();
    }
    qp_tail(q, af[3]);
    parrive(pc, j, lds);
    u32x4 qf[7][2];
#pragma unroll
    for (int t = 0; t < 7; ++t)
#pragma unroll
      for (int hf = 0; hf < 2; ++hf) {
        qf[t][hf] = u32x4{cvtpk(q.acc[0][t][2 * hf] * QSCALE, q.acc[1][t][2 * hf] * QSCALE), cvtpk(q.acc[2][t][2 * hf] * QSCALE, q.acc[3][t][2 * hf] * QSCALE),
                          cvtpk(q.acc[0][t][2 * hf + 1] * QSCALE, q.acc[1][t][2 * hf + 1] * QSCALE), cvtpk(q.acc[2][t][2 * hf + 1] * QSCALE, q.acc[3][t][2 * hf + 1] * QSCALE)};
        PIN(qf[t][hf]);
      }
    prog_wait<16, 0>(pa, pc);
    prog_wait<32, 0>(pa, 1);
#pragma unroll
    for (int t = 0; t < 7; ++t)
#pragma unroll
      for (int hf = 0; hf < 2; ++hf) *(LAS u32x4*)(lds + ((j >> 1) ? RC : RA) + (4 * (j & 1) + lg) * 512 + lr * 16 + t * 4096 + hf * 256) = qf[t][hf];
    parrive(pc, j, lds);
    PST();
  }
  __builtin_amdgcn_s_waitcnt(0x0F70);
}

__global__ __launch_bounds__(512, 2) void attn_fused(const char* __restrict__ x, const char* __restrict__ K, const char* __restrict__ V,
                                                     const char* __restrict__ wq, const char* __restrict__ wo, char* __restrict__ ws, char* __restrict__ out) {
  extern __shared__ __attribute__((aligned(1024))) char smem[];
  const int w = rfl(threadIdx.x >> 6);
  unsigned long long pc0 = 0;
  if (CODEPF) asm volatile("s_getpc_b64 %0" : "=s"(pc0));
  const int bid = blockIdx.x;
  const int b = ((bid >> 1) & 3) * 8 + (bid >> 5), g = (bid & 1) * 4 + ((bid >> 3) & 3);
  const int rot = ROT ? (((b & 7) * 5) / 4 + ((g & 3) * 5) / 2) % 10 : 0;
  lptr lds = (lptr)smem;
  const unsigned l0 = (unsigned)(uintptr_t)lds;
  const Ctx c = {ws, b, g};
  const rsrc_t rx = mkrsrc(x + (size_t)b * 143360, 143360), rwq = mkrsrc(wq, 640 * 8192);
  int nst = 0; (void)nst;
  if (w < 4) {
    const size_t kvb = ((size_t)b * 32 + g * 4) * 131072;
    attn_wave(w, fresh_lane(), rot, lds, l0, mkrsrc(K + kvb, 524288), mkrsrc(V + kvb, 524288), rx, rwq, wo, c, nst);
  } else {
    if (CODEPF && w < 6) dma_df(mkrsrc((const void*)(pc0 & ~255ull), CODEPF), fresh_lane() * 16, ((blockIdx.x >> 3) * 2 + (w - 4)) * 1024, l0 + DUMMY + 1024);
    proj_wave(w - 4, fresh_lane(), c, rot, lds, l0, rx, nst);
  }
  oproj_wave(w, c, lds, l0, nst);
  if (w >= 4) final_part(w - 4, c, lds, l0, out, nst);
}

extern "C" void kernel_launch(void* const* d_in, const int* in_sizes, int n_in, void* d_out, int out_size, void* d_ws, size_t ws_size, hipStream_t stream) {
  (void)hipFuncSetAttribute((const void*)attn_fused, hipFuncAttributeMaxDynamicSharedMemorySize, LDS_BYTES);
  attn_fused<<<256, 512, LDS_BYTES, stream>>>((const char*)d_in[0], (const char*)d_in[1], (const char*)d_in[2], (const char*)d_in[3], (const char*)d_in[4], (char*)d_ws + WSOFF, (char*)d_out);
}
```

```cpp
#include <hip/hip_runtime.h>
#include <stdint.h>
typedef __attribute__((ext_vector_type(8))) __bf16 bf16x8;
typedef __attribute__((ext_vector_type(2))) __bf16 bf16x2;
typedef __attribute__((ext_vector_type(4))) float f32x4;
typedef __attribute__((ext_vector_type(16))) float f32x16;
typedef __attribute__((ext_vector_type(2))) float f32x2;
typedef __attribute__((ext_vector_type(4))) unsigned u32x4;
typedef __attribute__((ext_vector_type(2))) unsigned u32x2;
typedef __attribute__((ext_vector_type(4))) short s16x4;
#define LAS __attribute__((address_space(3)))
#define DI __device__ __forceinline__
typedef LAS char* lptr;
typedef __amdgpu_buffer_rsrc_t rsrc_t;
typedef __attribute__((address_space(1))) unsigned long long gu64;
#define RLX_AGENT __ATOMIC_RELAXED, __HIP_MEMORY_SCOPE_AGENT

constexpr int SLOT = 16384, VOFF = 2 * SLOT, XCH = 14336, RA = 4 * SLOT, RB = RA + 2 * XCH, RC = RB + 2 * XCH, DUMMY = RC + 2 * XCH, XP = 6 * SLOT,
              TAGW = DUMMY + 4096, CNT = TAGW + 16, APROG = CNT + 32, PPROG = APROG + 16, QPROG = PPROG + 16, KPROG = QPROG + 16, STMP = CNT + 128, ARR = STMP + 512, LDS_BYTES = ARR + 1024;
constexpr float QSCALE = 0.08838834764831845f * 1.4426950408889634f;
constexpr unsigned OOB = 0x40000000u;
constexpr size_t WS_TF = 8192, WS_OF = 16384, WS_XF = WS_OF + 131072, WS_O = 1u << 20, WS_Q = 40u << 20, WS_WT = 48u << 20, WS_QT = 56u << 20, WS_PX = 64u << 20, WS_ST = 128u << 20;
constexpr int OWN3 = 0, OWNP = 2 * XCH;
constexpr unsigned P_Q23 = 12;

DI unsigned rfl(unsigned x) { return __builtin_amdgcn_readfirstlane(x); }
DI unsigned cvtpk(float lo, float hi) { f32x2 v = {lo, hi}; bf16x2 b = __builtin_convertvector(v, bf16x2); return __builtin_bit_cast(unsigned, b); }
DI f32x4 mfma(u32x4 a, u32x4 b, f32x4 c) { return __builtin_amdgcn_mfma_f32_16x16x32_bf16(__builtin_bit_cast(bf16x8, a), __builtin_bit_cast(bf16x8, b), c, 0, 0, 0); }
DI void mfma_ip(f32x4& c, const u32x4& a, const u32x4& b) { asm volatile("v_mfma_f32_16x16x32_bf16 %0, %1, %2, %0" : "+v"(c) : "v"(a), "v"(b)); }
#define MFMA_DONE() asm volatile("s_nop 15")
DI f32x16 mfma32(u32x4 a, u32x4 b, f32x16 c) { return __builtin_amdgcn_mfma_f32_32x32x16_bf16(__builtin_bit_cast(bf16x8, a), __builtin_bit_cast(bf16x8, b), c, 0, 0, 0); }
DI u32x4 lds128(lptr p) { return *(const LAS u32x4*)p; }
DI int fresh_lane() { int l; asm volatile("v_mbcnt_lo_u32_b32 %0, -1, 0\n\tv_mbcnt_hi_u32_b32 %0, -1, %0" : "=v"(l)); return l; }
DI lptr lp(unsigned a) { return (lptr)(uintptr_t)a; }
DI u32x2 ldstr(lptr p) { return __builtin_bit_cast(u32x2, __builtin_amdgcn_ds_read_tr16_b64_v4i16((LAS s16x4*)p)); }
DI rsrc_t mkrsrc(const void* p, unsigned bytes) { return __builtin_amdgcn_make_buffer_rsrc((void*)p, 0, bytes, 0x00020000); }
DI float xmax(float m) {
  auto a = __builtin_amdgcn_permlane16_swap(__float_as_uint(m), __float_as_uint(m), false, false); m = fmaxf(__uint_as_float(a[0]), __uint_as_float(a[1]));
  auto b = __builtin_amdgcn_permlane32_swap(__float_as_uint(m), __float_as_uint(m), false, false); return fmaxf(__uint_as_float(b[0]), __uint_as_float(b[1]));
}
DI float xsum(float m) {
  auto a = __builtin_amdgcn_permlane16_swap(__float_as_uint(m), __float_as_uint(m), false, false); m = __uint_as_float(a[0]) + __uint_as_float(a[1]);
  auto b = __builtin_amdgcn_permlane32_swap(__float_as_uint(m), __float_as_uint(m), false, false); return __uint_as_float(b[0]) + __uint_as_float(b[1]);
}
#define DFPOL ""
#define TAUX 0
#define KVPOL "nt"
template <int OFF> DI void dma_nt(rsrc_t r, unsigned voff, unsigned soff, unsigned ldsaddr) {
  asm volatile("s_mov_b32 m0, %3\n\ts_nop 0\n\tbuffer_load_dwordx4 %0, %1, %2 offen offset:%4 " KVPOL " lds" :: "v"(voff), "s"(r), "s"(soff), "s"(ldsaddr), "i"(OFF) : "memory");
}
DI void dma_df(rsrc_t r, unsigned voff, unsigned soff, unsigned ldsaddr) {
  asm volatile("s_mov_b32 m0, %3\n\ts_nop 0\n\tbuffer_load_dwordx4 %0, %1, %2 offen " DFPOL "lds" :: "v"(voff), "s"(r), "s"(soff), "s"(ldsaddr) : "memory");
}
DI void dma_sc1(rsrc_t r, unsigned voff, unsigned soff, unsigned ldsaddr) {
  asm volatile("s_mov_b32 m0, %3\n\ts_nop 0\n\tbuffer_load_dwordx4 %0, %1, %2 offen sc1 lds" :: "v"(voff), "s"(r), "s"(soff), "s"(ldsaddr) : "memory");
}
DI void touch(rsrc_t r, unsigned voff, unsigned ldsaddr) {
  asm volatile("s_mov_b32 m0, %2\n\ts_nop 0\n\tbuffer_load_dword %0, %1, 0 offen lds" :: "v"(voff), "s"(r), "s"(ldsaddr) : "memory");
}
#define SB() __builtin_amdgcn_sched_barrier(0)
#define WAITV(N) do { SB(); asm volatile("s_waitcnt vmcnt(" #N ") lgkmcnt(0)" ::: "memory"); SB(); } while (0)
#define WAITVM(N) do { SB(); asm volatile("s_waitcnt vmcnt(" #N ")" ::: "memory"); SB(); } while (0)
#define WAITL() do { SB(); asm volatile("s_waitcnt lgkmcnt(0)" ::: "memory"); SB(); } while (0)
#define BAR_L() do { SB(); asm volatile("s_waitcnt lgkmcnt(0)\n\ts_barrier" ::: "memory"); SB(); } while (0)
#define ST() do {} while (0)
#define PST() do {} while (0)
#define PST2() do {} while (0)
#define PCAP 0
#define PC() do { if (PCAP) asm volatile("s_waitcnt vmcnt(%0)" :: "i"(PCAP - 1) : "memory"); } while (0)
#define PIN(x) asm volatile("" : "+v"(x))
#define NODMA 0
#define DPOS 2
#define DSTR 8
#define CAP 16
#define SAUX 16
#define PPRIO 0
#define ABL 0
#define TE0 2
#define XPRIO 0
#define PF 0
#define VP0 2
#define VST 4
#define KP0 18
#define KST 4
#define QPACE 10
#define OPACE 0
#define OP0 26
#define MAP 4
#define OSA 16
#define WSOFF 0
#define CODEPF 0
#define NH 2
#define SSL 0
#define DSL 0
#define LPRIO 3
#define ILV 0
#define TC 1
#define TN0 0, 9, 8, 8, 0, 4, 0, 5, 0, 0
#define LCAP 0
#define RPACE 0
#define ROT 1
#define QLD0 6
#define PFD 16
constexpr float PLIM = 1024.f;

template <int OFF> DI unsigned prog_read(unsigned pa) { return *(volatile LAS unsigned*)(lp(pa) + OFF); }
template <int OFF, int SLEEP> DI void prog_wait(unsigned pa, unsigned v, unsigned t) {
  unsigned n;
  asm volatile("s_mov_b32 %1, 0x1000000\n"
               "1:\n\tv_cmp_gt_u32 vcc, %3, %0\n\ts_cbranch_vccz 2f\n\ts_sleep %5\n\tds_read_b32 %0, %2 offset:%4\n\ts_sub_u32 %1, %1, 1\n\ts_waitcnt lgkmcnt(0)\n\ts_cbranch_scc0 1b\n2:"
               : "+v"(v), "=&s"(n) : "v"(pa), "s"(t), "i"(OFF), "i"(SLEEP) : "memory", "vcc", "scc");
}
template <int OFF, int SLEEP> DI void prog_wait(unsigned pa, unsigned t) { prog_wait<OFF, SLEEP>(pa, prog_read<OFF>(pa), t); }
DI void prog_post(lptr p, unsigned v) { asm volatile("" ::: "memory"); *(volatile LAS unsigned*)p = v; }

DI void publish(gu64* flag, lptr cnt, unsigned long long tag, int lane) {
  asm volatile("s_waitcnt vmcnt(0)" ::: "memory");
  if (lane == 0) {
    const unsigned old = __hip_atomic_fetch_add((LAS unsigned*)cnt, 1u, __ATOMIC_RELAXED, __HIP_MEMORY_SCOPE_WORKGROUP);
    asm volatile("" : "+s"(tag));
    if (old == 3u) __hip_atomic_store(flag, tag, RLX_AGENT);
  }
}
DI void wait_flag(gu64* f, unsigned long long v, unsigned long long tag) {
#pragma nounroll
  for (unsigned spins = 0; spins < (1u << 15); ++spins) {
    if (__all(v == tag)) break;
    __builtin_amdgcn_s_sleep(2);
    v = __hip_atomic_load(f, RLX_AGENT);
  }
  __builtin_amdgcn_s_waitcnt(0x0F70);
  __builtin_amdgcn_fence(__ATOMIC_ACQUIRE, "wavefront");
  asm volatile("" ::: "memory");
}

struct Ctx {
  char* ws; int b, g;
  DI Ctx fresh() const { Ctx c = *this; asm volatile("" : "+s"(c.b), "+s"(c.g)); return c; }
  DI gu64* epoch() const { return (gu64*)ws; }
  DI gu64* tflags() const { return (gu64*)(ws + WS_TF + g * 256); }
  DI gu64* xflag(int dst) const { return (gu64*)(ws + WS_XF + (b * 8 + dst) * 128); }
  DI rsrc_t rpx(int dst) const { return mkrsrc(ws + WS_PX + (size_t)(b * 8 + dst) * 143360, 143360); }
  DI rsrc_t rwt() const { return mkrsrc(ws + WS_WT + (size_t)g * 655360, 655360); }
  DI rsrc_t rqt() const { return mkrsrc(ws + WS_QT + (size_t)g * 327680, 327680); }
};
template <int NQ> struct AttnState {
  u32x4 q[NQ][4], fr[8], p[2][NQ];
  f32x4 o[8][NQ], negm[NQ], sc[2][4][NQ];
  float l[NQ], rs[NQ], inv[NQ];
  unsigned ka[4], va[8], kvo, vvo, pa, pb, pg, oad, last;
};

DI int qreg(int r) { return r == 3 ? RC : ((r & 1) ? RB : RA); }
template <int NQ> DI void sm_pair(AttnState<NQ>& a, const f32x4 (&x)[4][NQ], u32x4 (&p)[NQ], const int hf, const int n) {
  const int t = n >> 2, d = n & 3;
  if (t >= NQ) return;
  const float e0 = __builtin_amdgcn_exp2f(x[2 * hf + (d >> 1)][t][2 * (d & 1)]), e1 = __builtin_amdgcn_exp2f(x[2 * hf + (d >> 1)][t][2 * (d & 1) + 1]);
  a.rs[t] = d ? (a.rs[t] + e0) + e1 : e0 + e1;
  p[t][d] = cvtpk(e0, e1);
  PIN(p[t][d]); PIN(a.rs[t]);
}
template <int NQ, int HF, bool ADJ>
DI void fixup(AttnState<NQ>& a, f32x4 (&x)[4][NQ], f32x4 (&y)[4][NQ]) {
#pragma unroll
  for (int t = 0; t < NQ; ++t) {
    float mx = x[3][t][3];
#pragma unroll
    for (int i = 8 * HF; i < 15; ++i) mx = fmaxf(mx, x[i >> 2][t][i & 3]);
    const float dl = __builtin_ceilf(fmaxf(xmax(mx), 0.f)), sc = __builtin_amdgcn_exp2f(-dl);
    a.l[t] *= sc; a.negm[t] -= dl;
#pragma unroll
    for (int dt = 0; dt < 8; ++dt) a.o[dt][t] *= sc;
    if (HF) {
#pragma unroll
      for (int d = 0; d < 4; ++d) { const unsigned v = a.p[0][t][d]; a.p[0][t][d] = cvtpk(__uint_as_float(v << 16) * sc, __uint_as_float(v & 0xffff0000u) * sc); }
    }
#pragma unroll
    for (int kt = 2 * HF; kt < 4; ++kt) x[kt][t] -= dl;
    float r = 0.f;
#pragma unroll
    for (int d = 0; d < 4; ++d) { const float e0 = __builtin_amdgcn_exp2f(x[2 * HF + (d >> 1)][t][2 * (d & 1)]), e1 = __builtin_amdgcn_exp2f(x[2 * HF + (d >> 1)][t][2 * (d & 1) + 1]);
      r += e0 + e1; a.p[HF][t][d] = cvtpk(e0, e1); }
    a.rs[t] = r;
    if (ADJ) {
#pragma unroll
      for (int kt = 0; kt < 4; ++kt) y[kt][t] -= dl;
    }
  }
}
template <int NQ, int HF, bool CHECK, bool ADJ>
DI void sm_close(AttnState<NQ>& a, f32x4 (&x)[4][NQ], f32x4 (&y)[4][NQ]) {
  if (CHECK) {
    if (NQ == 1) PIN(a.p[HF][0]);
    float m = NQ == 2 ? a.rs[0] + a.rs[NQ - 1] : a.rs[0]; PIN(m);
    if (__builtin_expect(__any(!(m <= PLIM)), 0)) fixup<NQ, HF, ADJ>(a, x, y);
  }
#pragma unroll
  for (int t = 0; t < NQ; ++t) { PIN(a.rs[t]); a.l[t] += a.rs[t]; PIN(a.l[t]); }
}
template <int NQ> DI u32x4 o_frag(const AttnState<NQ>& a, const int m, const int t) {
  const f32x4 x = a.o[2 * m][t] * a.inv[t], y = a.o[2 * m + 1][t] * a.inv[t];
  return u32x4{cvtpk(x[0], x[1]), cvtpk(x[2], x[3]), cvtpk(y[0], y[1]), cvtpk(y[2], y[3])};
}
template <bool V, int NQ> DI void frag_read(AttnState<NQ>& a, const int m, const int par) {
  if (!V) a.fr[m & 7] = lds128(lp(a.ka[m & 3]) + par * SLOT + (m >> 2) * 4096);
  else { lptr p = lp(a.va[m & 7]) + par * SLOT + (m >> 3) * 8192; const u32x2 v0 = ldstr(p), v1 = ldstr(p + 4096); a.fr[m & 7] = u32x4{v0[0], v0[1], v1[0], v1[1]}; }
}
template <bool V, int NQ> DI void tile_issue(const AttnState<NQ>& a, rsrc_t r, const unsigned gsrc, const unsigned dst, const unsigned st = 4096) {
  if (NODMA) return;
#pragma unroll
  for (int i = 0; i < 4; ++i) {
    if (LCAP) asm volatile("s_waitcnt vmcnt(%0)" :: "i"(LCAP - 4) : "memory");
    dma_nt<0>(r, V ? a.vvo : a.kvo, gsrc + i * 4096, dst + i * st);          dma_nt<1024>(r, V ? a.vvo ^ 128 : a.kvo ^ 64, gsrc + i * 4096, dst + i * st);
    dma_nt<2048>(r, V ? a.vvo : a.kvo ^ 128, gsrc + i * 4096, dst + i * st); dma_nt<3072>(r, V ? a.vvo ^ 128 : a.kvo ^ 192, gsrc + i * 4096, dst + i * st);
  }
}
template <bool CV, int NQ> DI void phase_io(AttnState<NQ>& a, const int i, const int cp, const int np, const unsigned val, const int w, lptr lds) {
  if (i <= 10) frag_read<CV>(a, i + 5, cp);
  if (NQ == 2 && i == 10) {
    SB();
    prog_post(lds + APROG + w * 4, val);
    a.pg = prog_read<0>(a.pb);
  }
  if (NQ == 2 && i == 12) prog_wait<0, 0>(a.pb, a.pg, val);
  if (i == 13 || i == 14) { frag_read<!CV>(a, 2 * (i - 13), np); frag_read<!CV>(a, 2 * (i - 13) + 1, np); }
  if (i == 15) frag_read<!CV>(a, 4, np);
}
template <bool V, int NQ> DI void loader_turn(AttnState<NQ>& a, const unsigned val, lptr lds, rsrc_t r, const unsigned gsrc, const unsigned dst) {
  if (NQ == 1) {
    WAITVM(16);
    prog_post(lds + APROG + 12, val + 1);
    prog_wait<0, 1>(a.pb, val);
    if (RPACE) {
      asm volatile("1:\n\ts_memrealtime vcc\n\ts_waitcnt lgkmcnt(0)\n\ts_sub_u32 vcc_hi, vcc_lo, %0\n\ts_cmp_lt_u32 vcc_hi, %1\n\ts_cbranch_scc1 1b\n\ts_mov_b32 %0, vcc_lo"
                   : "+s"(a.last) : "i"(RPACE) : "memory", "scc", "vcc");
    }
    tile_issue<V>(a, r, gsrc, gsrc < 32 * SLOT ? dst : (unsigned)(uintptr_t)lds + DUMMY, gsrc < 32 * SLOT ? 4096u : 0u);
    SB();
  }
}
template <int KIND, int PAR, int NQ>
DI void attn_step(AttnState<NQ>& a, const int s, const int w, const int lane, lptr lds, const unsigned l0, rsrc_t rk, rsrc_t rv) {
  const f32x4 zero = {0.f, 0.f, 0.f, 0.f};
  f32x4 (&c)[4][NQ] = a.sc[PAR]; f32x4 (&d)[4][NQ] = a.sc[PAR ^ 1];
  SB();
  if (KIND == 1) {
    prog_wait<16, 1>(a.pa, s < 16 ? 0u : P_Q23);
    const unsigned qa = l0 + qreg(s >> 3) + 2 * w * 4096 + lane * 16;
#pragma unroll
    for (int i = 0; i < 4; ++i)
#pragma unroll
      for (int t = 0; t < NQ; ++t) a.q[t][i] = lds128(lp(qa) + t * 4096 + i * 1024);
    a.oad = qa;
    SB();
  }
#pragma unroll
  for (int i = 0; i < 16; ++i) {
    constexpr int PA[16] = {-1, 0, 4, -1, 1, 5, -1, 2, 6, -1, -1, 3, 7, -1, -1, -1};
#pragma unroll
    for (int t = 0; t < NQ; ++t) {
      c[i >> 2][t] = mfma(a.fr[i & 7], a.q[t][i & 3], (i & 3) ? c[i >> 2][t] : (KIND == 1 ? zero : a.negm[t]));
      if (t == 0) { if (PA[i] >= 0) sm_pair(a, d, a.p[1], 1, PA[i]); SB(); }
    }
    if (i == 12) PIN(c[3][NQ - 1]);
    phase_io<false>(a, i, PAR, PAR ^ 1, 2 * s + 1, w, lds);
    if (KIND == 2 && i >= 8 && ((i - 8) >> 2) < NQ) *(LAS u32x4*)(lp(a.oad) + ((i - 8) >> 2) * 4096 + (i & 3) * 1024) = o_frag(a, i & 3, ((i - 8) >> 2) % NQ);
    SB();
  }
  if (KIND == 2) prog_post(lds + QPROG + w * 4, s);
  loader_turn<false>(a, 2 * s + 1, lds, rk, (s + 2) * SLOT, l0 + PAR * SLOT);
  sm_close<NQ, 1, KIND != 2, KIND == 0>(a, d, c);
  if (KIND == 1) {
#pragma unroll
    for (int t = 0; t < NQ; ++t) { a.inv[t] = __builtin_amdgcn_rcpf(xsum(a.l[t])); a.l[t] = 0.f; }
  }
  SB();
  u32x4 pn[NQ];
#pragma unroll
  for (int i = 0; i < 16; ++i) {
    constexpr int PB[16] = {-1, -1, -1, 0, -1, 4, -1, 1, -1, 5, -1, 2, 6, -1, 3, 7};
#pragma unroll
    for (int t = 0; t < NQ; ++t) {
      a.o[i & 7][t] = mfma(a.fr[i & 7], a.p[i >> 3][t], (KIND == 2 && i < 8) ? zero : a.o[i & 7][t]);
      if (t == 0) { if (PB[i] >= 0) sm_pair(a, c, pn, 0, PB[i]); SB(); }
    }
    if (i == 12) PIN(a.o[4][NQ - 1]);
    phase_io<true>(a, i, PAR ^ 1, PAR ^ 1, 2 * s + 2, w, lds);
    if (KIND == 1 && (i == 1 || i == 2) && i - 1 < NQ) {
      const int t = (i - 1) % NQ;
      float m0 = c[3][t][3];
#pragma unroll
      for (int e = 0; e < 15; ++e) m0 = fmaxf(m0, c[e >> 2][t][e & 3]);
      m0 = xmax(m0);
#pragma unroll
      for (int kt = 0; kt < 4; ++kt) { c[kt][t] -= m0; PIN(c[kt][t]); }
      a.negm[t] = f32x4{-m0, -m0, -m0, -m0}; PIN(a.negm[t]);
    }
    SB();
  }
#pragma unroll
  for (int t = 0; t < NQ; ++t) a.p[0][t] = pn[t];
  loader_turn<true>(a, 2 * s + 2, lds, rv, (s + 1) * SLOT, l0 + VOFF + (PAR ^ 1) * SLOT);
  sm_close<NQ, 0, KIND != 1, false>(a, c, c);
}

template <int NQ>
DI void attn_main(const int w, lptr lds, const unsigned l0, rsrc_t rk, rsrc_t rv, const Ctx& c0, int& nst) {
  const int lane = fresh_lane(), lg = lane >> 4, lr = lane & 15;
  AttnState<NQ> a;
  a.kvo = lg * 256 + ((lr ^ lg) << 4);
  a.vvo = lg * 256 + ((lr ^ (lg << 1)) << 4);
  if (NQ == 1) { tile_issue<false>(a, rk, 0, l0); }
  BAR_L();
  if (NQ == 1) { tile_issue<false>(a, rk, SLOT, l0 + SLOT); }
  BAR_L();
  if (NQ == 1) { tile_issue<true>(a, rv, 0, l0 + VOFF); prog_post(lds + APROG + 12, 1); __builtin_amdgcn_s_setprio(LPRIO); }
#pragma unroll
  for (int i = 0; i < 4; ++i) { a.ka[i] = l0 + lr * 256 + (((4 * i + lg) ^ lr) << 4); PIN(a.ka[i]); }
#pragma unroll
  for (int i = 0; i < 8; ++i) { a.va[i] = l0 + VOFF + (lane >> 2) * 256 + ((i ^ ((lane >> 2) & 7)) << 5) + (lane & 3) * 8; PIN(a.va[i]); }
  a.pg = 0; a.pa = l0 + APROG + (lane & 3) * 4; a.pb = l0 + APROG + (NQ == 2 ? 12 : ((lane & 3) == 3 ? 0 : (lane & 3) * 4)); a.oad = 0; a.last = RPACE ? (unsigned)__builtin_amdgcn_s_memrealtime() - 1000u : 0u;
#pragma unroll
  for (int t = 0; t < NQ; ++t) {
    a.l[t] = 0.f; a.inv[t] = 0.f; a.rs[t] = 0.f; a.p[0][t] = a.p[1][t] = u32x4{0u, 0u, 0u, 0u}; a.negm[t] = f32x4{0.f, 0.f, 0.f, 0.f};
#pragma unroll
    for (int i = 0; i < 8; ++i) { a.o[i][t] = f32x4{0.f, 0.f, 0.f, 0.f}; a.sc[1][i & 3][t] = f32x4{0.f, 0.f, 0.f, 0.f}; }
  }
  WAITVM(32);
  BAR_L(); ST();
#pragma unroll
  for (int m = 0; m < 5; ++m) frag_read<false>(a, m, 0);
#pragma nounroll
  for (int r = 0; r < 4; ++r) {
    attn_step<1, 0>(a, 8 * r, w, lane, lds, l0, rk, rv); ST();
    attn_step<2, 1>(a, 8 * r + 1, w, lane, lds, l0, rk, rv); ST();
#pragma nounroll
    for (int t = 2; t < 8; t += 2) {
      attn_step<0, 0>(a, 8 * r + t, w, lane, lds, l0, rk, rv); ST();
      attn_step<0, 1>(a, 8 * r + t + 1, w, lane, lds, l0, rk, rv); ST();
    }
  }
  {
#pragma unroll
    for (int i = 0; i < 8; ++i) sm_pair(a, a.sc[1], a.p[1], 1, i);
    if (NQ == 2) prog_wait<0, 0>(a.pb, 65);
#pragma unroll
    for (int m = 0; m < 8; ++m) frag_read<true>(a, m, 1);
    sm_close<NQ, 1, true, false>(a, a.sc[1], a.sc[1]);
    SB();
#pragma unroll
    for (int i = 0; i < 16; ++i) {
#pragma unroll
      for (int t = 0; t < NQ; ++t) a.o[i & 7][t] = mfma(a.fr[i & 7], a.p[i >> 3][t], a.o[i & 7][t]);
      if (i < 8) frag_read<true>(a, i + 8, 1);
      SB();
    }
#pragma unroll
    for (int t = 0; t < NQ; ++t) a.inv[t] = __builtin_amdgcn_rcpf(xsum(a.l[t]));
  }
#pragma unroll
  for (int t = 0; t < NQ; ++t)
#pragma unroll
    for (int m = 0; m < 4; ++m) *(LAS u32x4*)(lds + OWN3 + (2 * w + t) * 4096 + m * 1024 + lane * 16) = o_frag(a, m, t);
  prog_post(lds + QPROG + w * 4, 33);
  __builtin_amdgcn_s_setprio(0);
  ST();
}

DI void attn_wave(const int w, const int lane, const int rot, lptr lds, const unsigned l0, rsrc_t rk, rsrc_t rv, rsrc_t rx, rsrc_t rwq, const char* wo, const Ctx& c0, int& nst) {
  const int lg = lane >> 4, lr = lane & 15, h = lane >> 5;
  ST();
  {
    const unsigned wvo = h * 8192 + (((lane & 31) ^ (h | ((w & 1) << 3))) << 4) + c0.g * 1024;
    const unsigned xvo = (lane >> 3) * 1280 + (((lane & 7) ^ ((4 * (w & 1) + lg) & 7)) << 4);
    auto issue = [&](int c, int buf) {
      const int cr = c + rot >= 10 ? c + rot - 10 : c + rot;
      const unsigned xs = w * 10240 + cr * 128, xd = l0 + XP + buf * XCH + w * 1024, ws_ = (cr * 64 + 2 * w) * 8192, wd = l0 + buf * 2 * SLOT + w * 1024;
#pragma unroll
      for (int i = 0; i < 3; ++i) dma_df(rx, xvo, xs + i * 40960, xd + i * 4096);
      dma_df(rx, xvo, xs + 3 * 40960, w < 2 ? xd + 3 * 4096 : l0 + DUMMY);
#pragma unroll
      for (int i = 0; i < 8; ++i) dma_df(rwq, wvo, ws_ + i * 65536, wd + i * 4096);
    };
    issue(0, 2); issue(1, 0);
    if (w == 0) { *(LAS u32x4*)(lds + CNT + (lane & 7) * 16) = u32x4{0u, 0u, 0u, 0u}; }
    const int b = c0.b, g = c0.g; const rsrc_t rwo = mkrsrc(wo, 4096 * 1280);
    u32x2 rw[8], rq2[8]; unsigned short r5[8]; unsigned long long e1;
    const unsigned rb = 4 * lg * 1280 + (b >> 4) * 640, wv0 = rb + w * 128 + lr * 8, wv1 = rb + 512 + w * 32 + lr * 2;
    const unsigned s2 = ((4 * g + ((b >> 2) & 3)) * 128 + (b & 3) * 32) * 1280;
    const unsigned qv = lg * 8 * 8192 + lr * 8 + w * 128 + g * 1024 + 512, sq = b < 20 ? b * 32 * 8192 : OOB;
    constexpr int TN[10] = {0, 0, 0, 9, 8, 8, 4, 0, 5, 0};
#pragma unroll
    for (int c = 0; c < 8; ++c) {
      SB(); asm volatile("s_waitcnt vmcnt(%0) lgkmcnt(0)\n\ts_barrier" :: "i"(12 + TN[c] + (c ? TN[c - 1] : 0)) : "memory"); SB();
      if (c == 5) { _Pragma("unroll") for (int i = 0; i < 8; ++i) PIN(rq2[i]); }
      if (c == 7) { _Pragma("unroll") for (int i = 0; i < 8; ++i) { PIN(rw[i]); PIN(r5[i]); } }
      SB();
      issue(c + 2, (c + 1) % 3);
      SB();
      if (c == 2) {
        e1 = __hip_atomic_load(c0.epoch(), RLX_AGENT) + 1;
#pragma unroll
        for (int jj = 0; jj < 8; ++jj) rq2[jj] = __builtin_amdgcn_raw_buffer_load_b64(rwq, qv, sq + jj * 8192, TAUX);
      }
      if (c == 3) {
#pragma unroll
        for (int jj = 0; jj < 8; ++jj) rw[jj] = __builtin_amdgcn_raw_buffer_load_b64(rwo, wv0 + (jj & 3) * 1280, s2 + (jj >> 2) * 16 * 1280, TAUX);
      }
      if (c == 4) {
#pragma unroll
        for (int jj = 0; jj < 8; ++jj) r5[jj] = __builtin_amdgcn_raw_buffer_load_b16(rwo, wv1 + (jj & 3) * 1280, s2 + (jj >> 2) * 16 * 1280, TAUX);
      }
      if (c == 5) {
#pragma unroll
        for (int e = 0; e < 4; ++e) { u32x4 f;
#pragma unroll
          for (int pp = 0; pp < 4; ++pp) f[pp] = __builtin_amdgcn_perm(rq2[2 * pp + 1][e >> 1], rq2[2 * pp][e >> 1], (e & 1) ? 0x07060302u : 0x05040100u);
          __builtin_amdgcn_raw_buffer_store_b128(f, c0.rqt(), (b * 4 + w) * 4096 + e * 1024 + lane * 16, 0, 16); }
      }
      if (c == 7) {
#pragma unroll
        for (int e = 0; e < 5; ++e) { u32x4 f;
#pragma unroll
          for (int pp = 0; pp < 4; ++pp) f[pp] = e < 4 ? __builtin_amdgcn_perm(rw[2 * pp + 1][(e >> 1) & 1], rw[2 * pp][(e >> 1) & 1], (e & 1) ? 0x07060302u : 0x05040100u)
                                                       : __builtin_amdgcn_perm((unsigned)r5[2 * pp + 1], (unsigned)r5[2 * pp], 0x05040100u);
          __builtin_amdgcn_raw_buffer_store_b128(f, c0.rwt(), (b * 4 + w) * 5120 + e * 1024 + lane * 16, 0, 16); }
      }
    }
    WAITV(17); BAR_L();
    {
      const unsigned long long tag = ((unsigned long long)rfl((unsigned)(e1 >> 32)) << 32) | rfl((unsigned)e1);
      *(LAS unsigned long long*)(lds + TAGW) = tag;
      publish(c0.tflags() + b, lds + CNT + 16, tag, lane);
    }
  }
  if (w < 3) attn_main<2>(w, lds, l0, rk, rv, c0, nst); else attn_main<1>(w, lds, l0, rk, rv, c0, nst);
}

constexpr int LAG = 3;
DI void parrive(unsigned& pc, const int j, lptr lds) { WAITL(); ++pc; prog_post(lds + PPROG + j * 4, pc); }
template <int NE> constexpr int efrag(int e) { return NE == 5 ? e : (NE == 3 ? (e < 2 ? e : 4) : e + 2); }
template <int NE> DI u32x4 wload(rsrc_t rwt, const unsigned lv, const unsigned so, const int e) {
  return __builtin_amdgcn_raw_buffer_load_b128(rwt, lv + (efrag<NE>(e) & 3) * 1024, so + (efrag<NE>(e) >> 2) * 4096, 16);
}
template <int NE>
DI void oproj_pass(const int j, const bool pstv, const Ctx& c0, lptr lds, const unsigned l0, int& nst) {
  const int lane = fresh_lane();
  const Ctx c = c0.fresh();
  const int mrot = ROT ? c.b : 0, side = (c.g ^ (NE != 5)) & 1;
  const rsrc_t rwt = c.rwt();
  const unsigned pa = l0 + APROG + (lane & 3) * 4, lv = lane * 16, tb = (side * 64 + j) * 5120;
  f32x4 acc[NE][7]; u32x4 af[4][NE], bf[4];
#pragma unroll
  for (int k = 0; k < 3; ++k)
#pragma unroll
    for (int e = 0; e < NE; ++e) { af[k][e] = wload<NE>(rwt, lv, tb + ((k + mrot) & 3) * 20480, e); SB(); }
#pragma unroll
  for (int e = 0; e < NE; ++e)
#pragma unroll
    for (int t = 0; t < 7; ++t) acc[e][t] = f32x4{0.f, 0.f, 0.f, 0.f};
  asm volatile("s_nop 1"); SB();
#pragma nounroll
  for (int r = 0; r < 3; ++r) {
    const unsigned reg = qreg(r + 1);
    if (NE != 2) prog_wait<32, 2>(pa, 8 * r + 9);
    PST();
    auto baddr = [&](int gi) { return lp(l0 + reg + ((gi / 7 + mrot) & 3) * 1024 + lv) + (gi % 7) * 4096; };
#pragma unroll
    for (int gi = 0; gi < LAG; ++gi) bf[gi] = lds128(baddr(gi));
    SB();
#pragma unroll
    for (int k = 0; k < 4; ++k) {
      int r2 = r; asm volatile("" : "+s"(r2));
      const unsigned so = tb + ((k ? r2 + 1 : r2) * 4 + ((k + 3 + mrot) & 3)) * 20480;
#pragma unroll
      for (int t = 0; t < 7; ++t) {
        const int gi = 7 * k + t;
#pragma unroll
        for (int e = 0; e < NE; ++e) {
          mfma_ip(acc[e][t], af[k][e], bf[gi & 3]);
          if (e == 0 && gi + LAG < 28) bf[(gi + LAG) & 3] = lds128(baddr(gi + LAG));
          if (e == NE / 2 && t >= 1 && t <= NE) af[(k + 3) & 3][t - 1] = wload<NE>(rwt, lv, so, t - 1);
          SB();
        }
      }
    }
    PST();
  }
#pragma unroll
  for (int e = 0; e < NE; ++e) af[3][e] = wload<NE>(rwt, lv, tb + (12 + ((3 + mrot) & 3)) * 20480, e);
  if (NE != 2) prog_wait<32, 1>(pa, 33);
  if (NE == 5) __builtin_amdgcn_s_setprio(2);
  PST();
  const Ctx c2 = c0.fresh(); const int dst = 2 * j + side;
  const bool keep = NE == 5 && dst == c2.g;
  const rsrc_t rpx = c2.rpx(dst); const unsigned po = c2.g * 17920;
  unsigned ba[4];
#pragma unroll
  for (int k = 0; k < 4; ++k) ba[k] = l0 + OWN3 + ((k + mrot) & 3) * 1024 + lv;
  auto out = [&](int t, int part) {
    if (keep) { *(LAS f32x4*)(lp(l0 + OWNP + lv) + (t * 5 + part) * 1024) = acc[part % NE][t]; if (part == 3) *(LAS f32x4*)(lp(l0 + OWNP + lv) + (t * 5 + 4) * 1024) = acc[NE - 1][t]; }
    else if (part < NE / 2) {
      const f32x4 x = acc[2 * part][t], y = acc[2 * part + 1][t];
      __builtin_amdgcn_raw_buffer_store_b128(u32x4{cvtpk(x[0], y[0]), cvtpk(x[1], y[1]), cvtpk(x[2], y[2]), cvtpk(x[3], y[3])}, rpx, po + lv + t * 2560 + efrag<NE>(2 * part) * 512, 0, 16);
    } else if (part == 2 && (NE & 1)) { const f32x4 x = acc[NE - 1][t];
      __builtin_amdgcn_raw_buffer_store_b64(u32x2{cvtpk(x[0], x[1]), cvtpk(x[2], x[3])}, rpx, po + (lv >> 1) + t * 2560 + 2048, 0, 16); }
  };
#pragma unroll
  for (int gi = 0; gi < LAG; ++gi) bf[gi] = lds128(lp(ba[gi & 3]) + (gi >> 2) * 4096);
  SB();
#pragma unroll
  for (int gi = 0; gi < 28; ++gi) {
    const int t = gi >> 2, k = gi & 3;
#pragma unroll
    for (int e = 0; e < NE; ++e) {
      mfma_ip(acc[e][t], af[k][e], bf[gi & 3]);
      if (e == 0 && gi + LAG < 28) bf[(gi + LAG) & 3] = lds128(lp(ba[(gi + LAG) & 3]) + ((gi + LAG) >> 2) * 4096);
      if (e == NE - 1 && k == 0 && t > 0) {
        asm volatile("s_nop %0" :: "i"(8 - NE));
#pragma unroll
        for (int e2 = 0; e2 < NE; ++e2) PIN(acc[e2][t - 1]);
      }
      if (e == NE - 1 && t > 0) out(t - 1, k);
      SB();
    }
  }
  MFMA_DONE();
#pragma unroll
  for (int e2 = 0; e2 < NE; ++e2) PIN(acc[e2][6]);
  SB();
#pragma unroll
  for (int k = 0; k < 4; ++k) out(6, k);
  if (keep) prog_post(lds + KPROG, 1);
  else {
    const unsigned long long tag = *(const LAS unsigned long long*)(lds + TAGW);
    asm volatile("s_waitcnt vmcnt(0)" ::: "memory");
    if (lv == 0) __hip_atomic_store(c2.xflag(dst) + c2.g + (NE == 2 ? 8 : 0), tag, RLX_AGENT);
  }
  __builtin_amdgcn_s_waitcnt(0x0F70);
  PST();
}

DI void final_part(const int j, const Ctx& c0, lptr lds, const unsigned l0, char* out, int& nst) {
  const Ctx c = c0.fresh(); const int b = c.b, g = c.g; const rsrc_t rpx = c.rpx(g);
  const int lane = fresh_lane(), p = g >> 1, lg = lane >> 4, lr = lane & 15;
  const bool pstv = j == 0; (void)pstv;
  const unsigned long long tg = *(const LAS unsigned long long*)(lds + TAGW), tag = ((unsigned long long)rfl((unsigned)(tg >> 32)) << 32) | rfl((unsigned)tg);
  auto lo = [](unsigned v) { return __uint_as_float(v << 16); };
  auto hi = [](unsigned v) { return __uint_as_float(v & 0xffff0000u); };
  f32x4 a[2][5];
#pragma unroll
  for (int late = 0; late < 2; ++late) {
    {
      const int k = lane & 3;
      gu64* f = c.xflag(g) + (late ? 2 * k + 1 - (g & 1) + (lane & 4) * 2 : 2 * (k == p ? (p + 1) & 3 : k) + (g & 1));
      wait_flag(f, __hip_atomic_load(f, RLX_AGENT), tag);
    }
    u32x4 in[2][4][2]; u32x2 in2[2][4];
#pragma unroll
    for (int i = 0; i < 2; ++i)
#pragma unroll
      for (int k = 0; k < 3 + late; ++k) {
        const int src = late ? 2 * k + 1 - (g & 1) : 2 * (k + (k >= p)) + (g & 1);
        const unsigned o = (i == 1 && j == 3) ? OOB : src * 17920 + (j + 4 * i) * 2560;
        in[i][k][0] = __builtin_amdgcn_raw_buffer_load_b128(rpx, lane * 16 + o, 0, 16);
        in[i][k][1] = __builtin_amdgcn_raw_buffer_load_b128(rpx, lane * 16 + o + 1024, 0, 16);
        in2[i][k] = __builtin_amdgcn_raw_buffer_load_b64(rpx, lane * 8 + o + 2048, 0, 16);
      }
    if (!late) {
      prog_wait<0, 1>(l0 + KPROG, 1);
#pragma unroll
      for (int i = 0; i < 2; ++i)
#pragma unroll
        for (int e = 0; e < 5; ++e) a[i][e] = *(LAS f32x4*)(lds + OWNP + (((i == 1 && j == 3) ? 0 : j + 4 * i) * 5 + e) * 1024 + lane * 16);
    }
#pragma unroll
    for (int i = 0; i < 2; ++i)
#pragma unroll
      for (int k = 0; k < 3 + late; ++k) {
#pragma unroll
        for (int d = 0; d < 8; ++d) { const int reg = d & 3, e = 2 * (d >> 2); a[i][e][reg] += lo(in[i][k][d >> 2][d & 3]); a[i][e + 1][reg] += hi(in[i][k][d >> 2][d & 3]); }
#pragma unroll
        for (int d = 0; d < 2; ++d) { a[i][4][2 * d] += lo(in2[i][k][d]); a[i][4][2 * d + 1] += hi(in2[i][k][d]); }
      }
    PST();
  }
  if (j == 0 && g == 0 && b == 0 && lane == 0) __hip_atomic_store(c.epoch(), tag, RLX_AGENT);
  const rsrc_t rout = mkrsrc(out + (size_t)b * 143360, 143360);
#pragma unroll
  for (int i = 0; i < 2; ++i) {
    const unsigned ob = (i == 1 && j == 3) ? OOB : ((j + 4 * i) * 16 + lr) * 1280 + (g & 1) * 640;
#pragma unroll
    for (int hf = 0; hf < 2; ++hf)
      __builtin_amdgcn_raw_buffer_store_b128(u32x4{cvtpk(a[i][0][2 * hf], a[i][1][2 * hf]), cvtpk(a[i][2][2 * hf], a[i][3][2 * hf]), cvtpk(a[i][0][2 * hf + 1], a[i][1][2 * hf + 1]),
                                                   cvtpk(a[i][2][2 * hf + 1], a[i][3][2 * hf + 1])}, rout, ob + p * 128 + lg * 32 + hf * 16, 0, 0);
    __builtin_amdgcn_raw_buffer_store_b64(u32x2{cvtpk(a[i][4][0], a[i][4][1]), cvtpk(a[i][4][2], a[i][4][3])}, rout, ob + 512 + p * 32 + lg * 8, 0, 0);
  }
}

struct QpState { f32x4 acc[4][7]; u32x4 xf[7]; lptr xa0, xa1; };
DI void qp_init(QpState& q, lptr xb, int lane) {
  const int lg = lane >> 4, lr = lane & 15;
  q.xa0 = xb + lr * 128 + ((lg ^ (lr >> 1)) << 4); q.xa1 = xb + lr * 128 + (((4 + lg) ^ (lr >> 1)) << 4);
#pragma unroll
  for (int t = 0; t < 7; ++t) { q.xf[t] = u32x4{0u, 0u, 0u, 0u};
#pragma unroll
    for (int e = 0; e < 4; ++e) q.acc[e][t] = f32x4{0.f, 0.f, 0.f, 0.f}; }
}
template <bool TR> DI void qp_pack(const QpState& q, u32x4 (&qf)[7][2]) {
#pragma unroll
  for (int t = 0; t < 7; ++t)
#pragma unroll
    for (int hf = 0; hf < 2; ++hf) {
#pragma unroll
      for (int d = 0; d < 4; ++d) { const int i0 = 8 * hf + 2 * d, i1 = i0 + 1;
        qf[t][hf][d] = TR ? cvtpk(q.acc[i0 >> 2][t][i0 & 3] * QSCALE, q.acc[i1 >> 2][t][i1 & 3] * QSCALE) : cvtpk(q.acc[i0 & 3][t][i0 >> 2] * QSCALE, q.acc[i1 & 3][t][i1 >> 2] * QSCALE); }
      PIN(qf[t][hf]);
    }
}
template <int PAR>
DI void qp2_chunk(QpState& q, u32x4 (&af)[4][4], const int c, const int j, const int rot, unsigned& pc, const unsigned pa, const unsigned xvo, const unsigned lv, lptr lds, const unsigned l0,
                  rsrc_t rx, rsrc_t rqt) {
  if (QPACE) prog_wait<0, 2>(pa, (c * QPACE) >> 2);
  WAITV(8);
  ++pc; prog_post(lds + PPROG + j * 4, pc);
  const unsigned pg = prog_read<16>(pa);
  const int c1 = c + 1 + rot >= 10 ? c + 1 + rot - 10 : c + 1 + rot, c2 = c + 2 + rot >= 10 ? c + 2 + rot - 10 : c + 2 + rot;
  const unsigned s1 = c < 9 ? ((2 * c1 + 1) * 4 + j) * 4096 : OOB, s2 = c < 8 ? ((2 * c2) * 4 + j) * 4096 : OOB;
  SB();
#pragma unroll
  for (int u = 0; u < 14; ++u) {
    const int v = (u + 14 - LAG) % 14, t = v % 7, set = u < LAG ? 2 * (PAR ^ 1) + 1 : 2 * PAR + v / 7;
#pragma unroll
    for (int e = 0; e < 4; ++e) {
      mfma_ip(q.acc[e][t], af[set][e], q.xf[t]);
      if (e == 0 && u >= 2) q.xf[u % 7] = lds128((u >= 7 ? q.xa1 : q.xa0) + PAR * XCH + (u % 7) * 2048);
      if (e == 2 && ((u >= LAG && u < LAG + 4) || u >= 10)) PC();
      if (e == 2 && u >= LAG && u < LAG + 4) af[2 * (PAR ^ 1) + 1][u - LAG] = __builtin_amdgcn_raw_buffer_load_b128(rqt, lv + (u - LAG) * 1024, s1, 16);
      if (e == 2 && u >= 10) af[2 * PAR][u - 10] = __builtin_amdgcn_raw_buffer_load_b128(rqt, lv + (u - 10) * 1024, s2, 16);
      SB();
    }
    if (u == 0) { prog_wait<16, 0>(pa, pg, pc); q.xf[0] = lds128(q.xa0 + PAR * XCH); q.xf[1] = lds128(q.xa0 + PAR * XCH + 2048); SB(); }
    if (u == 1) {
      const unsigned xs = c < 9 ? j * 10240 + c1 * 128 : OOB, xd = l0 + (c < 9 ? RC + (PAR ^ 1) * XCH + j * 1024 : DUMMY), st = c < 9 ? 4096u : 0u;
#pragma unroll
      for (int i = 0; i < 3; ++i) { PC(); dma_df(rx, xvo, xs + i * 40960, xd + i * st); }
      PC(); dma_df(rx, xvo, xs + 3 * 40960, j < 2 ? xd + 3 * st : l0 + DUMMY);
      SB();
    }
  }
}

DI void proj_wave(const int j, const int lane, const Ctx& c0, const int rot, lptr lds, const unsigned l0, rsrc_t rx, int& nst) {
  unsigned long long tag, tv;
  const bool pstv = j == 0; (void)pstv;
  if (PPRIO) __builtin_amdgcn_s_setprio(PPRIO);
  const int lg = lane >> 4, lr = lane & 15;
  QpState q;
  u32x4 qf[7][2];
  const unsigned qo = (4 * (j & 1) + lg) * 512 + lr * 16;
  {
    qp_init(q, lds + XP, lane);
    u32x4 af[2][4];
#pragma unroll
    for (int e = 0; e < 4; ++e) af[1][e] = u32x4{0u, 0u, 0u, 0u};
    const int q4 = lr >> 2;
    const unsigned wa = l0 + (8 * lg + q4) * 512 + ((j * 8 + 2 * (lane & 3)) << 4 ^ (((q4 & 1) | ((q4 >> 1) << 3)) << 4));
    int buf = 2;
#pragma nounroll
    for (int c = 0; c < 10; ++c) {
      const unsigned wb0 = wa + buf * 2 * SLOT, wb1 = wb0 ^ 16; const int xo = buf * XCH;
      BAR_L();
#pragma unroll
      for (int u = 0; u < 14; ++u) {
        const int v = (u + 14 - LAG) % 14, t = v % 7, set = u < LAG ? 1 : v / 7;
        auto ard = [&](int ks, int e) { lptr p = lp((e >> 1) ? wb1 : wb0) + ks * 16384 + (e & 1) * 8; const u32x2 lo = ldstr(p), hi = ldstr(p + 2048); af[ks][e] = u32x4{lo[0], lo[1], hi[0], hi[1]}; };
        if (u == 0) { ard(0, 0); q.xf[0] = lds128(q.xa0 + xo); ard(0, 1); SB(); }
#pragma unroll
        for (int e = 0; e < 4; ++e) {
          mfma_ip(q.acc[e][t], af[set][e], q.xf[t]);
          if (e == 0 && u >= 1) q.xf[u % 7] = lds128((u >= 7 ? q.xa1 : q.xa0) + xo + (u % 7) * 2048);
          if (u == 0 && (e & 1)) ard(0, 2 + (e >> 1));
          if (e == 2 && u >= 4 && u < 8) ard(1, u - 4);
          SB();
        }
      }
      buf = buf == 2 ? 0 : buf + 1;
      PST();
    }
#pragma unroll
    for (int t = 7 - LAG; t < 7; ++t)
#pragma unroll
      for (int e = 0; e < 4; ++e) mfma_ip(q.acc[e][t], af[1][e], q.xf[t]);
    MFMA_DONE(); SB();
    BAR_L();
    qp_pack<true>(q, qf);
    lptr qp = lds + ((j >> 1) ? RB : RA) + qo;
#pragma unroll
    for (int t = 0; t < 7; ++t)
#pragma unroll
      for (int hf = 0; hf < 2; ++hf) *(LAS u32x4*)(qp + t * 4096 + hf * 256) = qf[t][hf];
    { const unsigned long long tg = *(const LAS unsigned long long*)(lds + TAGW); tag = ((unsigned long long)rfl((unsigned)(tg >> 32)) << 32) | rfl((unsigned)tg); }
    tv = __hip_atomic_load(c0.fresh().tflags() + (lane & 31), RLX_AGENT);
    BAR_L();
    PST();
  }
  unsigned pc = 0;
  {
    const Ctx c = c0.fresh();
    const rsrc_t rqt = c.rqt();
    const unsigned pa = l0 + APROG + (lane & 3) * 4, lv = lane * 16;
    const unsigned xvo = (lane >> 3) * 1280 + (((lane & 7) ^ ((4 * (j & 1) + lg) & 7)) << 4);
#pragma unroll
    for (int i = 0; i < 3; ++i) dma_df(rx, xvo, j * 10240 + rot * 128 + i * 40960, l0 + RC + j * 1024 + i * 4096);
    dma_df(rx, xvo, j * 10240 + rot * 128 + 3 * 40960, j < 2 ? l0 + RC + j * 1024 + 3 * 4096 : l0 + DUMMY);
    wait_flag(c.tflags() + (lane & 31), tv, tag);
    u32x4 af[4][4];
    const int r1 = rot == 9 ? 0 : rot + 1;
#pragma unroll
    for (int k = 0; k < 3; ++k)
#pragma unroll
      for (int e = 0; e < 4; ++e) { af[k][e] = __builtin_amdgcn_raw_buffer_load_b128(rqt, lv + e * 1024, ((k < 2 ? 2 * rot + k : 2 * r1) * 4 + j) * 4096, 16); SB(); }
#pragma unroll
    for (int e = 0; e < 4; ++e) af[3][e] = u32x4{0u, 0u, 0u, 0u};
    qp_init(q, lds + RC, lane);
    asm volatile("s_nop 1"); SB();
    PST();
#pragma nounroll
    for (int cc = 0; cc < 10; cc += 2) {
      qp2_chunk<0>(q, af, cc, j, rot, pc, pa, xvo, lv, lds, l0, rx, rqt);
      qp2_chunk<1>(q, af, cc + 1, j, rot, pc, pa, xvo, lv, lds, l0, rx, rqt);
      PST();
    }
#pragma unroll
    for (int t = 7 - LAG; t < 7; ++t)
#pragma unroll
      for (int e = 0; e < 4; ++e) mfma_ip(q.acc[e][t], af[3][e], q.xf[t]);
    MFMA_DONE(); SB();
    parrive(pc, j, lds);
    __builtin_amdgcn_s_setprio(XPRIO);
    qp_pack<false>(q, qf);
    prog_wait<16, 0>(pa, pc);
    prog_wait<32, 0>(pa, 1);
#pragma unroll
    for (int t = 0; t < 7; ++t)
#pragma unroll
      for (int hf = 0; hf < 2; ++hf) *(LAS u32x4*)(lds + ((j >> 1) ? RC : RA) + qo + t * 4096 + hf * 256) = qf[t][hf];
    parrive(pc, j, lds);
    PST();
  }
  __builtin_amdgcn_s_waitcnt(0x0F70);
}

__global__ __launch_bounds__(512, 2) void attn_fused(const char* __restrict__ x, const char* __restrict__ K, const char* __restrict__ V,
                                                     const char* __restrict__ wq, const char* __restrict__ wo, char* __restrict__ ws, char* __restrict__ out) {
  extern __shared__ __attribute__((aligned(1024))) char smem[];
  const int w = rfl(threadIdx.x >> 6);
  unsigned long long pc0 = 0;
  if (CODEPF) asm volatile("s_getpc_b64 %0" : "=s"(pc0));
  const int bid = blockIdx.x;
  const int b = ((bid >> 1) & 3) * 8 + (bid >> 5), g = (bid & 1) * 4 + ((bid >> 3) & 3);
  const int rot = ROT ? (((b & 7) * 5) / 4 + ((g & 3) * 5) / 2) % 10 : 0;
  lptr lds = (lptr)smem;
  const unsigned l0 = (unsigned)(uintptr_t)lds;
  const Ctx c = {ws, b, g};
  const rsrc_t rx = mkrsrc(x + (size_t)b * 143360, 143360), rwq = mkrsrc(wq, 640 * 8192);
  int nst = 0; (void)nst;
  if (w < 4) {
    const size_t kvb = ((size_t)b * 32 + g * 4) * 131072;
    attn_wave(w, fresh_lane(), rot, lds, l0, mkrsrc(K + kvb, 524288), mkrsrc(V + kvb, 524288), rx, rwq, wo, c, nst);
    oproj_pass<3>(w, false, c, lds, l0, nst);
    ST(); ST();
  } else {
    if (CODEPF && w < 6) dma_df(mkrsrc((const void*)(pc0 & ~255ull), CODEPF), fresh_lane() * 16, ((blockIdx.x >> 3) * 2 + (w - 4)) * 1024, l0 + DUMMY + 1024);
    proj_wave(w - 4, fresh_lane(), c, rot, lds, l0, rx, nst);
    oproj_pass<5>(w - 4, w == 4, c, lds, l0, nst);
    oproj_pass<2>(w - 4, false, c, lds, l0, nst);
    final_part(w - 4, c, lds, l0, out, nst);
    __builtin_amdgcn_s_waitcnt(0x0F70);
  }
}

extern "C" void kernel_launch(void* const* d_in, const int* in_sizes, int n_in, void* d_out, int out_size, void* d_ws, size_t ws_size, hipStream_t stream) {
  (void)hipFuncSetAttribute((const void*)attn_fused, hipFuncAttributeMaxDynamicSharedMemorySize, LDS_BYTES);
  attn_fused<<<256, 512, LDS_BYTES, stream>>>((const char*)d_in[0], (const char*)d_in[1], (const char*)d_in[2], (const char*)d_in[3], (const char*)d_in[4], (char*)d_ws + WSOFF, (char*)d_out);
}
```

```cpp
#include <hip/hip_runtime.h>
#include <stdint.h>
typedef __attribute__((ext_vector_type(8))) __bf16 bf16x8;
typedef __attribute__((ext_vector_type(2))) __bf16 bf16x2;
typedef __attribute__((ext_vector_type(4))) float f32x4;
typedef __attribute__((ext_vector_type(16))) float f32x16;
typedef __attribute__((ext_vector_type(2))) float f32x2;
typedef __attribute__((ext_vector_type(4))) unsigned u32x4;
typedef __attribute__((ext_vector_type(2))) unsigned u32x2;
typedef __attribute__((ext_vector_type(4))) short s16x4;
#define LAS __attribute__((address_space(3)))
#define DI __device__ __forceinline__
typedef LAS char* lptr;
typedef __amdgpu_buffer_rsrc_t rsrc_t;
typedef __attribute__((address_space(1))) unsigned long long gu64;
#define RLX_AGENT __ATOMIC_RELAXED, __HIP_MEMORY_SCOPE_AGENT

constexpr int SLOT = 16384, VOFF = 2 * SLOT, XCH = 14336, RA = 4 * SLOT, RB = RA + 2 * XCH, RC = RB + 2 * XCH, DUMMY = RC + 2 * XCH, XP = 6 * SLOT,
              TAGW = DUMMY + 4096, CNT = TAGW + 16, APROG = CNT + 32, PPROG = APROG + 16, QPROG = PPROG + 16, KPROG = QPROG + 16, STMP = CNT + 128, ARR = STMP + 512, LDS_BYTES = ARR + 1024;
constexpr float QSCALE = 0.08838834764831845f * 1.4426950408889634f;
constexpr unsigned OOB = 0x40000000u;
constexpr size_t WS_TF = 8192, WS_OF = 16384, WS_XF = WS_OF + 131072, WS_O = 1u << 20, WS_Q = 40u << 20, WS_WT = 48u << 20, WS_QT = 56u << 20, WS_PX = 64u << 20, WS_ST = 128u << 20;
constexpr int OWN3 = 0, OWNP = 2 * XCH;
constexpr unsigned P_Q23 = 12;

DI unsigned rfl(unsigned x) { return __builtin_amdgcn_readfirstlane(x); }
DI unsigned cvtpk(float lo, float hi) { f32x2 v = {lo, hi}; bf16x2 b = __builtin_convertvector(v, bf16x2); return __builtin_bit_cast(unsigned, b); }
DI f32x4 mfma(u32x4 a, u32x4 b, f32x4 c) { return __builtin_amdgcn_mfma_f32_16x16x32_bf16(__builtin_bit_cast(bf16x8, a), __builtin_bit_cast(bf16x8, b), c, 0, 0, 0); }
DI void mfma_ip(f32x4& c, const u32x4& a, const u32x4& b) { asm volatile("v_mfma_f32_16x16x32_bf16 %0, %1, %2, %0" : "+v"(c) : "v"(a), "v"(b)); }
#define MFMA_DONE() asm volatile("s_nop 15")
DI f32x16 mfma32(u32x4 a, u32x4 b, f32x16 c) { return __builtin_amdgcn_mfma_f32_32x32x16_bf16(__builtin_bit_cast(bf16x8, a), __builtin_bit_cast(bf16x8, b), c, 0, 0, 0); }
DI u32x4 lds128(lptr p) { return *(const LAS u32x4*)p; }
DI int fresh_lane() { int l; asm volatile("v_mbcnt_lo_u32_b32 %0, -1, 0\n\tv_mbcnt_hi_u32_b32 %0, -1, %0" : "=v"(l)); return l; }
DI lptr lp(unsigned a) { return (lptr)(uintptr_t)a; }
DI u32x2 ldstr(lptr p) { return __builtin_bit_cast(u32x2, __builtin_amdgcn_ds_read_tr16_b64_v4i16((LAS s16x4*)p)); }
DI rsrc_t mkrsrc(const void* p, unsigned bytes) { return __builtin_amdgcn_make_buffer_rsrc((void*)p, 0, bytes, 0x00020000); }
DI float xmax(float m) {
  auto a = __builtin_amdgcn_permlane16_swap(__float_as_uint(m), __float_as_uint(m), false, false); m = fmaxf(__uint_as_float(a[0]), __uint_as_float(a[1]));
  auto b = __builtin_amdgcn_permlane32_swap(__float_as_uint(m), __float_as_uint(m), false, false); return fmaxf(__uint_as_float(b[0]), __uint_as_float(b[1]));
}
DI float xsum(float m) {
  auto a = __builtin_amdgcn_permlane16_swap(__float_as_uint(m), __float_as_uint(m), false, false); m = __uint_as_float(a[0]) + __uint_as_float(a[1]);
  auto b = __builtin_amdgcn_permlane32_swap(__float_as_uint(m), __float_as_uint(m), false, false); return __uint_as_float(b[0]) + __uint_as_float(b[1]);
}
#define DFPOL ""
#define TAUX 0
#define KVPOL "nt"
#define VPOL ""
template <int OFF, bool V = false> DI void dma_nt(rsrc_t r, unsigned voff, unsigned soff, unsigned ldsaddr) {
  if (V) asm volatile("s_mov_b32 m0, %3\n\ts_nop 0\n\tbuffer_load_dwordx4 %0, %1, %2 offen offset:%4 " VPOL " lds" :: "v"(voff), "s"(r), "s"(soff), "s"(ldsaddr), "i"(OFF) : "memory");
  else asm volatile("s_mov_b32 m0, %3\n\ts_nop 0\n\tbuffer_load_dwordx4 %0, %1, %2 offen offset:%4 " KVPOL " lds" :: "v"(voff), "s"(r), "s"(soff), "s"(ldsaddr), "i"(OFF) : "memory");
}
DI void dma_df(rsrc_t r, unsigned voff, unsigned soff, unsigned ldsaddr) {
  asm volatile("s_mov_b32 m0, %3\n\ts_nop 0\n\tbuffer_load_dwordx4 %0, %1, %2 offen " DFPOL "lds" :: "v"(voff), "s"(r), "s"(soff), "s"(ldsaddr) : "memory");
}
DI void dma_sc1(rsrc_t r, unsigned voff, unsigned soff, unsigned ldsaddr) {
  asm volatile("s_mov_b32 m0, %3\n\ts_nop 0\n\tbuffer_load_dwordx4 %0, %1, %2 offen sc1 lds" :: "v"(voff), "s"(r), "s"(soff), "s"(ldsaddr) : "memory");
}
DI void touch(rsrc_t r, unsigned voff, unsigned ldsaddr) {
  asm volatile("s_mov_b32 m0, %2\n\ts_nop 0\n\tbuffer_load_dword %0, %1, 0 offen lds" :: "v"(voff), "s"(r), "s"(ldsaddr) : "memory");
}
#define SB() __builtin_amdgcn_sched_barrier(0)
#define WAITV(N) do { SB(); asm volatile("s_waitcnt vmcnt(" #N ") lgkmcnt(0)" ::: "memory"); SB(); } while (0)
#define WAITVM(N) do { SB(); asm volatile("s_waitcnt vmcnt(" #N ")" ::: "memory"); SB(); } while (0)
#define WAITL() do { SB(); asm volatile("s_waitcnt lgkmcnt(0)" ::: "memory"); SB(); } while (0)
#define BAR_L() do { SB(); asm volatile("s_waitcnt lgkmcnt(0)\n\ts_barrier" ::: "memory"); SB(); } while (0)
#define ST() do {} while (0)
#define PST() do {} while (0)
#define PST2() do {} while (0)
#define PCAP 0
#define PC() do { if (PCAP) asm volatile("s_waitcnt vmcnt(%0)" :: "i"(PCAP - 1) : "memory"); } while (0)
#define PIN(x) asm volatile("" : "+v"(x))
#define NODMA 0
#define DPOS 2
#define DSTR 8
#define CAP 16
#define SAUX 16
#define PPRIO 0
#define ABL 0
#define TE0 2
#define XPRIO 0
#define PF 0
#define VP0 2
#define VST 4
#define KP0 18
#define KST 4
#define QPACE 10
#define OPACE 0
#define OP0 26
#define MAP 4
#define OSA 16
#define WSOFF 0
#define CODEPF 0
#define NH 2
#define SSL 0
#define DSL 0
#define LPRIO 3
#define ILV 0
#define TC 1
#define TN0 0, 9, 8, 8, 0, 4, 0, 5, 0, 0
#define LCAP 0
#define RPACE 0
#define ROT 1
#define QLD0 6
#define PFD 16
constexpr float PLIM = 1024.f;

template <int OFF> DI unsigned prog_read(unsigned pa) { return *(volatile LAS unsigned*)(lp(pa) + OFF); }
template <int OFF, int SLEEP> DI void prog_wait(unsigned pa, unsigned v, unsigned t) {
  unsigned n;
  asm volatile("s_mov_b32 %1, 0x1000000\n"
               "1:\n\tv_cmp_gt_u32 vcc, %3, %0\n\ts_cbranch_vccz 2f\n\ts_sleep %5\n\tds_read_b32 %0, %2 offset:%4\n\ts_sub_u32 %1, %1, 1\n\ts_waitcnt lgkmcnt(0)\n\ts_cbranch_scc0 1b\n2:"
               : "+v"(v), "=&s"(n) : "v"(pa), "s"(t), "i"(OFF), "i"(SLEEP) : "memory", "vcc", "scc");
}
template <int OFF, int SLEEP> DI void prog_wait(unsigned pa, unsigned t) { prog_wait<OFF, SLEEP>(pa, prog_read<OFF>(pa), t); }
DI void prog_post(lptr p, unsigned v) { asm volatile("" ::: "memory"); *(volatile LAS unsigned*)p = v; }

DI void publish(gu64* flag, lptr cnt, unsigned long long tag, int lane) {
  asm volatile("s_waitcnt vmcnt(0)" ::: "memory");
  if (lane == 0) {
    const unsigned old = __hip_atomic_fetch_add((LAS unsigned*)cnt, 1u, __ATOMIC_RELAXED, __HIP_MEMORY_SCOPE_WORKGROUP);
    asm volatile("" : "+s"(tag));
    if (old == 3u) __hip_atomic_store(flag, tag, RLX_AGENT);
  }
}
DI void wait_flag(gu64* f, unsigned long long v, unsigned long long tag) {
#pragma nounroll
  for (unsigned spins = 0; spins < (1u << 15); ++spins) {
    if (__all(v == tag)) break;
    __builtin_amdgcn_s_sleep(2);
    v = __hip_atomic_load(f, RLX_AGENT);
  }
  __builtin_amdgcn_s_waitcnt(0x0F70);
  __builtin_amdgcn_fence(__ATOMIC_ACQUIRE, "wavefront");
  asm volatile("" ::: "memory");
}

struct Ctx {
  char* ws; int b, g;
  DI Ctx fresh() const { Ctx c = *this; asm volatile("" : "+s"(c.b), "+s"(c.g)); return c; }
  DI gu64* epoch() const { return (gu64*)ws; }
  DI gu64* tflags() const { return (gu64*)(ws + WS_TF + g * 256); }
  DI gu64* xflag(int dst) const { return (gu64*)(ws + WS_XF + (b * 8 + dst) * 128); }
  DI rsrc_t rpx(int dst) const { return mkrsrc(ws + WS_PX + (size_t)(b * 8 + dst) * 143360, 143360); }
  DI rsrc_t rwt() const { return mkrsrc(ws + WS_WT + (size_t)g * 655360, 655360); }
  DI rsrc_t rqt() const { return mkrsrc(ws + WS_QT + (size_t)g * 327680, 327680); }
};
template <int NQ> struct AttnState {
  u32x4 q[NQ][4], fr[8], p[2][NQ];
  f32x4 o[8][NQ], negm[NQ], sc[2][4][NQ];
  float l[NQ], rs[NQ], inv[NQ];
  unsigned ka[4], va[8], kvo, vvo, pa, pb, pg, oad, last;
};

DI int qreg(int r) { return r == 3 ? RC : ((r & 1) ? RB : RA); }
template <int NQ> DI void sm_pair(AttnState<NQ>& a, const f32x4 (&x)[4][NQ], u32x4 (&p)[NQ], const int hf, const int n) {
  const int t = n >> 2, d = n & 3;
  if (t >= NQ) return;
  const float e0 = __builtin_amdgcn_exp2f(x[2 * hf + (d >> 1)][t][2 * (d & 1)]), e1 = __builtin_amdgcn_exp2f(x[2 * hf + (d >> 1)][t][2 * (d & 1) + 1]);
  a.rs[t] = d ? (a.rs[t] + e0) + e1 : e0 + e1;
  p[t][d] = cvtpk(e0, e1);
  PIN(p[t][d]); PIN(a.rs[t]);
}
template <int NQ, int HF, bool ADJ>
DI void fixup(AttnState<NQ>& a, f32x4 (&x)[4][NQ], f32x4 (&y)[4][NQ]) {
#pragma unroll
  for (int t = 0; t < NQ; ++t) {
    float mx = x[3][t][3];
#pragma unroll
    for (int i = 8 * HF; i < 15; ++i) mx = fmaxf(mx, x[i >> 2][t][i & 3]);
    const float dl = __builtin_ceilf(fmaxf(xmax(mx), 0.f)), sc = __builtin_amdgcn_exp2f(-dl);
    a.l[t] *= sc; a.negm[t] -= dl;
#pragma unroll
    for (int dt = 0; dt < 8; ++dt) a.o[dt][t] *= sc;
    if (HF) {
#pragma unroll
      for (int d = 0; d < 4; ++d) { const unsigned v = a.p[0][t][d]; a.p[0][t][d] = cvtpk(__uint_as_float(v << 16) * sc, __uint_as_float(v & 0xffff0000u) * sc); }
    }
#pragma unroll
    for (int kt = 2 * HF; kt < 4; ++kt) x[kt][t] -= dl;
    float r = 0.f;
#pragma unroll
    for (int d = 0; d < 4; ++d) { const float e0 = __builtin_amdgcn_exp2f(x[2 * HF + (d >> 1)][t][2 * (d & 1)]), e1 = __builtin_amdgcn_exp2f(x[2 * HF + (d >> 1)][t][2 * (d & 1) + 1]);
      r += e0 + e1; a.p[HF][t][d] = cvtpk(e0, e1); }
    a.rs[t] = r;
    if (ADJ) {
#pragma unroll
      for (int kt = 0; kt < 4; ++kt) y[kt][t] -= dl;
    }
  }
}
template <int NQ, int HF, bool CHECK, bool ADJ>
DI void sm_close(AttnState<NQ>& a, f32x4 (&x)[4][NQ], f32x4 (&y)[4][NQ]) {
  if (CHECK) {
    if (NQ == 1) PIN(a.p[HF][0]);
    float m = NQ == 2 ? a.rs[0] + a.rs[NQ - 1] : a.rs[0]; PIN(m);
    if (__builtin_expect(__any(!(m <= PLIM)), 0)) fixup<NQ, HF, ADJ>(a, x, y);
  }
#pragma unroll
  for (int t = 0; t < NQ; ++t) { PIN(a.rs[t]); a.l[t] += a.rs[t]; PIN(a.l[t]); }
}
template <int NQ> DI u32x4 o_frag(const AttnState<NQ>& a, const int m, const int t) {
  const f32x4 x = a.o[2 * m][t] * a.inv[t], y = a.o[2 * m + 1][t] * a.inv[t];
  return u32x4{cvtpk(x[0], x[1]), cvtpk(x[2], x[3]), cvtpk(y[0], y[1]), cvtpk(y[2], y[3])};
}
template <bool V, int NQ> DI void frag_read(AttnState<NQ>& a, const int m, const int par) {
  if (!V) a.fr[m & 7] = lds128(lp(a.ka[m & 3]) + par * SLOT + (m >> 2) * 4096);
  else { lptr p = lp(a.va[m & 7]) + par * SLOT + (m >> 3) * 8192; const u32x2 v0 = ldstr(p), v1 = ldstr(p + 4096); a.fr[m & 7] = u32x4{v0[0], v0[1], v1[0], v1[1]}; }
}
template <bool V, int NQ> DI void tile_issue(const AttnState<NQ>& a, rsrc_t r, const unsigned gsrc, const unsigned dst, const unsigned st = 4096) {
  if (NODMA) return;
#pragma unroll
  for (int i = 0; i < 4; ++i) {
    if (LCAP) asm volatile("s_waitcnt vmcnt(%0)" :: "i"(LCAP - 4) : "memory");
    dma_nt<0, V>(r, V ? a.vvo : a.kvo, gsrc + i * 4096, dst + i * st);          dma_nt<1024, V>(r, V ? a.vvo ^ 128 : a.kvo ^ 64, gsrc + i * 4096, dst + i * st);
    dma_nt<2048, V>(r, V ? a.vvo : a.kvo ^ 128, gsrc + i * 4096, dst + i * st); dma_nt<3072, V>(r, V ? a.vvo ^ 128 : a.kvo ^ 192, gsrc + i * 4096, dst + i * st);
  }
}
template <bool CV, int NQ> DI void phase_io(AttnState<NQ>& a, const int i, const int cp, const int np, const unsigned val, const int w, lptr lds) {
  if (i <= 10) frag_read<CV>(a, i + 5, cp);
  if (NQ == 2 && i == 10) {
    SB();
    prog_post(lds + APROG + w * 4, val);
    a.pg = prog_read<0>(a.pb);
  }
  if (NQ == 2 && i == 12) prog_wait<0, 0>(a.pb, a.pg, val);
  if (i == 13 || i == 14) { frag_read<!CV>(a, 2 * (i - 13), np); frag_read<!CV>(a, 2 * (i - 13) + 1, np); }
  if (i == 15) frag_read<!CV>(a, 4, np);
}
template <bool V, int NQ> DI void loader_turn(AttnState<NQ>& a, const unsigned val, lptr lds, rsrc_t r, const unsigned gsrc, const unsigned dst) {
  if (NQ == 1) {
    WAITVM(16);
    prog_post(lds + APROG + 12, val + 1);
    prog_wait<0, 1>(a.pb, val);
    if (RPACE) {
      asm volatile("1:\n\ts_memrealtime vcc\n\ts_waitcnt lgkmcnt(0)\n\ts_sub_u32 vcc_hi, vcc_lo, %0\n\ts_cmp_lt_u32 vcc_hi, %1\n\ts_cbranch_scc1 1b\n\ts_mov_b32 %0, vcc_lo"
                   : "+s"(a.last) : "i"(RPACE) : "memory", "scc", "vcc");
    }
    tile_issue<V>(a, r, gsrc, gsrc < 32 * SLOT ? dst : (unsigned)(uintptr_t)lds + DUMMY, gsrc < 32 * SLOT ? 4096u : 0u);
    SB();
  }
}
template <int KIND, int PAR, int NQ>
DI void attn_step(AttnState<NQ>& a, const int s, const int w, const int lane, lptr lds, const unsigned l0, rsrc_t rk, rsrc_t rv) {
  const f32x4 zero = {0.f, 0.f, 0.f, 0.f};
  f32x4 (&c)[4][NQ] = a.sc[PAR]; f32x4 (&d)[4][NQ] = a.sc[PAR ^ 1];
  SB();
  if (KIND == 1) {
    prog_wait<16, 1>(a.pa, s < 16 ? 0u : P_Q23);
    const unsigned qa = l0 + qreg(s >> 3) + 2 * w * 4096 + lane * 16;
#pragma unroll
    for (int i = 0; i < 4; ++i)
#pragma unroll
      for (int t = 0; t < NQ; ++t) a.q[t][i] = lds128(lp(qa) + t * 4096 + i * 1024);
    a.oad = qa;
    SB();
  }
#pragma unroll
  for (int i = 0; i < 16; ++i) {
    constexpr int PA[16] = {-1, 0, 4, -1, 1, 5, -1, 2, 6, -1, -1, 3, 7, -1, -1, -1};
#pragma unroll
    for (int t = 0; t < NQ; ++t) {
      c[i >> 2][t] = mfma(a.fr[i & 7], a.q[t][i & 3], (i & 3) ? c[i >> 2][t] : (KIND == 1 ? zero : a.negm[t]));
      if (t == 0) { if (PA[i] >= 0) sm_pair(a, d, a.p[1], 1, PA[i]); SB(); }
    }
    if (i == 12) PIN(c[3][NQ - 1]);
    phase_io<false>(a, i, PAR, PAR ^ 1, 2 * s + 1, w, lds);
    if (KIND == 2 && i >= 8 && ((i - 8) >> 2) < NQ) *(LAS u32x4*)(lp(a.oad) + ((i - 8) >> 2) * 4096 + (i & 3) * 1024) = o_frag(a, i & 3, ((i - 8) >> 2) % NQ);
    SB();
  }
  if (KIND == 2) prog_post(lds + QPROG + w * 4, s);
  loader_turn<false>(a, 2 * s + 1, lds, rk, (s + 2) * SLOT, l0 + PAR * SLOT);
  sm_close<NQ, 1, KIND != 2, KIND == 0>(a, d, c);
  if (KIND == 1) {
#pragma unroll
    for (int t = 0; t < NQ; ++t) { a.inv[t] = __builtin_amdgcn_rcpf(xsum(a.l[t])); a.l[t] = 0.f; }
  }
  SB();
  u32x4 pn[NQ];
#pragma unroll
  for (int i = 0; i < 16; ++i) {
    constexpr int PB[16] = {-1, -1, -1, 0, -1, 4, -1, 1, -1, 5, -1, 2, 6, -1, 3, 7};
#pragma unroll
    for (int t = 0; t < NQ; ++t) {
      a.o[i & 7][t] = mfma(a.fr[i & 7], a.p[i >> 3][t], (KIND == 2 && i < 8) ? zero : a.o[i & 7][t]);
      if (t == 0) { if (PB[i] >= 0) sm_pair(a, c, pn, 0, PB[i]); SB(); }
    }
    if (i == 12) PIN(a.o[4][NQ - 1]);
    phase_io<true>(a, i, PAR ^ 1, PAR ^ 1, 2 * s + 2, w, lds);
    if (KIND == 1 && (i == 1 || i == 2) && i - 1 < NQ) {
      const int t = (i - 1) % NQ;
      float m0 = c[3][t][3];
#pragma unroll
      for (int e = 0; e < 15; ++e) m0 = fmaxf(m0, c[e >> 2][t][e & 3]);
      m0 = xmax(m0);
#pragma unroll
      for (int kt = 0; kt < 4; ++kt) { c[kt][t] -= m0; PIN(c[kt][t]); }
      a.negm[t] = f32x4{-m0, -m0, -m0, -m0}; PIN(a.negm[t]);
    }
    SB();
  }
#pragma unroll
  for (int t = 0; t < NQ; ++t) a.p[0][t] = pn[t];
  loader_turn<true>(a, 2 * s + 2, lds, rv, (s + 1) * SLOT, l0 + VOFF + (PAR ^ 1) * SLOT);
  sm_close<NQ, 0, KIND != 1, false>(a, c, c);
}

template <int NQ>
DI void attn_main(const int w, lptr lds, const unsigned l0, rsrc_t rk, rsrc_t rv, const Ctx& c0, int& nst) {
  const int lane = fresh_lane(), lg = lane >> 4, lr = lane & 15;
  AttnState<NQ> a;
  a.kvo = lg * 256 + ((lr ^ lg) << 4);
  a.vvo = lg * 256 + ((lr ^ (lg << 1)) << 4);
  if (NQ == 1) { tile_issue<false>(a, rk, 0, l0); }
  BAR_L();
  if (NQ == 1) { tile_issue<false>(a, rk, SLOT, l0 + SLOT); }
  BAR_L();
  if (NQ == 1) { tile_issue<true>(a, rv, 0, l0 + VOFF); prog_post(lds + APROG + 12, 1); __builtin_amdgcn_s_setprio(LPRIO); }
#pragma unroll
  for (int i = 0; i < 4; ++i) { a.ka[i] = l0 + lr * 256 + (((4 * i + lg) ^ lr) << 4); PIN(a.ka[i]); }
#pragma unroll
  for (int i = 0; i < 8; ++i) { a.va[i] = l0 + VOFF + (lane >> 2) * 256 + ((i ^ ((lane >> 2) & 7)) << 5) + (lane & 3) * 8; PIN(a.va[i]); }
  a.pg = 0; a.pa = l0 + APROG + (lane & 3) * 4; a.pb = l0 + APROG + (NQ == 2 ? 12 : ((lane & 3) == 3 ? 0 : (lane & 3) * 4)); a.oad = 0; a.last = RPACE ? (unsigned)__builtin_amdgcn_s_memrealtime() - 1000u : 0u;
#pragma unroll
  for (int t = 0; t < NQ; ++t) {
    a.l[t] = 0.f; a.inv[t] = 0.f; a.rs[t] = 0.f; a.p[0][t] = a.p[1][t] = u32x4{0u, 0u, 0u, 0u}; a.negm[t] = f32x4{0.f, 0.f, 0.f, 0.f};
#pragma unroll
    for (int i = 0; i < 8; ++i) { a.o[i][t] = f32x4{0.f, 0.f, 0.f, 0.f}; a.sc[1][i & 3][t] = f32x4{0.f, 0.f, 0.f, 0.f}; }
  }
  WAITVM(32);
  BAR_L(); ST();
#pragma unroll
  for (int m = 0; m < 5; ++m) frag_read<false>(a, m, 0);
#pragma nounroll
  for (int r = 0; r < 4; ++r) {
    attn_step<1, 0>(a, 8 * r, w, lane, lds, l0, rk, rv); ST();
    attn_step<2, 1>(a, 8 * r + 1, w, lane, lds, l0, rk, rv); ST();
#pragma nounroll
    for (int t = 2; t < 8; t += 2) {
      attn_step<0, 0>(a, 8 * r + t, w, lane, lds, l0, rk, rv); ST();
      attn_step<0, 1>(a, 8 * r + t + 1, w, lane, lds, l0, rk, rv); ST();
    }
  }
  {
#pragma unroll
    for (int i = 0; i < 8; ++i) sm_pair(a, a.sc[1], a.p[1], 1, i);
    if (NQ == 2) prog_wait<0, 0>(a.pb, 65);
#pragma unroll
    for (int m = 0; m < 8; ++m) frag_read<true>(a, m, 1);
    sm_close<NQ, 1, true, false>(a, a.sc[1], a.sc[1]);
    SB();
#pragma unroll
    for (int i = 0; i < 16; ++i) {
#pragma unroll
      for (int t = 0; t < NQ; ++t) a.o[i & 7][t] = mfma(a.fr[i & 7], a.p[i >> 3][t], a.o[i & 7][t]);
      if (i < 8) frag_read<true>(a, i + 8, 1);
      SB();
    }
#pragma unroll
    for (int t = 0; t < NQ; ++t) a.inv[t] = __builtin_amdgcn_rcpf(xsum(a.l[t]));
  }
#pragma unroll
  for (int t = 0; t < NQ; ++t)
#pragma unroll
    for (int m = 0; m < 4; ++m) *(LAS u32x4*)(lds + OWN3 + (2 * w + t) * 4096 + m * 1024 + lane * 16) = o_frag(a, m, t);
  prog_post(lds + QPROG + w * 4, 33);
  __builtin_amdgcn_s_setprio(0);
  ST();
}

DI void attn_wave(const int w, const int lane, const int rot, lptr lds, const unsigned l0, rsrc_t rk, rsrc_t rv, rsrc_t rx, rsrc_t rwq, const char* wo, const Ctx& c0, int& nst) {
  const int lg = lane >> 4, lr = lane & 15, h = lane >> 5;
  ST();
  {
    const unsigned wvo = h * 8192 + (((lane & 31) ^ (h | ((w & 1) << 3))) << 4) + c0.g * 1024;
    const unsigned xvo = (lane >> 3) * 1280 + (((lane & 7) ^ ((4 * (w & 1) + lg) & 7)) << 4);
    auto issue = [&](int c, int buf) {
      const int cr = c + rot >= 10 ? c + rot - 10 : c + rot;
      const unsigned xs = w * 10240 + cr * 128, xd = l0 + XP + buf * XCH + w * 1024, ws_ = (cr * 64 + 2 * w) * 8192, wd = l0 + buf * 2 * SLOT + w * 1024;
#pragma unroll
      for (int i = 0; i < 3; ++i) dma_df(rx, xvo, xs + i * 40960, xd + i * 4096);
      dma_df(rx, xvo, xs + 3 * 40960, w < 2 ? xd + 3 * 4096 : l0 + DUMMY);
#pragma unroll
      for (int i = 0; i < 8; ++i) dma_df(rwq, wvo, ws_ + i * 65536, wd + i * 4096);
    };
    issue(0, 2); issue(1, 0);
    if (w == 0) { *(LAS u32x4*)(lds + CNT + (lane & 7) * 16) = u32x4{0u, 0u, 0u, 0u}; }
    const int b = c0.b, g = c0.g; const rsrc_t rwo = mkrsrc(wo, 4096 * 1280);
    u32x2 rw[8], rq2[8]; unsigned short r5[8]; unsigned long long e1;
    const unsigned rb = 4 * lg * 1280 + (b >> 4) * 640, wv0 = rb + w * 128 + lr * 8, wv1 = rb + 512 + w * 32 + lr * 2;
    const unsigned s2 = ((4 * g + ((b >> 2) & 3)) * 128 + (b & 3) * 32) * 1280;
    const unsigned qv = lg * 8 * 8192 + lr * 8 + w * 128 + g * 1024 + 512, sq = b < 20 ? b * 32 * 8192 : OOB;
    constexpr int TN[10] = {0, 0, 0, 9, 8, 8, 4, 0, 5, 0};
#pragma unroll
    for (int c = 0; c < 8; ++c) {
      SB(); asm volatile("s_waitcnt vmcnt(%0) lgkmcnt(0)\n\ts_barrier" :: "i"(12 + TN[c] + (c ? TN[c - 1] : 0)) : "memory"); SB();
      if (c == 5) { _Pragma("unroll") for (int i = 0; i < 8; ++i) PIN(rq2[i]); }
      if (c == 7) { _Pragma("unroll") for (int i = 0; i < 8; ++i) { PIN(rw[i]); PIN(r5[i]); } }
      SB();
      issue(c + 2, (c + 1) % 3);
      SB();
      if (c == 2) {
        e1 = __hip_atomic_load(c0.epoch(), RLX_AGENT) + 1;
#pragma unroll
        for (int jj = 0; jj < 8; ++jj) rq2[jj] = __builtin_amdgcn_raw_buffer_load_b64(rwq, qv, sq + jj * 8192, TAUX);
      }
      if (c == 3) {
#pragma unroll
        for (int jj = 0; jj < 8; ++jj) rw[jj] = __builtin_amdgcn_raw_buffer_load_b64(rwo, wv0 + (jj & 3) * 1280, s2 + (jj >> 2) * 16 * 1280, TAUX);
      }
      if (c == 4) {
#pragma unroll
        for (int jj = 0; jj < 8; ++jj) r5[jj] = __builtin_amdgcn_raw_buffer_load_b16(rwo, wv1 + (jj & 3) * 1280, s2 + (jj >> 2) * 16 * 1280, TAUX);
      }
      if (c == 5) {
#pragma unroll
        for (int e = 0; e < 4; ++e) { u32x4 f;
#pragma unroll
          for (int pp = 0; pp < 4; ++pp) f[pp] = __builtin_amdgcn_perm(rq2[2 * pp + 1][e >> 1], rq2[2 * pp][e >> 1], (e & 1) ? 0x07060302u : 0x05040100u);
          __builtin_amdgcn_raw_buffer_store_b128(f, c0.rqt(), (b * 4 + w) * 4096 + e * 1024 + lane * 16, 0, 16); }
      }
      if (c == 7) {
#pragma unroll
        for (int e = 0; e < 5; ++e) { u32x4 f;
#pragma unroll
          for (int pp = 0; pp < 4; ++pp) f[pp] = e < 4 ? __builtin_amdgcn_perm(rw[2 * pp + 1][(e >> 1) & 1], rw[2 * pp][(e >> 1) & 1], (e & 1) ? 0x07060302u : 0x05040100u)
                                                       : __builtin_amdgcn_perm((unsigned)r5[2 * pp + 1], (unsigned)r5[2 * pp], 0x05040100u);
          __builtin_amdgcn_raw_buffer_store_b128(f, c0.rwt(), (b * 4 + w) * 5120 + e * 1024 + lane * 16, 0, 16); }
      }
    }
    WAITV(17); BAR_L();
    {
      const unsigned long long tag = ((unsigned long long)rfl((unsigned)(e1 >> 32)) << 32) | rfl((unsigned)e1);
      *(LAS unsigned long long*)(lds + TAGW) = tag;
      publish(c0.tflags() + b, lds + CNT + 16, tag, lane);
    }
  }
  if (w < 3) attn_main<2>(w, lds, l0, rk, rv, c0, nst); else attn_main<1>(w, lds, l0, rk, rv, c0, nst);
}

constexpr int LAG = 3;
DI void parrive(unsigned& pc, const int j, lptr lds) { WAITL(); ++pc; prog_post(lds + PPROG + j * 4, pc); }
template <int NE> constexpr int efrag(int e) { return NE == 5 ? e : (NE == 3 ? (e < 2 ? e : 4) : e + 2); }
template <int NE> DI u32x4 wload(rsrc_t rwt, const unsigned lv, const unsigned so, const int e) {
  return __builtin_amdgcn_raw_buffer_load_b128(rwt, lv + (efrag<NE>(e) & 3) * 1024, so + (efrag<NE>(e) >> 2) * 4096, 16);
}
template <int NE>
DI void oproj_pass(const int j, const bool pstv, const Ctx& c0, lptr lds, const unsigned l0, int& nst) {
  const int lane = fresh_lane();
  const Ctx c = c0.fresh();
  const int mrot = ROT ? c.b : 0, side = (c.g ^ (NE != 5)) & 1;
  const rsrc_t rwt = c.rwt();
  const unsigned pa = l0 + APROG + (lane & 3) * 4, lv = lane * 16, tb = (side * 64 + j) * 5120;
  f32x4 acc[NE][7]; u32x4 af[4][NE], bf[4];
#pragma unroll
  for (int k = 0; k < 3; ++k)
#pragma unroll
    for (int e = 0; e < NE; ++e) { af[k][e] = wload<NE>(rwt, lv, tb + ((k + mrot) & 3) * 20480, e); SB(); }
#pragma unroll
  for (int e = 0; e < NE; ++e)
#pragma unroll
    for (int t = 0; t < 7; ++t) acc[e][t] = f32x4{0.f, 0.f, 0.f, 0.f};
  asm volatile("s_nop 1"); SB();
#pragma nounroll
  for (int r = 0; r < 3; ++r) {
    const unsigned reg = qreg(r + 1);
    if (NE != 2) prog_wait<32, 2>(pa, 8 * r + 9);
    PST();
    auto baddr = [&](int gi) { return lp(l0 + reg + ((gi / 7 + mrot) & 3) * 1024 + lv) + (gi % 7) * 4096; };
#pragma unroll
    for (int gi = 0; gi < LAG; ++gi) bf[gi] = lds128(baddr(gi));
    SB();
#pragma unroll
    for (int k = 0; k < 4; ++k) {
      int r2 = r; asm volatile("" : "+s"(r2));
      const unsigned so = tb + ((k ? r2 + 1 : r2) * 4 + ((k + 3 + mrot) & 3)) * 20480;
#pragma unroll
      for (int t = 0; t < 7; ++t) {
        const int gi = 7 * k + t;
#pragma unroll
        for (int e = 0; e < NE; ++e) {
          mfma_ip(acc[e][t], af[k][e], bf[gi & 3]);
          if (e == 0 && gi + LAG < 28) bf[(gi + LAG) & 3] = lds128(baddr(gi + LAG));
          if (e == NE / 2 && t >= 1 && t <= NE) af[(k + 3) & 3][t - 1] = wload<NE>(rwt, lv, so, t - 1);
          SB();
        }
      }
    }
    PST();
  }
#pragma unroll
  for (int e = 0; e < NE; ++e) af[3][e] = wload<NE>(rwt, lv, tb + (12 + ((3 + mrot) & 3)) * 20480, e);
  if (NE != 2) prog_wait<32, 1>(pa, 33);
  if (NE == 5) __builtin_amdgcn_s_setprio(2);
  PST();
  const Ctx c2 = c0.fresh(); const int dst = 2 * j + side;
  const bool keep = NE == 5 && dst == c2.g;
  const rsrc_t rpx = c2.rpx(dst); const unsigned po = c2.g * 17920;
  unsigned ba[4];
#pragma unroll
  for (int k = 0; k < 4; ++k) ba[k] = l0 + OWN3 + ((k + mrot) & 3) * 1024 + lv;
  auto out = [&](int t, int part) {
    if (keep) { *(LAS f32x4*)(lp(l0 + OWNP + lv) + (t * 5 + part) * 1024) = acc[part % NE][t]; if (part == 3) *(LAS f32x4*)(lp(l0 + OWNP + lv) + (t * 5 + 4) * 1024) = acc[NE - 1][t]; }
    else if (part < NE / 2) {
      const f32x4 x = acc[2 * part][t], y = acc[2 * part + 1][t];
      __builtin_amdgcn_raw_buffer_store_b128(u32x4{cvtpk(x[0], y[0]), cvtpk(x[1], y[1]), cvtpk(x[2], y[2]), cvtpk(x[3], y[3])}, rpx, po + lv + t * 2560 + efrag<NE>(2 * part) * 512, 0, 16);
    } else if (part == 2 && (NE & 1)) { const f32x4 x = acc[NE - 1][t];
      __builtin_amdgcn_raw_buffer_store_b64(u32x2{cvtpk(x[0], x[1]), cvtpk(x[2], x[3])}, rpx, po + (lv >> 1) + t * 2560 + 2048, 0, 16); }
  };
#pragma unroll
  for (int gi = 0; gi < LAG; ++gi) bf[gi] = lds128(lp(ba[gi & 3]) + (gi >> 2) * 4096);
  SB();
#pragma unroll
  for (int gi = 0; gi < 28; ++gi) {
    const int t = gi >> 2, k = gi & 3;
#pragma unroll
    for (int e = 0; e < NE; ++e) {
      mfma_ip(acc[e][t], af[k][e], bf[gi & 3]);
      if (e == 0 && gi + LAG < 28) bf[(gi + LAG) & 3] = lds128(lp(ba[(gi + LAG) & 3]) + ((gi + LAG) >> 2) * 4096);
      if (e == NE - 1 && k == 0 && t > 0) {
        asm volatile("s_nop %0" :: "i"(8 - NE));
#pragma unroll
        for (int e2 = 0; e2 < NE; ++e2) PIN(acc[e2][t - 1]);
      }
      if (e == NE - 1 && t > 0) out(t - 1, k);
      SB();
    }
  }
  MFMA_DONE();
#pragma unroll
  for (int e2 = 0; e2 < NE; ++e2) PIN(acc[e2][6]);
  SB();
#pragma unroll
  for (int k = 0; k < 4; ++k) out(6, k);
  if (keep) prog_post(lds + KPROG, 1);
  else {
    const unsigned long long tag = *(const LAS unsigned long long*)(lds + TAGW);
    asm volatile("s_waitcnt vmcnt(0)" ::: "memory");
    if (lv == 0) __hip_atomic_store(c2.xflag(dst) + c2.g + (NE == 2 ? 8 : 0), tag, RLX_AGENT);
  }
  __builtin_amdgcn_s_waitcnt(0x0F70);
  PST();
}

DI void final_part(const int j, const Ctx& c0, lptr lds, const unsigned l0, char* out, int& nst) {
  const Ctx c = c0.fresh(); const int b = c.b, g = c.g; const rsrc_t rpx = c.rpx(g);
  const int lane = fresh_lane(), p = g >> 1, lg = lane >> 4, lr = lane & 15;
  const bool pstv = j == 0; (void)pstv;
  const unsigned long long tg = *(const LAS unsigned long long*)(lds + TAGW), tag = ((unsigned long long)rfl((unsigned)(tg >> 32)) << 32) | rfl((unsigned)tg);
  auto lo = [](unsigned v) { return __uint_as_float(v << 16); };
  auto hi = [](unsigned v) { return __uint_as_float(v & 0xffff0000u); };
  f32x4 a[2][5];
#pragma unroll
  for (int late = 0; late < 2; ++late) {
    {
      const int k = lane & 3;
      gu64* f = c.xflag(g) + (late ? 2 * k + 1 - (g & 1) + (lane & 4) * 2 : 2 * (k == p ? (p + 1) & 3 : k) + (g & 1));
      wait_flag(f, __hip_atomic_load(f, RLX_AGENT), tag);
    }
    u32x4 in[2][4][2]; u32x2 in2[2][4];
#pragma unroll
    for (int i = 0; i < 2; ++i)
#pragma unroll
      for (int k = 0; k < 3 + late; ++k) {
        const int src = late ? 2 * k + 1 - (g & 1) : 2 * (k + (k >= p)) + (g & 1);
        const unsigned o = (i == 1 && j == 3) ? OOB : src * 17920 + (j + 4 * i) * 2560;
        in[i][k][0] = __builtin_amdgcn_raw_buffer_load_b128(rpx, lane * 16 + o, 0, 16);
        in[i][k][1] = __builtin_amdgcn_raw_buffer_load_b128(rpx, lane * 16 + o + 1024, 0, 16);
        in2[i][k] = __builtin_amdgcn_raw_buffer_load_b64(rpx, lane * 8 + o + 2048, 0, 16);
      }
    if (!late) {
      prog_wait<0, 1>(l0 + KPROG, 1);
#pragma unroll
      for (int i = 0; i < 2; ++i)
#pragma unroll
        for (int e = 0; e < 5; ++e) a[i][e] = *(LAS f32x4*)(lds + OWNP + (((i == 1 && j == 3) ? 0 : j + 4 * i) * 5 + e) * 1024 + lane * 16);
    }
#pragma unroll
    for (int i = 0; i < 2; ++i)
#pragma unroll
      for (int k = 0; k < 3 + late; ++k) {
#pragma unroll
        for (int d = 0; d < 8; ++d) { const int reg = d & 3, e = 2 * (d >> 2); a[i][e][reg] += lo(in[i][k][d >> 2][d & 3]); a[i][e + 1][reg] += hi(in[i][k][d >> 2][d & 3]); }
#pragma unroll
        for (int d = 0; d < 2; ++d) { a[i][4][2 * d] += lo(in2[i][k][d]); a[i][4][2 * d + 1] += hi(in2[i][k][d]); }
      }
    PST();
  }
  if (j == 0 && g == 0 && b == 0 && lane == 0) __hip_atomic_store(c.epoch(), tag, RLX_AGENT);
  const rsrc_t rout = mkrsrc(out + (size_t)b * 143360, 143360);
#pragma unroll
  for (int i = 0; i < 2; ++i) {
    const unsigned ob = (i == 1 && j == 3) ? OOB : ((j + 4 * i) * 16 + lr) * 1280 + (g & 1) * 640;
#pragma unroll
    for (int hf = 0; hf < 2; ++hf)
      __builtin_amdgcn_raw_buffer_store_b128(u32x4{cvtpk(a[i][0][2 * hf], a[i][1][2 * hf]), cvtpk(a[i][2][2 * hf], a[i][3][2 * hf]), cvtpk(a[i][0][2 * hf + 1], a[i][1][2 * hf + 1]),
                                                   cvtpk(a[i][2][2 * hf + 1], a[i][3][2 * hf + 1])}, rout, ob + p * 128 + lg * 32 + hf * 16, 0, 0);
    __builtin_amdgcn_raw_buffer_store_b64(u32x2{cvtpk(a[i][4][0], a[i][4][1]), cvtpk(a[i][4][2], a[i][4][3])}, rout, ob + 512 + p * 32 + lg * 8, 0, 0);
  }
}

struct QpState { f32x4 acc[4][7]; u32x4 xf[7]; lptr xa0, xa1; };
DI void qp_init(QpState& q, lptr xb, int lane) {
  const int lg = lane >> 4, lr = lane & 15;
  q.xa0 = xb + lr * 128 + ((lg ^ (lr >> 1)) << 4); q.xa1 = xb + lr * 128 + (((4 + lg) ^ (lr >> 1)) << 4);
#pragma unroll
  for (int t = 0; t < 7; ++t) { q.xf[t] = u32x4{0u, 0u, 0u, 0u};
#pragma unroll
    for (int e = 0; e < 4; ++e) q.acc[e][t] = f32x4{0.f, 0.f, 0.f, 0.f}; }
}
template <bool TR> DI void qp_pack(const QpState& q, u32x4 (&qf)[7][2]) {
#pragma unroll
  for (int t = 0; t < 7; ++t)
#pragma unroll
    for (int hf = 0; hf < 2; ++hf) {
#pragma unroll
      for (int d = 0; d < 4; ++d) { const int i0 = 8 * hf + 2 * d, i1 = i0 + 1;
        qf[t][hf][d] = TR ? cvtpk(q.acc[i0 >> 2][t][i0 & 3] * QSCALE, q.acc[i1 >> 2][t][i1 & 3] * QSCALE) : cvtpk(q.acc[i0 & 3][t][i0 >> 2] * QSCALE, q.acc[i1 & 3][t][i1 >> 2] * QSCALE); }
      PIN(qf[t][hf]);
    }
}
template <int PAR>
DI void qp2_chunk(QpState& q, u32x4 (&af)[4][4], const int c, const int j, const int rot, unsigned& pc, const unsigned pa, const unsigned xvo, const unsigned lv, lptr lds, const unsigned l0,
                  rsrc_t rx, rsrc_t rqt) {
  if (QPACE) prog_wait<0, 2>(pa, (c * QPACE) >> 2);
  WAITV(8);
  ++pc; prog_post(lds + PPROG + j * 4, pc);
  const unsigned pg = prog_read<16>(pa);
  const int c1 = c + 1 + rot >= 10 ? c + 1 + rot - 10 : c + 1 + rot, c2 = c + 2 + rot >= 10 ? c + 2 + rot - 10 : c + 2 + rot;
  const unsigned s1 = c < 9 ? ((2 * c1 + 1) * 4 + j) * 4096 : OOB, s2 = c < 8 ? ((2 * c2) * 4 + j) * 4096 : OOB;
  SB();
#pragma unroll
  for (int u = 0; u < 14; ++u) {
    const int v = (u + 14 - LAG) % 14, t = v % 7, set = u < LAG ? 2 * (PAR ^ 1) + 1 : 2 * PAR + v / 7;
#pragma unroll
    for (int e = 0; e < 4; ++e) {
      mfma_ip(q.acc[e][t], af[set][e], q.xf[t]);
      if (e == 0 && u >= 2) q.xf[u % 7] = lds128((u >= 7 ? q.xa1 : q.xa0) + PAR * XCH + (u % 7) * 2048);
      if (e == 2 && ((u >= LAG && u < LAG + 4) || u >= 10)) PC();
      if (e == 2 && u >= LAG && u < LAG + 4) af[2 * (PAR ^ 1) + 1][u - LAG] = __builtin_amdgcn_raw_buffer_load_b128(rqt, lv + (u - LAG) * 1024, s1, 16);
      if (e == 2 && u >= 10) af[2 * PAR][u - 10] = __builtin_amdgcn_raw_buffer_load_b128(rqt, lv + (u - 10) * 1024, s2, 16);
      SB();
    }
    if (u == 0) { prog_wait<16, 0>(pa, pg, pc); q.xf[0] = lds128(q.xa0 + PAR * XCH); q.xf[1] = lds128(q.xa0 + PAR * XCH + 2048); SB(); }
    if (u == 1) {
      const unsigned xs = c < 9 ? j * 10240 + c1 * 128 : OOB, xd = l0 + (c < 9 ? RC + (PAR ^ 1) * XCH + j * 1024 : DUMMY), st = c < 9 ? 4096u : 0u;
#pragma unroll
      for (int i = 0; i < 3; ++i) { PC(); dma_df(rx, xvo, xs + i * 40960, xd + i * st); }
      PC(); dma_df(rx, xvo, xs + 3 * 40960, j < 2 ? xd + 3 * st : l0 + DUMMY);
      SB();
    }
  }
}

DI void proj_wave(const int j, const int lane, const Ctx& c0, const int rot, lptr lds, const unsigned l0, rsrc_t rx, int& nst) {
  unsigned long long tag, tv;
  const bool pstv = j == 0; (void)pstv;
  if (PPRIO) __builtin_amdgcn_s_setprio(PPRIO);
  const int lg = lane >> 4, lr = lane & 15;
  QpState q;
  u32x4 qf[7][2];
  const unsigned qo = (4 * (j & 1) + lg) * 512 + lr * 16;
  {
    qp_init(q, lds + XP, lane);
    u32x4 af[2][4];
#pragma unroll
    for (int e = 0; e < 4; ++e) af[1][e] = u32x4{0u, 0u, 0u, 0u};
    const int q4 = lr >> 2;
    const unsigned wa = l0 + (8 * lg + q4) * 512 + ((j * 8 + 2 * (lane & 3)) << 4 ^ (((q4 & 1) | ((q4 >> 1) << 3)) << 4));
    int buf = 2;
#pragma nounroll
    for (int c = 0; c < 10; ++c) {
      const unsigned wb0 = wa + buf * 2 * SLOT, wb1 = wb0 ^ 16; const int xo = buf * XCH;
      BAR_L();
#pragma unroll
      for (int u = 0; u < 14; ++u) {
        const int v = (u + 14 - LAG) % 14, t = v % 7, set = u < LAG ? 1 : v / 7;
        auto ard = [&](int ks, int e) { lptr p = lp((e >> 1) ? wb1 : wb0) + ks * 16384 + (e & 1) * 8; const u32x2 lo = ldstr(p), hi = ldstr(p + 2048); af[ks][e] = u32x4{lo[0], lo[1], hi[0], hi[1]}; };
        if (u == 0) { ard(0, 0); q.xf[0] = lds128(q.xa0 + xo); ard(0, 1); SB(); }
#pragma unroll
        for (int e = 0; e < 4; ++e) {
          mfma_ip(q.acc[e][t], af[set][e], q.xf[t]);
          if (e == 0 && u >= 1) q.xf[u % 7] = lds128((u >= 7 ? q.xa1 : q.xa0) + xo + (u % 7) * 2048);
          if (u == 0 && (e & 1)) ard(0, 2 + (e >> 1));
          if (e == 2 && u >= 4 && u < 8) ard(1, u - 4);
          SB();
        }
      }
      buf = buf == 2 ? 0 : buf + 1;
      PST();
    }
#pragma unroll
    for (int t = 7 - LAG; t < 7; ++t)
#pragma unroll
      for (int e = 0; e < 4; ++e) mfma_ip(q.acc[e][t], af[1][e], q.xf[t]);
    MFMA_DONE(); SB();
    BAR_L();
    qp_pack<true>(q, qf);
    lptr qp = lds + ((j >> 1) ? RB : RA) + qo;
#pragma unroll
    for (int t = 0; t < 7; ++t)
#pragma unroll
      for (int hf = 0; hf < 2; ++hf) *(LAS u32x4*)(qp + t * 4096 + hf * 256) = qf[t][hf];
    { const unsigned long long tg = *(const LAS unsigned long long*)(lds + TAGW); tag = ((unsigned long long)rfl((unsigned)(tg >> 32)) << 32) | rfl((unsigned)tg); }
    tv = __hip_atomic_load(c0.fresh().tflags() + (lane & 31), RLX_AGENT);
    BAR_L();
    PST();
  }
  unsigned pc = 0;
  {
    const Ctx c = c0.fresh();
    const rsrc_t rqt = c.rqt();
    const unsigned pa = l0 + APROG + (lane & 3) * 4, lv = lane * 16;
    const unsigned xvo = (lane >> 3) * 1280 + (((lane & 7) ^ ((4 * (j & 1) + lg) & 7)) << 4);
#pragma unroll
    for (int i = 0; i < 3; ++i) dma_df(rx, xvo, j * 10240 + rot * 128 + i * 40960, l0 + RC + j * 1024 + i * 4096);
    dma_df(rx, xvo, j * 10240 + rot * 128 + 3 * 40960, j < 2 ? l0 + RC + j * 1024 + 3 * 4096 : l0 + DUMMY);
    wait_flag(c.tflags() + (lane & 31), tv, tag);
    u32x4 af[4][4];
    const int r1 = rot == 9 ? 0 : rot + 1;
#pragma unroll
    for (int k = 0; k < 3; ++k)
#pragma unroll
      for (int e = 0; e < 4; ++e) { af[k][e] = __builtin_amdgcn_raw_buffer_load_b128(rqt, lv + e * 1024, ((k < 2 ? 2 * rot + k : 2 * r1) * 4 + j) * 4096, 16); SB(); }
#pragma unroll
    for (int e = 0; e < 4; ++e) af[3][e] = u32x4{0u, 0u, 0u, 0u};
    qp_init(q, lds + RC, lane);
    asm volatile("s_nop 1"); SB();
    PST();
#pragma nounroll
    for (int cc = 0; cc < 10; cc += 2) {
      qp2_chunk<0>(q, af, cc, j, rot, pc, pa, xvo, lv, lds, l0, rx, rqt);
      qp2_chunk<1>(q, af, cc + 1, j, rot, pc, pa, xvo, lv, lds, l0, rx, rqt);
      PST();
    }
#pragma unroll
    for (int t = 7 - LAG; t < 7; ++t)
#pragma unroll
      for (int e = 0; e < 4; ++e) mfma_ip(q.acc[e][t], af[3][e], q.xf[t]);
    MFMA_DONE(); SB();
    parrive(pc, j, lds);
    __builtin_amdgcn_s_setprio(XPRIO);
    qp_pack<false>(q, qf);
    prog_wait<16, 0>(pa, pc);
    prog_wait<32, 0>(pa, 1);
#pragma unroll
    for (int t = 0; t < 7; ++t)
#pragma unroll
      for (int hf = 0; hf < 2; ++hf) *(LAS u32x4*)(lds + ((j >> 1) ? RC : RA) + qo + t * 4096 + hf * 256) = qf[t][hf];
    parrive(pc, j, lds);
    PST();
  }
  __builtin_amdgcn_s_waitcnt(0x0F70);
}

__global__ __launch_bounds__(512, 2) void attn_fused(const char* __restrict__ x, const char* __restrict__ K, const char* __restrict__ V,
                                                     const char* __restrict__ wq, const char* __restrict__ wo, char* __restrict__ ws, char* __restrict__ out) {
  extern __shared__ __attribute__((aligned(1024))) char smem[];
  const int w = rfl(threadIdx.x >> 6);
  unsigned long long pc0 = 0;
  if (CODEPF) asm volatile("s_getpc_b64 %0" : "=s"(pc0));
  const int bid = blockIdx.x;
  const int b = ((bid >> 1) & 3) * 8 + (bid >> 5), g = (bid & 1) * 4 + ((bid >> 3) & 3);
  const int rot = ROT ? (((b & 7) * 5) / 4 + ((g & 3) * 5) / 2) % 10 : 0;
  lptr lds = (lptr)smem;
  const unsigned l0 = (unsigned)(uintptr_t)lds;
  const Ctx c = {ws, b, g};
  const rsrc_t rx = mkrsrc(x + (size_t)b * 143360, 143360), rwq = mkrsrc(wq, 640 * 8192);
  int nst = 0; (void)nst;
  if (w < 4) {
    const size_t kvb = ((size_t)b * 32 + g * 4) * 131072;
    attn_wave(w, fresh_lane(), rot, lds, l0, mkrsrc(K + kvb, 524288), mkrsrc(V + kvb, 524288), rx, rwq, wo, c, nst);
    oproj_pass<3>(w, false, c, lds, l0, nst);
    ST(); ST();
  } else {
    if (CODEPF && w < 6) dma_df(mkrsrc((const void*)(pc0 & ~255ull), CODEPF), fresh_lane() * 16, ((blockIdx.x >> 3) * 2 + (w - 4)) * 1024, l0 + DUMMY + 1024);
    proj_wave(w - 4, fresh_lane(), c, rot, lds, l0, rx, nst);
    oproj_pass<5>(w - 4, w == 4, c, lds, l0, nst);
    oproj_pass<2>(w - 4, false, c, lds, l0, nst);
    final_part(w - 4, c, lds, l0, out, nst);
    __builtin_amdgcn_s_waitcnt(0x0F70);
  }
}

extern "C" void kernel_launch(void* const* d_in, const int* in_sizes, int n_in, void* d_out, int out_size, void* d_ws, size_t ws_size, hipStream_t stream) {
  (void)hipFuncSetAttribute((const void*)attn_fused, hipFuncAttributeMaxDynamicSharedMemorySize, LDS_BYTES);
  attn_fused<<<256, 512, LDS_BYTES, stream>>>((const char*)d_in[0], (const char*)d_in[1], (const char*)d_in[2], (const char*)d_in[3], (const char*)d_in[4], (char*)d_ws + WSOFF, (char*)d_out);
}
```

```cpp
#include <hip/hip_runtime.h>
#include <stdint.h>
typedef __attribute__((ext_vector_type(8))) __bf16 bf16x8;
typedef __attribute__((ext_vector_type(2))) __bf16 bf16x2;
typedef __attribute__((ext_vector_type(4))) float f32x4;
typedef __attribute__((ext_vector_type(16))) float f32x16;
typedef __attribute__((ext_vector_type(2))) float f32x2;
typedef __attribute__((ext_vector_type(4))) unsigned u32x4;
typedef __attribute__((ext_vector_type(2))) unsigned u32x2;
typedef __attribute__((ext_vector_type(4))) short s16x4;
#define LAS __attribute__((address_space(3)))
#define DI __device__ __forceinline__
typedef LAS char* lptr;
typedef __amdgpu_buffer_rsrc_t rsrc_t;
typedef __attribute__((address_space(1))) unsigned long long gu64;
#define RLX_AGENT __ATOMIC_RELAXED, __HIP_MEMORY_SCOPE_AGENT

constexpr int SLOT = 16384, VOFF = 2 * SLOT, XCH = 14336, RA = 4 * SLOT, RB = RA + 2 * XCH, RC = RB + 2 * XCH, PBUF = SLOT + XCH, DUMMY = VOFF + 4 * PBUF,
              TAGW = DUMMY + 4096, CNT = TAGW + 16, APROG = CNT + 32, PPROG = APROG + 16, QPROG = PPROG + 16, KPROG = QPROG + 16, STMP = CNT + 128, ARR = STMP + 512, LDS_BYTES = ARR + 1024;
constexpr float QSCALE = 0.08838834764831845f * 1.4426950408889634f;
constexpr unsigned OOB = 0x40000000u;
constexpr size_t WS_TF = 8192, WS_OF = 16384, WS_XF = WS_OF + 131072, WS_O = 1u << 20, WS_Q = 40u << 20, WS_WT = 48u << 20, WS_QT = 56u << 20, WS_PX = 64u << 20, WS_ST = 128u << 20;
constexpr int OWN3 = 0, OWNP = 2 * XCH;
constexpr unsigned P_Q1 = 12, P_Q23 = 24;

DI unsigned rfl(unsigned x) { return __builtin_amdgcn_readfirstlane(x); }
DI unsigned cvtpk(float lo, float hi) { f32x2 v = {lo, hi}; bf16x2 b = __builtin_convertvector(v, bf16x2); return __builtin_bit_cast(unsigned, b); }
DI f32x4 mfma(u32x4 a, u32x4 b, f32x4 c) { return __builtin_amdgcn_mfma_f32_16x16x32_bf16(__builtin_bit_cast(bf16x8, a), __builtin_bit_cast(bf16x8, b), c, 0, 0, 0); }
DI void mfma_ip(f32x4& c, const u32x4& a, const u32x4& b) { asm volatile("v_mfma_f32_16x16x32_bf16 %0, %1, %2, %0" : "+v"(c) : "v"(a), "v"(b)); }
#define MFMA_DONE() asm volatile("s_nop 15")
DI f32x16 mfma32(u32x4 a, u32x4 b, f32x16 c) { return __builtin_amdgcn_mfma_f32_32x32x16_bf16(__builtin_bit_cast(bf16x8, a), __builtin_bit_cast(bf16x8, b), c, 0, 0, 0); }
DI u32x4 lds128(lptr p) { return *(const LAS u32x4*)p; }
DI int fresh_lane() { int l; asm volatile("v_mbcnt_lo_u32_b32 %0, -1, 0\n\tv_mbcnt_hi_u32_b32 %0, -1, %0" : "=v"(l)); return l; }
DI lptr lp(unsigned a) { return (lptr)(uintptr_t)a; }
DI u32x2 ldstr(lptr p) { return __builtin_bit_cast(u32x2, __builtin_amdgcn_ds_read_tr16_b64_v4i16((LAS s16x4*)p)); }
DI rsrc_t mkrsrc(const void* p, unsigned bytes) { return __builtin_amdgcn_make_buffer_rsrc((void*)p, 0, bytes, 0x00020000); }
DI float xmax(float m) {
  auto a = __builtin_amdgcn_permlane16_swap(__float_as_uint(m), __float_as_uint(m), false, false); m = fmaxf(__uint_as_float(a[0]), __uint_as_float(a[1]));
  auto b = __builtin_amdgcn_permlane32_swap(__float_as_uint(m), __float_as_uint(m), false, false); return fmaxf(__uint_as_float(b[0]), __uint_as_float(b[1]));
}
DI float xsum(float m) {
  auto a = __builtin_amdgcn_permlane16_swap(__float_as_uint(m), __float_as_uint(m), false, false); m = __uint_as_float(a[0]) + __uint_as_float(a[1]);
  auto b = __builtin_amdgcn_permlane32_swap(__float_as_uint(m), __float_as_uint(m), false, false); return __uint_as_float(b[0]) + __uint_as_float(b[1]);
}
#define DFPOL ""
#define TAUX 0
#define KVPOL "nt"
#define VPOL ""
template <int OFF, bool V = false> DI void dma_nt(rsrc_t r, unsigned voff, unsigned soff, unsigned ldsaddr) {
  if (V) asm volatile("s_mov_b32 m0, %3\n\ts_nop 0\n\tbuffer_load_dwordx4 %0, %1, %2 offen offset:%4 " VPOL " lds" :: "v"(voff), "s"(r), "s"(soff), "s"(ldsaddr), "i"(OFF) : "memory");
  else asm volatile("s_mov_b32 m0, %3\n\ts_nop 0\n\tbuffer_load_dwordx4 %0, %1, %2 offen offset:%4 " KVPOL " lds" :: "v"(voff), "s"(r), "s"(soff), "s"(ldsaddr), "i"(OFF) : "memory");
}
DI void dma_df(rsrc_t r, unsigned voff, unsigned soff, unsigned ldsaddr) {
  asm volatile("s_mov_b32 m0, %3\n\ts_nop 0\n\tbuffer_load_dwordx4 %0, %1, %2 offen " DFPOL "lds" :: "v"(voff), "s"(r), "s"(soff), "s"(ldsaddr) : "memory");
}
DI void dma_sc1(rsrc_t r, unsigned voff, unsigned soff, unsigned ldsaddr) {
  asm volatile("s_mov_b32 m0, %3\n\ts_nop 0\n\tbuffer_load_dwordx4 %0, %1, %2 offen sc1 lds" :: "v"(voff), "s"(r), "s"(soff), "s"(ldsaddr) : "memory");
}
DI void touch(rsrc_t r, unsigned voff, unsigned ldsaddr) {
  asm volatile("s_mov_b32 m0, %2\n\ts_nop 0\n\tbuffer_load_dword %0, %1, 0 offen lds" :: "v"(voff), "s"(r), "s"(ldsaddr) : "memory");
}
#define SB() __builtin_amdgcn_sched_barrier(0)
#define WAITV(N) do { SB(); asm volatile("s_waitcnt vmcnt(" #N ") lgkmcnt(0)" ::: "memory"); SB(); } while (0)
#define WAITVM(N) do { SB(); asm volatile("s_waitcnt vmcnt(" #N ")" ::: "memory"); SB(); } while (0)
#define WAITL() do { SB(); asm volatile("s_waitcnt lgkmcnt(0)" ::: "memory"); SB(); } while (0)
#define BAR_L() do { SB(); asm volatile("s_waitcnt lgkmcnt(0)\n\ts_barrier" ::: "memory"); SB(); } while (0)
#define ST() do {} while (0)
#define PST() do {} while (0)
#define PST2() do {} while (0)
#define PCAP 0
#define PC() do { if (PCAP) asm volatile("s_waitcnt vmcnt(%0)" :: "i"(PCAP - 1) : "memory"); } while (0)
#define PIN(x) asm volatile("" : "+v"(x))
#define NODMA 0
#define DPOS 2
#define DSTR 8
#define CAP 16
#define SAUX 16
#define PPRIO 0
#define ABL 0
#define TE0 2
#define XPRIO 0
#define PF 0
#define VP0 2
#define VST 4
#define KP0 18
#define KST 4
#define QPACE 10
#define OPACE 0
#define OP0 26
#define MAP 4
#define OSA 16
#define WSOFF 0
#define CODEPF 0
#define NH 2
#define SSL 0
#define DSL 0
#define LPRIO 3
#define ILV 0
#define TC 1
#define TN0 0, 9, 8, 8, 0, 4, 0, 5, 0, 0
#define LCAP 0
#define RPACE 0
#define WARM 0
#define ROT 1
#define QLD0 6
#define PFD 16
constexpr float PLIM = 1024.f;

template <int OFF> DI unsigned prog_read(unsigned pa) { return *(volatile LAS unsigned*)(lp(pa) + OFF); }
template <int OFF, int SLEEP> DI void prog_wait(unsigned pa, unsigned v, unsigned t) {
  unsigned n;
  asm volatile("s_mov_b32 %1, 0x1000000\n"
               "1:\n\tv_cmp_gt_u32 vcc, %3, %0\n\ts_cbranch_vccz 2f\n\ts_sleep %5\n\tds_read_b32 %0, %2 offset:%4\n\ts_sub_u32 %1, %1, 1\n\ts_waitcnt lgkmcnt(0)\n\ts_cbranch_scc0 1b\n2:"
               : "+v"(v), "=&s"(n) : "v"(pa), "s"(t), "i"(OFF), "i"(SLEEP) : "memory", "vcc", "scc");
}
template <int OFF, int SLEEP> DI void prog_wait(unsigned pa, unsigned t) { prog_wait<OFF, SLEEP>(pa, prog_read<OFF>(pa), t); }
DI void prog_post(lptr p, unsigned v) { asm volatile("" ::: "memory"); *(volatile LAS unsigned*)p = v; }

DI void publish(gu64* flag, lptr cnt, unsigned long long tag, int lane) {
  asm volatile("s_waitcnt vmcnt(0)" ::: "memory");
  if (lane == 0) {
    const unsigned old = __hip_atomic_fetch_add((LAS unsigned*)cnt, 1u, __ATOMIC_RELAXED, __HIP_MEMORY_SCOPE_WORKGROUP);
    asm volatile("" : "+s"(tag));
    if (old == 3u) __hip_atomic_store(flag, tag, RLX_AGENT);
  }
}
DI void wait_flag(gu64* f, unsigned long long v, unsigned long long tag) {
#pragma nounroll
  for (unsigned spins = 0; spins < (1u << 15); ++spins) {
    if (__all(v == tag)) break;
    __builtin_amdgcn_s_sleep(2);
    v = __hip_atomic_load(f, RLX_AGENT);
  }
  __builtin_amdgcn_s_waitcnt(0x0F70);
  __builtin_amdgcn_fence(__ATOMIC_ACQUIRE, "wavefront");
  asm volatile("" ::: "memory");
}

struct Ctx {
  char* ws; int b, g;
  DI Ctx fresh() const { Ctx c = *this; asm volatile("" : "+s"(c.b), "+s"(c.g)); return c; }
  DI gu64* epoch() const { return (gu64*)ws; }
  DI gu64* tflags() const { return (gu64*)(ws + WS_TF + g * 256); }
  DI gu64* xflag(int dst) const { return (gu64*)(ws + WS_XF + (b * 8 + dst) * 128); }
  DI rsrc_t rpx(int dst) const { return mkrsrc(ws + WS_PX + (size_t)(b * 8 + dst) * 143360, 143360); }
  DI rsrc_t rwt() const { return mkrsrc(ws + WS_WT + (size_t)g * 655360, 655360); }
  DI rsrc_t rqt() const { return mkrsrc(ws + WS_QT + (size_t)g * 491520, 491520); }
};
template <int NQ> struct AttnState {
  u32x4 q[NQ][4], fr[8], p[2][NQ];
  f32x4 o[8][NQ], negm[NQ], sc[2][4][NQ];
  float l[NQ], rs[NQ], inv[NQ];
  unsigned ka[4], va[8], kvo, vvo, pa, pb, pg, oad, last;
};

DI int qreg(int r) { return r == 3 ? RC : ((r & 1) ? RB : RA); }
template <int NQ> DI void sm_pair(AttnState<NQ>& a, const f32x4 (&x)[4][NQ], u32x4 (&p)[NQ], const int hf, const int n) {
  const int t = n >> 2, d = n & 3;
  if (t >= NQ) return;
  const float e0 = __builtin_amdgcn_exp2f(x[2 * hf + (d >> 1)][t][2 * (d & 1)]), e1 = __builtin_amdgcn_exp2f(x[2 * hf + (d >> 1)][t][2 * (d & 1) + 1]);
  a.rs[t] = d ? (a.rs[t] + e0) + e1 : e0 + e1;
  p[t][d] = cvtpk(e0, e1);
  PIN(p[t][d]); PIN(a.rs[t]);
}
template <int NQ, int HF, bool ADJ>
DI void fixup(AttnState<NQ>& a, f32x4 (&x)[4][NQ], f32x4 (&y)[4][NQ]) {
#pragma unroll
  for (int t = 0; t < NQ; ++t) {
    float mx = x[3][t][3];
#pragma unroll
    for (int i = 8 * HF; i < 15; ++i) mx = fmaxf(mx, x[i >> 2][t][i & 3]);
    const float dl = __builtin_ceilf(fmaxf(xmax(mx), 0.f)), sc = __builtin_amdgcn_exp2f(-dl);
    a.l[t] *= sc; a.negm[t] -= dl;
#pragma unroll
    for (int dt = 0; dt < 8; ++dt) a.o[dt][t] *= sc;
    if (HF) {
#pragma unroll
      for (int d = 0; d < 4; ++d) { const unsigned v = a.p[0][t][d]; a.p[0][t][d] = cvtpk(__uint_as_float(v << 16) * sc, __uint_as_float(v & 0xffff0000u) * sc); }
    }
#pragma unroll
    for (int kt = 2 * HF; kt < 4; ++kt) x[kt][t] -= dl;
    float r = 0.f;
#pragma unroll
    for (int d = 0; d < 4; ++d) { const float e0 = __builtin_amdgcn_exp2f(x[2 * HF + (d >> 1)][t][2 * (d & 1)]), e1 = __builtin_amdgcn_exp2f(x[2 * HF + (d >> 1)][t][2 * (d & 1) + 1]);
      r += e0 + e1; a.p[HF][t][d] = cvtpk(e0, e1); }
    a.rs[t] = r;
    if (ADJ) {
#pragma unroll
      for (int kt = 0; kt < 4; ++kt) y[kt][t] -= dl;
    }
  }
}
template <int NQ, int HF, bool CHECK, bool ADJ>
DI void sm_close(AttnState<NQ>& a, f32x4 (&x)[4][NQ], f32x4 (&y)[4][NQ]) {
  if (CHECK) {
    if (NQ == 1) PIN(a.p[HF][0]);
    float m = NQ == 2 ? a.rs[0] + a.rs[NQ - 1] : a.rs[0]; PIN(m);
    if (__builtin_expect(__any(!(m <= PLIM)), 0)) fixup<NQ, HF, ADJ>(a, x, y);
  }
#pragma unroll
  for (int t = 0; t < NQ; ++t) { PIN(a.rs[t]); a.l[t] += a.rs[t]; PIN(a.l[t]); }
}
template <int NQ> DI u32x4 o_frag(const AttnState<NQ>& a, const int m, const int t) {
  const f32x4 x = a.o[2 * m][t] * a.inv[t], y = a.o[2 * m + 1][t] * a.inv[t];
  return u32x4{cvtpk(x[0], x[1]), cvtpk(x[2], x[3]), cvtpk(y[0], y[1]), cvtpk(y[2], y[3])};
}
template <bool V, int NQ> DI void frag_read(AttnState<NQ>& a, const int m, const int par) {
  if (!V) a.fr[m & 7] = lds128(lp(a.ka[m & 3]) + par * SLOT + (m >> 2) * 4096);
  else { lptr p = lp(a.va[m & 7]) + par * SLOT + (m >> 3) * 8192; const u32x2 v0 = ldstr(p), v1 = ldstr(p + 4096); a.fr[m & 7] = u32x4{v0[0], v0[1], v1[0], v1[1]}; }
}
template <bool V, int NQ> DI void tile_issue(const AttnState<NQ>& a, rsrc_t r, const unsigned gsrc, const unsigned dst, const unsigned st = 4096) {
  if (NODMA) return;
#pragma unroll
  for (int i = 0; i < 4; ++i) {
    if (LCAP) asm volatile("s_waitcnt vmcnt(%0)" :: "i"(LCAP - 4) : "memory");
    dma_nt<0, V>(r, V ? a.vvo : a.kvo, gsrc + i * 4096, dst + i * st);          dma_nt<1024, V>(r, V ? a.vvo ^ 128 : a.kvo ^ 64, gsrc + i * 4096, dst + i * st);
    dma_nt<2048, V>(r, V ? a.vvo : a.kvo ^ 128, gsrc + i * 4096, dst + i * st); dma_nt<3072, V>(r, V ? a.vvo ^ 128 : a.kvo ^ 192, gsrc + i * 4096, dst + i * st);
  }
}
template <bool CV, int NQ> DI void phase_io(AttnState<NQ>& a, const int i, const int cp, const int np, const unsigned val, const int w, lptr lds) {
  if (i <= 10) frag_read<CV>(a, i + 5, cp);
  if (NQ == 2 && i == 10) {
    SB();
    prog_post(lds + APROG + w * 4, val);
    a.pg = prog_read<0>(a.pb);
  }
  if (NQ == 2 && i == 12) prog_wait<0, 0>(a.pb, a.pg, val);
  if (i == 13 || i == 14) { frag_read<!CV>(a, 2 * (i - 13), np); frag_read<!CV>(a, 2 * (i - 13) + 1, np); }
  if (i == 15) frag_read<!CV>(a, 4, np);
}
template <bool V, int NQ> DI void loader_turn(AttnState<NQ>& a, const unsigned val, lptr lds, rsrc_t r, const unsigned gsrc, const unsigned dst) {
  if (NQ == 1) {
    WAITVM(16);
    prog_post(lds + APROG + 12, val + 1);
    prog_wait<0, 1>(a.pb, val);
    if (RPACE) {
      asm volatile("1:\n\ts_memrealtime vcc\n\ts_waitcnt lgkmcnt(0)\n\ts_sub_u32 vcc_hi, vcc_lo, %0\n\ts_cmp_lt_u32 vcc_hi, %1\n\ts_cbranch_scc1 1b\n\ts_mov_b32 %0, vcc_lo"
                   : "+s"(a.last) : "i"(RPACE) : "memory", "scc", "vcc");
    }
    tile_issue<V>(a, r, gsrc, gsrc < 32 * SLOT ? dst : (unsigned)(uintptr_t)lds + DUMMY, gsrc < 32 * SLOT ? 4096u : 0u);
    SB();
  }
}
template <int KIND, int PAR, int NQ>
DI void attn_step(AttnState<NQ>& a, const int s, const int w, const int lane, lptr lds, const unsigned l0, rsrc_t rk, rsrc_t rv) {
  const f32x4 zero = {0.f, 0.f, 0.f, 0.f};
  f32x4 (&c)[4][NQ] = a.sc[PAR]; f32x4 (&d)[4][NQ] = a.sc[PAR ^ 1];
  SB();
  if (KIND == 1) {
    prog_wait<16, 1>(a.pa, s == 0 ? 0u : (s == 8 ? P_Q1 : P_Q23));
    const unsigned qa = l0 + qreg(s >> 3) + 2 * w * 4096 + lane * 16;
#pragma unroll
    for (int i = 0; i < 4; ++i)
#pragma unroll
      for (int t = 0; t < NQ; ++t) a.q[t][i] = lds128(lp(qa) + t * 4096 + i * 1024);
    a.oad = qa;
    SB();
  }
#pragma unroll
  for (int i = 0; i < 16; ++i) {
    constexpr int PA[16] = {-1, 0, 4, -1, 1, 5, -1, 2, 6, -1, -1, 3, 7, -1, -1, -1};
#pragma unroll
    for (int t = 0; t < NQ; ++t) {
      c[i >> 2][t] = mfma(a.fr[i & 7], a.q[t][i & 3], (i & 3) ? c[i >> 2][t] : (KIND == 1 ? zero : a.negm[t]));
      if (t == 0) { if (PA[i] >= 0) sm_pair(a, d, a.p[1], 1, PA[i]); SB(); }
    }
    if (i == 12) PIN(c[3][NQ - 1]);
    phase_io<false>(a, i, PAR, PAR ^ 1, 2 * s + 1, w, lds);
    if (KIND == 2 && i >= 8 && ((i - 8) >> 2) < NQ) *(LAS u32x4*)(lp(a.oad) + ((i - 8) >> 2) * 4096 + (i & 3) * 1024) = o_frag(a, i & 3, ((i - 8) >> 2) % NQ);
    SB();
  }
  if (KIND == 2) prog_post(lds + QPROG + w * 4, s);
  loader_turn<false>(a, 2 * s + 1, lds, rk, (s + 2) * SLOT, l0 + PAR * SLOT);
  sm_close<NQ, 1, KIND != 2, KIND == 0>(a, d, c);
  if (KIND == 1) {
#pragma unroll
    for (int t = 0; t < NQ; ++t) { a.inv[t] = __builtin_amdgcn_rcpf(xsum(a.l[t])); a.l[t] = 0.f; }
  }
  SB();
  u32x4 pn[NQ];
#pragma unroll
  for (int i = 0; i < 16; ++i) {
    constexpr int PB[16] = {-1, -1, -1, 0, -1, 4, -1, 1, -1, 5, -1, 2, 6, -1, 3, 7};
#pragma unroll
    for (int t = 0; t < NQ; ++t) {
      a.o[i & 7][t] = mfma(a.fr[i & 7], a.p[i >> 3][t], (KIND == 2 && i < 8) ? zero : a.o[i & 7][t]);
      if (t == 0) { if (PB[i] >= 0) sm_pair(a, c, pn, 0, PB[i]); SB(); }
    }
    if (i == 12) PIN(a.o[4][NQ - 1]);
    phase_io<true>(a, i, PAR ^ 1, PAR ^ 1, 2 * s + 2, w, lds);
    if (KIND == 1 && (i == 1 || i == 2) && i - 1 < NQ) {
      const int t = (i - 1) % NQ;
      float m0 = c[3][t][3];
#pragma unroll
      for (int e = 0; e < 15; ++e) m0 = fmaxf(m0, c[e >> 2][t][e & 3]);
      m0 = xmax(m0);
#pragma unroll
      for (int kt = 0; kt < 4; ++kt) { c[kt][t] -= m0; PIN(c[kt][t]); }
      a.negm[t] = f32x4{-m0, -m0, -m0, -m0}; PIN(a.negm[t]);
    }
    SB();
  }
#pragma unroll
  for (int t = 0; t < NQ; ++t) a.p[0][t] = pn[t];
  loader_turn<true>(a, 2 * s + 2, lds, rv, (s + 1) * SLOT, l0 + VOFF + (PAR ^ 1) * SLOT);
  sm_close<NQ, 0, KIND != 1, false>(a, c, c);
}

template <int NQ>
DI void attn_main(const int w, lptr lds, const unsigned l0, rsrc_t rk, rsrc_t rv, const Ctx& c0, int& nst) {
  const int lane = fresh_lane(), lg = lane >> 4, lr = lane & 15;
  AttnState<NQ> a;
  a.kvo = lg * 256 + ((lr ^ lg) << 4);
  a.vvo = lg * 256 + ((lr ^ (lg << 1)) << 4);
  if (NQ == 1) { tile_issue<false>(a, rk, 0, l0); }
  BAR_L();
  if (NQ == 1) { tile_issue<false>(a, rk, SLOT, l0 + SLOT); }
  BAR_L();
  if (NQ == 1) { tile_issue<true>(a, rv, 0, l0 + VOFF); prog_post(lds + APROG + 12, 1); __builtin_amdgcn_s_setprio(LPRIO); }
#pragma unroll
  for (int i = 0; i < 4; ++i) { a.ka[i] = l0 + lr * 256 + (((4 * i + lg) ^ lr) << 4); PIN(a.ka[i]); }
#pragma unroll
  for (int i = 0; i < 8; ++i) { a.va[i] = l0 + VOFF + (lane >> 2) * 256 + ((i ^ ((lane >> 2) & 7)) << 5) + (lane & 3) * 8; PIN(a.va[i]); }
  a.pg = 0; a.pa = l0 + APROG + (lane & 3) * 4; a.pb = l0 + APROG + (NQ == 2 ? 12 : ((lane & 3) == 3 ? 0 : (lane & 3) * 4)); a.oad = 0; a.last = RPACE ? (unsigned)__builtin_amdgcn_s_memrealtime() - 1000u : 0u;
#pragma unroll
  for (int t = 0; t < NQ; ++t) {
    a.l[t] = 0.f; a.inv[t] = 0.f; a.rs[t] = 0.f; a.p[0][t] = a.p[1][t] = u32x4{0u, 0u, 0u, 0u}; a.negm[t] = f32x4{0.f, 0.f, 0.f, 0.f};
#pragma unroll
    for (int i = 0; i < 8; ++i) { a.o[i][t] = f32x4{0.f, 0.f, 0.f, 0.f}; a.sc[1][i & 3][t] = f32x4{0.f, 0.f, 0.f, 0.f}; }
  }
  WAITVM(32);
  BAR_L(); ST();
#pragma unroll
  for (int m = 0; m < 5; ++m) frag_read<false>(a, m, 0);
#pragma nounroll
  for (int r = 0; r < 4; ++r) {
    attn_step<1, 0>(a, 8 * r, w, lane, lds, l0, rk, rv); ST();
    attn_step<2, 1>(a, 8 * r + 1, w, lane, lds, l0, rk, rv); ST();
#pragma nounroll
    for (int t = 2; t < 8; t += 2) {
      attn_step<0, 0>(a, 8 * r + t, w, lane, lds, l0, rk, rv); ST();
      attn_step<0, 1>(a, 8 * r + t + 1, w, lane, lds, l0, rk, rv); ST();
    }
  }
  {
#pragma unroll
    for (int i = 0; i < 8; ++i) sm_pair(a, a.sc[1], a.p[1], 1, i);
    if (NQ == 2) prog_wait<0, 0>(a.pb, 65);
#pragma unroll
    for (int m = 0; m < 8; ++m) frag_read<true>(a, m, 1);
    sm_close<NQ, 1, true, false>(a, a.sc[1], a.sc[1]);
    SB();
#pragma unroll
    for (int i = 0; i < 16; ++i) {
#pragma unroll
      for (int t = 0; t < NQ; ++t) a.o[i & 7][t] = mfma(a.fr[i & 7], a.p[i >> 3][t], a.o[i & 7][t]);
      if (i < 8) frag_read<true>(a, i + 8, 1);
      SB();
    }
#pragma unroll
    for (int t = 0; t < NQ; ++t) a.inv[t] = __builtin_amdgcn_rcpf(xsum(a.l[t]));
  }
#pragma unroll
  for (int t = 0; t < NQ; ++t)
#pragma unroll
    for (int m = 0; m < 4; ++m) *(LAS u32x4*)(lds + OWN3 + (2 * w + t) * 4096 + m * 1024 + lane * 16) = o_frag(a, m, t);
  prog_post(lds + QPROG + w * 4, 33);
  __builtin_amdgcn_s_setprio(0);
  ST();
}

DI void attn_wave(const int w, const int lane, const int rot, lptr lds, const unsigned l0, rsrc_t rk, rsrc_t rv, rsrc_t rx, rsrc_t rwq, const char* wo, const Ctx& c0, int& nst) {
  const int lg = lane >> 4, lr = lane & 15;
  ST();
  {
    const unsigned wvo = lg * 8192 + ((lr ^ (lg << 2)) << 4) + c0.g * 1024;
    const unsigned xvo = (lane >> 3) * 1280 + (((lane & 7) ^ ((4 * (w & 1) + lg) & 7)) << 4);
    auto issue = [&](int c) {
      const int cr = c + rot >= 10 ? c + rot - 10 : c + rot;
      const unsigned wd = l0 + VOFF + (c & 3) * PBUF + w * 1024, xd = wd + SLOT, xs = w * 10240 + cr * 128, ws_ = (cr * 64 + 4 * w) * 8192;
#pragma unroll
      for (int i = 0; i < 3; ++i) dma_df(rx, xvo, xs + i * 40960, xd + i * 4096);
      dma_df(rx, xvo, xs + 3 * 40960, w < 2 ? xd + 3 * 4096 : l0 + DUMMY);
#pragma unroll
      for (int i = 0; i < 4; ++i) dma_df(rwq, wvo, ws_ + i * 131072, wd + i * 4096);
    };
    issue(0); issue(1); issue(2);
    if (w == 0) { *(LAS u32x4*)(lds + CNT + (lane & 7) * 16) = u32x4{0u, 0u, 0u, 0u}; }
    const int b = c0.b, g = c0.g; const rsrc_t rwo = mkrsrc(wo, 4096 * 1280);
    u32x2 rw[8], rq2[8]; unsigned short r5[8]; unsigned long long e1;
    const unsigned rb = 4 * lg * 1280 + (b >> 4) * 640, wv0 = rb + w * 128 + lr * 8, wv1 = rb + 512 + w * 32 + lr * 2;
    const unsigned s2 = ((4 * g + ((b >> 2) & 3)) * 128 + (b & 3) * 32) * 1280;
    const int ti = (b - 20) * 4 + w, tk = b < 20 ? b : ti >> 1, tu = b < 20 ? w + 2 : ti & 1;
    const unsigned qv = lg * 8 * 8192 + lr * 8 + g * 1024 + 256, sq = tk * 32 * 8192 + tu * 128;
    constexpr int TN[13] = {0, 0, 0, TN0};
#pragma unroll
    for (int c = 0; c < 9; ++c) {
      SB(); asm volatile("s_waitcnt vmcnt(%0) lgkmcnt(0)\n\ts_barrier" :: "i"((c < 8 ? 16 : 8) + TN[c] + TN[c + 1] + TN[c + 2]) : "memory"); SB();
      if (c == TC + 4) { _Pragma("unroll") for (int i = 0; i < 8; ++i) PIN(rq2[i]); }
      if (c == TC + 6) { _Pragma("unroll") for (int i = 0; i < 8; ++i) { PIN(rw[i]); PIN(r5[i]); } }
      SB();
      if (c < 7) issue(c + 3);
      SB();
      if (c == TC) {
        e1 = __hip_atomic_load(c0.epoch(), RLX_AGENT) + 1;
#pragma unroll
        for (int jj = 0; jj < 8; ++jj) rq2[jj] = __builtin_amdgcn_raw_buffer_load_b64(rwq, qv, sq + jj * 8192, 0);
      }
      if (c == TC + 1) {
#pragma unroll
        for (int jj = 0; jj < 8; ++jj) rw[jj] = __builtin_amdgcn_raw_buffer_load_b64(rwo, wv0 + (jj & 3) * 1280, s2 + (jj >> 2) * 16 * 1280, 0);
      }
      if (c == TC + 2) {
#pragma unroll
        for (int jj = 0; jj < 8; ++jj) r5[jj] = __builtin_amdgcn_raw_buffer_load_b16(rwo, wv1 + (jj & 3) * 1280, s2 + (jj >> 2) * 16 * 1280, 0);
      }
      if (c == TC + 4) {
#pragma unroll
        for (int e = 0; e < 4; ++e) { u32x4 f;
#pragma unroll
          for (int pp = 0; pp < 4; ++pp) f[pp] = __builtin_amdgcn_perm(rq2[2 * pp + 1][e >> 1], rq2[2 * pp][e >> 1], (e & 1) ? 0x07060302u : 0x05040100u);
          __builtin_amdgcn_raw_buffer_store_b128(f, c0.rqt(), (tk * 6 + tu) * 4096 + e * 1024 + lane * 16, 0, 16); }
      }
      if (c == TC + 6) {
#pragma unroll
        for (int e = 0; e < 5; ++e) { u32x4 f;
#pragma unroll
          for (int pp = 0; pp < 4; ++pp) f[pp] = e < 4 ? __builtin_amdgcn_perm(rw[2 * pp + 1][(e >> 1) & 1], rw[2 * pp][(e >> 1) & 1], (e & 1) ? 0x07060302u : 0x05040100u)
                                                       : __builtin_amdgcn_perm((unsigned)r5[2 * pp + 1], (unsigned)r5[2 * pp], 0x05040100u);
          __builtin_amdgcn_raw_buffer_store_b128(f, c0.rwt(), (b * 4 + w) * 5120 + e * 1024 + lane * 16, 0, 16); }
      }
    }
    {
      const unsigned long long tag = ((unsigned long long)rfl((unsigned)(e1 >> 32)) << 32) | rfl((unsigned)e1);
      *(LAS unsigned long long*)(lds + TAGW) = tag;
      publish(c0.tflags() + b, lds + CNT + 16, tag, lane);
    }
  }
  if (w < 3) attn_main<2>(w, lds, l0, rk, rv, c0, nst); else attn_main<1>(w, lds, l0, rk, rv, c0, nst);
}

constexpr int LAG = 3;
DI void parrive(unsigned& pc, const int j, lptr lds) { WAITL(); ++pc; prog_post(lds + PPROG + j * 4, pc); }
template <int NE> constexpr int efrag(int e) { return NE == 5 ? e : (NE == 3 ? (e < 2 ? e : 4) : e + 2); }
template <int NE> DI u32x4 wload(rsrc_t rwt, const unsigned lv, const unsigned so, const int e) {
  return __builtin_amdgcn_raw_buffer_load_b128(rwt, lv + (efrag<NE>(e) & 3) * 1024, so + (efrag<NE>(e) >> 2) * 4096, 16);
}
DI void send_flag(const Ctx& c, const int dst, const int part, const unsigned lv, lptr lds) {
  const unsigned long long tag = *(const LAS unsigned long long*)(lds + TAGW);
  asm volatile("s_waitcnt vmcnt(0)" ::: "memory");
  if (lv == 0) __hip_atomic_store(c.xflag(dst) + c.g + part, tag, RLX_AGENT);
}
template <int NE>
DI void oproj_pass(const int j, const bool pstv, const Ctx& c0, lptr lds, const unsigned l0, int& nst) {
  const int lane = fresh_lane();
  const Ctx c = c0.fresh();
  const int mrot = ROT ? c.b : 0, side = (c.g ^ (NE != 5)) & 1;
  const rsrc_t rwt = c.rwt();
  const unsigned pa = l0 + APROG + (lane & 3) * 4, lv = lane * 16, tb = (side * 64 + j) * 5120;
  f32x4 acc[NE][7]; u32x4 af[4][NE], bf[4];
#pragma unroll
  for (int k = 0; k < 3; ++k)
#pragma unroll
    for (int e = 0; e < NE; ++e) { af[k][e] = wload<NE>(rwt, lv, tb + ((k + mrot) & 3) * 20480, e); SB(); }
#pragma unroll
  for (int e = 0; e < NE; ++e)
#pragma unroll
    for (int t = 0; t < 7; ++t) acc[e][t] = f32x4{0.f, 0.f, 0.f, 0.f};
  asm volatile("s_nop 1"); SB();
#pragma nounroll
  for (int r = 0; r < 3; ++r) {
    const unsigned reg = qreg(r + 1);
    if (NE != 2) prog_wait<32, 2>(pa, 8 * r + 9);
    if (NE == 2 && r == 2) send_flag(c0.fresh(), 2 * j + (side ^ 1), 0, lv, lds);
    PST();
    auto baddr = [&](int gi) { return lp(l0 + reg + ((gi / 7 + mrot) & 3) * 1024 + lv) + (gi % 7) * 4096; };
#pragma unroll
    for (int gi = 0; gi < LAG; ++gi) bf[gi] = lds128(baddr(gi));
    SB();
#pragma unroll
    for (int k = 0; k < 4; ++k) {
      int r2 = r; asm volatile("" : "+s"(r2));
      const unsigned so = tb + ((k ? r2 + 1 : r2) * 4 + ((k + 3 + mrot) & 3)) * 20480;
#pragma unroll
      for (int t = 0; t < 7; ++t) {
        const int gi = 7 * k + t;
#pragma unroll
        for (int e = 0; e < NE; ++e) {
          mfma_ip(acc[e][t], af[k][e], bf[gi & 3]);
          if (e == 0 && gi + LAG < 28) bf[(gi + LAG) & 3] = lds128(baddr(gi + LAG));
          if (e == NE / 2 && t >= 1 && t <= NE) af[(k + 3) & 3][t - 1] = wload<NE>(rwt, lv, so, t - 1);
          SB();
        }
      }
    }
    PST();
  }
#pragma unroll
  for (int e = 0; e < NE; ++e) af[3][e] = wload<NE>(rwt, lv, tb + (12 + ((3 + mrot) & 3)) * 20480, e);
  if (NE == 5 && WARM) {
    prog_wait<0, 8>(pa, WARM);
#pragma unroll
    for (int i = 0; i < 10; ++i) dma_sc1(rwt, lv, (side ^ 1) * 327680 + ((c.b & 7) * 40 + j * 10 + i) * 1024, l0 + DUMMY);
  }
  if (NE != 2) prog_wait<32, 1>(pa, 33);
  if (NE == 5) __builtin_amdgcn_s_setprio(2);
  PST();
  const Ctx c2 = c0.fresh(); const int dst = 2 * j + side;
  const bool keep = NE == 5 && dst == c2.g;
  const rsrc_t rpx = c2.rpx(dst); const unsigned po = c2.g * 17920;
  unsigned ba[4];
#pragma unroll
  for (int k = 0; k < 4; ++k) ba[k] = l0 + OWN3 + ((k + mrot) & 3) * 1024 + lv;
  auto out = [&](int t, int part) {
    if (keep) { *(LAS f32x4*)(lp(l0 + OWNP + lv) + (t * 5 + part) * 1024) = acc[part % NE][t]; if (part == 3) *(LAS f32x4*)(lp(l0 + OWNP + lv) + (t * 5 + 4) * 1024) = acc[NE - 1][t]; }
    else if (part < NE / 2) {
      const f32x4 x = acc[2 * part][t], y = acc[2 * part + 1][t];
      __builtin_amdgcn_raw_buffer_store_b128(u32x4{cvtpk(x[0], y[0]), cvtpk(x[1], y[1]), cvtpk(x[2], y[2]), cvtpk(x[3], y[3])}, rpx, po + lv + t * 2560 + efrag<NE>(2 * part) * 512, 0, 16);
    } else if (part == 2 && (NE & 1)) { const f32x4 x = acc[NE - 1][t];
      __builtin_amdgcn_raw_buffer_store_b64(u32x2{cvtpk(x[0], x[1]), cvtpk(x[2], x[3])}, rpx, po + (lv >> 1) + t * 2560 + 2048, 0, 16); }
  };
#pragma unroll
  for (int gi = 0; gi < LAG; ++gi) bf[gi] = lds128(lp(ba[gi & 3]) + (gi >> 2) * 4096);
  SB();
#pragma unroll
  for (int gi = 0; gi < 28; ++gi) {
    const int t = gi >> 2, k = gi & 3;
#pragma unroll
    for (int e = 0; e < NE; ++e) {
      mfma_ip(acc[e][t], af[k][e], bf[gi & 3]);
      if (e == 0 && gi + LAG < 28) bf[(gi + LAG) & 3] = lds128(lp(ba[(gi + LAG) & 3]) + ((gi + LAG) >> 2) * 4096);
      if (e == NE - 1 && k == 0 && t > 0) {
        asm volatile("s_nop %0" :: "i"(8 - NE));
#pragma unroll
        for (int e2 = 0; e2 < NE; ++e2) PIN(acc[e2][t - 1]);
      }
      if (e == NE - 1 && t > 0) out(t - 1, k);
      SB();
    }
  }
  MFMA_DONE();
#pragma unroll
  for (int e2 = 0; e2 < NE; ++e2) PIN(acc[e2][6]);
  SB();
#pragma unroll
  for (int k = 0; k < 4; ++k) out(6, k);
  if (keep) prog_post(lds + KPROG, 1);
  if (NE != 5) { send_flag(c2, dst, NE == 2 ? 8 : 0, lv, lds); __builtin_amdgcn_s_waitcnt(0x0F70); }
  PST();
}

DI void final_part(const int j, const Ctx& c0, lptr lds, const unsigned l0, char* out, int& nst) {
  const Ctx c = c0.fresh(); const int b = c.b, g = c.g; const rsrc_t rpx = c.rpx(g);
  const int lane = fresh_lane(), p = g >> 1, lg = lane >> 4, lr = lane & 15;
  const bool pstv = j == 0; (void)pstv;
  const unsigned long long tg = *(const LAS unsigned long long*)(lds + TAGW), tag = ((unsigned long long)rfl((unsigned)(tg >> 32)) << 32) | rfl((unsigned)tg);
  auto lo = [](unsigned v) { return __uint_as_float(v << 16); };
  auto hi = [](unsigned v) { return __uint_as_float(v & 0xffff0000u); };
  f32x4 a[2][5];
#pragma unroll
  for (int st = 0; st < 3; ++st) {
    const int ns = st ? 4 : 3;
    {
      const int k = lane & 3;
      gu64* f = c.xflag(g) + (st ? 2 * k + 1 - (g & 1) + (st == 1 ? 8 : 0) : 2 * (k == p ? (p + 1) & 3 : k) + (g & 1));
      wait_flag(f, __hip_atomic_load(f, RLX_AGENT), tag);
    }
    u32x4 in[2][4][2]; u32x2 in2[2][4];
#pragma unroll
    for (int i = 0; i < 2; ++i)
#pragma unroll
      for (int k = 0; k < ns; ++k) {
        const int src = st ? 2 * k + 1 - (g & 1) : 2 * (k + (k >= p)) + (g & 1);
        const unsigned o = (i == 1 && j == 3) ? OOB : src * 17920 + (j + 4 * i) * 2560;
        if (st != 1) in[i][k][0] = __builtin_amdgcn_raw_buffer_load_b128(rpx, lane * 16 + o, 0, 16);
        if (st != 2) in[i][k][1] = __builtin_amdgcn_raw_buffer_load_b128(rpx, lane * 16 + o + 1024, 0, 16);
        if (st != 1) in2[i][k] = __builtin_amdgcn_raw_buffer_load_b64(rpx, lane * 8 + o + 2048, 0, 16);
      }
    if (st == 0) {
      prog_wait<0, 1>(l0 + KPROG, 1);
#pragma unroll
      for (int i = 0; i < 2; ++i)
#pragma unroll
        for (int e = 0; e < 5; ++e) a[i][e] = *(LAS f32x4*)(lds + OWNP + (((i == 1 && j == 3) ? 0 : j + 4 * i) * 5 + e) * 1024 + lane * 16);
    }
#pragma unroll
    for (int i = 0; i < 2; ++i)
#pragma unroll
      for (int k = 0; k < ns; ++k) {
#pragma unroll
        for (int d = (st == 1 ? 4 : 0); d < (st == 2 ? 4 : 8); ++d) { const int reg = d & 3, e = 2 * (d >> 2); a[i][e][reg] += lo(in[i][k][d >> 2][d & 3]); a[i][e + 1][reg] += hi(in[i][k][d >> 2][d & 3]); }
        if (st != 1) {
#pragma unroll
          for (int d = 0; d < 2; ++d) { a[i][4][2 * d] += lo(in2[i][k][d]); a[i][4][2 * d + 1] += hi(in2[i][k][d]); }
        }
      }
    PST();
  }
  if (j == 0 && g == 0 && b == 0 && lane == 0) __hip_atomic_store(c.epoch(), tag, RLX_AGENT);
  const rsrc_t rout = mkrsrc(out + (size_t)b * 143360, 143360);
#pragma unroll
  for (int i = 0; i < 2; ++i) {
    const unsigned ob = (i == 1 && j == 3) ? OOB : ((j + 4 * i) * 16 + lr) * 1280 + (g & 1) * 640;
#pragma unroll
    for (int hf = 0; hf < 2; ++hf)
      __builtin_amdgcn_raw_buffer_store_b128(u32x4{cvtpk(a[i][0][2 * hf], a[i][1][2 * hf]), cvtpk(a[i][2][2 * hf], a[i][3][2 * hf]), cvtpk(a[i][0][2 * hf + 1], a[i][1][2 * hf + 1]),
                                                   cvtpk(a[i][2][2 * hf + 1], a[i][3][2 * hf + 1])}, rout, ob + p * 128 + lg * 32 + hf * 16, 0, 0);
    __builtin_amdgcn_raw_buffer_store_b64(u32x2{cvtpk(a[i][4][0], a[i][4][1]), cvtpk(a[i][4][2], a[i][4][3])}, rout, ob + 512 + p * 32 + lg * 8, 0, 0);
  }
}

template <int NE> struct QpState { f32x4 acc[NE][7]; u32x4 xf[7]; lptr xa0, xa1; };
template <int NE> DI void qp_init(QpState<NE>& q, lptr xb, int lane) {
  const int lg = lane >> 4, lr = lane & 15;
  q.xa0 = xb + lr * 128 + ((lg ^ (lr >> 1)) << 4); q.xa1 = xb + lr * 128 + (((4 + lg) ^ (lr >> 1)) << 4);
#pragma unroll
  for (int t = 0; t < 7; ++t) { q.xf[t] = u32x4{0u, 0u, 0u, 0u};
#pragma unroll
    for (int e = 0; e < NE; ++e) q.acc[e][t] = f32x4{0.f, 0.f, 0.f, 0.f}; }
  asm volatile("s_nop 1"); SB();
}
template <int PAR, int NE, bool DEEP>
DI void qp2_chunk(QpState<NE>& q, u32x4 (&af)[4][NE], const int c, const int j, const int rot, unsigned& pc, const unsigned pa, const unsigned xvo, const unsigned lv, const unsigned tb,
                  lptr lds, const unsigned l0, rsrc_t rx, rsrc_t rqt) {
  SB(); asm volatile("s_waitcnt vmcnt(%0) lgkmcnt(0)" :: "i"(DEEP ? 6 * NE + 8 : 2 * NE) : "memory"); SB();
  ++pc; prog_post(lds + PPROG + j * 4, pc);
  const unsigned pg = prog_read<16>(pa);
  const int c1 = c + 1 + rot >= 10 ? c + 1 + rot - 10 : c + 1 + rot, c2 = c + 2 + rot >= 10 ? c + 2 + rot - 10 : c + 2 + rot;
  const unsigned s1 = c < 9 ? (2 * c1 + 1) * 24576 + tb : OOB, s2 = c < 8 ? (2 * c2) * 24576 + tb : OOB;
  lptr x0 = q.xa0 + (DEEP ? (c & 2) * XCH : 0), x1 = q.xa1 + (DEEP ? (c & 2) * XCH : 0);
  SB();
#pragma unroll
  for (int u = 0; u < 14; ++u) {
    const int v = (u + 14 - LAG) % 14, t = v % 7, set = u < LAG ? 2 * (PAR ^ 1) + 1 : 2 * PAR + v / 7;
#pragma unroll
    for (int e = 0; e < NE; ++e) {
      mfma_ip(q.acc[e][t], af[set][e], q.xf[t]);
      if (e == 0 && u >= 2) q.xf[u % 7] = lds128((u >= 7 ? x1 : x0) + PAR * XCH + (u % 7) * 2048);
      if (e == NE / 2 && u >= LAG && u < LAG + NE) af[2 * (PAR ^ 1) + 1][u - LAG] = __builtin_amdgcn_raw_buffer_load_b128(rqt, lv + (u - LAG) * 1024, s1, 16);
      if (e == NE / 2 && u >= 10 && u < 10 + NE) af[2 * PAR][u - 10] = __builtin_amdgcn_raw_buffer_load_b128(rqt, lv + (u - 10) * 1024, s2, 16);
      SB();
    }
    if (u == 0) { prog_wait<16, 0>(pa, pg, pc); q.xf[0] = lds128(x0 + PAR * XCH); q.xf[1] = lds128(x0 + PAR * XCH + 2048); SB(); }
    if (u == 1) {
      const bool on = DEEP ? c < 8 : c < 9;
      const int cx = !DEEP ? c1 : (c + 3 + rot >= 10 ? c + 3 + rot - 10 : c + 3 + rot);
      const unsigned xs = on ? j * 10240 + cx * 128 : OOB, xd = l0 + (on ? (DEEP ? RB + ((c + 3) & 3) * XCH : RC + (PAR ^ 1) * XCH) + j * 1024 : DUMMY), st = on ? 4096u : 0u;
#pragma unroll
      for (int i = 0; i < 3; ++i) dma_df(rx, xvo, xs + i * 40960, xd + i * st);
      dma_df(rx, xvo, xs + 3 * 40960, j < 2 ? xd + 3 * st : l0 + DUMMY);
      SB();
    }
  }
}
template <int I>
DI void qp1_chunk(QpState<2>& q, u32x4 (&af)[10][2], const int c, const int j, const int rot, unsigned& pc, const unsigned pa, const unsigned xvo, const unsigned lv,
                  lptr lds, const unsigned l0, rsrc_t rx, rsrc_t rqt) {
  WAITV(20);
  ++pc; prog_post(lds + PPROG + j * 4, pc);
  const unsigned pg = prog_read<16>(pa);
  const int c4 = c + 4 + rot >= 10 ? c + 4 + rot - 10 : c + 4 + rot, c5 = c + 5 + rot >= 10 ? c + 5 + rot - 10 : c + 5 + rot;
  const unsigned s1 = c < 6 ? (2 * c4 + 1) * 24576 + j * 2048 : OOB, s2 = c < 5 ? (2 * c5) * 24576 + j * 2048 : OOB;
  lptr x0 = q.xa0 + (c & 3) * XCH, x1 = q.xa1 + (c & 3) * XCH;
  constexpr int SP = (2 * I + 9) % 10;
  SB();
#pragma unroll
  for (int u = 0; u < 14; ++u) {
    const int v = (u + 14 - LAG) % 14, t = v % 7, set = u < LAG ? SP : 2 * I + v / 7;
#pragma unroll
    for (int e = 0; e < 2; ++e) {
      mfma_ip(q.acc[e][t], af[set][e], q.xf[t]);
      if (e == 0 && u >= 2) q.xf[u % 7] = lds128((u >= 7 ? x1 : x0) + (u % 7) * 2048);
      if (e == 1 && (u == 4 || u == 6)) af[SP][(u - 4) >> 1] = __builtin_amdgcn_raw_buffer_load_b128(rqt, lv + ((u - 4) >> 1) * 1024, s1, 16);
      if (e == 1 && (u == 10 || u == 12)) af[2 * I][(u - 10) >> 1] = __builtin_amdgcn_raw_buffer_load_b128(rqt, lv + ((u - 10) >> 1) * 1024, s2, 16);
      SB();
    }
    if (u == 0) { prog_wait<16, 0>(pa, pg, pc); q.xf[0] = lds128(x0); q.xf[1] = lds128(x0 + 2048); SB(); }
    if (u == 1) {
      const bool on = c < 8;
      const int cx = c + 3 + rot >= 10 ? c + 3 + rot - 10 : c + 3 + rot;
      const unsigned xs = on ? j * 10240 + cx * 128 : OOB, xd = l0 + (on ? RB + ((c + 3) & 3) * XCH + j * 1024 : DUMMY), st = on ? 4096u : 0u;
#pragma unroll
      for (int i = 0; i < 3; ++i) dma_df(rx, xvo, xs + i * 40960, xd + i * st);
      dma_df(rx, xvo, xs + 3 * 40960, j < 2 ? xd + 3 * st : l0 + DUMMY);
      SB();
    }
  }
}
template <int NE> DI void qp2_first(u32x4 (&af)[4][NE], const int rot, const unsigned lv, const unsigned tb, rsrc_t rqt) {
  const int r1 = rot == 9 ? 0 : rot + 1;
#pragma unroll
  for (int k = 0; k < 3; ++k)
#pragma unroll
    for (int e = 0; e < NE; ++e) { af[k][e] = __builtin_amdgcn_raw_buffer_load_b128(rqt, lv + e * 1024, (k < 2 ? 2 * rot + k : 2 * r1) * 24576 + tb, 16); SB(); }
#pragma unroll
  for (int e = 0; e < NE; ++e) af[3][e] = u32x4{0u, 0u, 0u, 0u};
}
template <int NE> DI void qp_tail(QpState<NE>& q, const u32x4 (&a)[NE]) {
#pragma unroll
  for (int t = 7 - LAG; t < 7; ++t)
#pragma unroll
    for (int e = 0; e < NE; ++e) mfma_ip(q.acc[e][t], a[e], q.xf[t]);
  MFMA_DONE(); SB();
}

DI void proj_wave(const int j, const int lane, const Ctx& c0, const int rot, lptr lds, const unsigned l0, rsrc_t rx, int& nst) {
  unsigned long long tag;
  const bool pstv = j == 0; (void)pstv;
  const int lg = lane >> 4, lr = lane & 15;
  {
    QpState<2> q;
    u32x4 af[2][2];
    af[1][0] = af[1][1] = u32x4{0u, 0u, 0u, 0u};
    qp_init(q, lds + VOFF + SLOT, lane);
    const int q4 = lr >> 2;
    const unsigned wa = l0 + VOFF + (8 * lg + q4) * 256 + (((j ^ q4) * 4 + (lane & 3)) << 4);
#pragma nounroll
    for (int c = 0; c < 10; ++c) {
      const int bo = (c & 3) * PBUF;
      BAR_L();
#pragma unroll
      for (int u = 0; u < 14; ++u) {
        const int v = (u + 14 - LAG) % 14, t = v % 7, set = u < LAG ? 1 : v / 7;
        auto ard = [&](int ks, int e) { lptr p = lp(wa + bo) + ks * 8192 + e * 8; const u32x2 lo = ldstr(p), hi = ldstr(p + 1024); af[ks][e] = u32x4{lo[0], lo[1], hi[0], hi[1]}; };
        if (u == 0) { ard(0, 0); q.xf[0] = lds128(q.xa0 + bo); ard(0, 1); SB(); }
#pragma unroll
        for (int e = 0; e < 2; ++e) {
          mfma_ip(q.acc[e][t], af[set][e], q.xf[t]);
          if (e == 0 && u >= 1) q.xf[u % 7] = lds128((u >= 7 ? q.xa1 : q.xa0) + bo + (u % 7) * 2048);
          if (e == 1 && (u == 4 || u == 5)) ard(1, u - 4);
          SB();
        }
      }
      PST();
    }
    qp_tail(q, af[1]);
    BAR_L();
#pragma unroll
    for (int t = 0; t < 7; ++t)
      *(LAS u32x4*)(lds + RA + t * 4096 + j * 1024 + lane * 16) = u32x4{cvtpk(q.acc[0][t][0] * QSCALE, q.acc[0][t][1] * QSCALE), cvtpk(q.acc[0][t][2] * QSCALE, q.acc[0][t][3] * QSCALE),
                                                                        cvtpk(q.acc[1][t][0] * QSCALE, q.acc[1][t][1] * QSCALE), cvtpk(q.acc[1][t][2] * QSCALE, q.acc[1][t][3] * QSCALE)};
    { const unsigned long long tg = *(const LAS unsigned long long*)(lds + TAGW); tag = ((unsigned long long)rfl((unsigned)(tg >> 32)) << 32) | rfl((unsigned)tg); }
    BAR_L();
    PST();
  }
  unsigned pc = 0;
  {
    const Ctx c = c0.fresh();
    const rsrc_t rqt = c.rqt();
    const unsigned pa = l0 + APROG + (lane & 3) * 4, lv = lane * 16;
    const unsigned xvo = (lane >> 3) * 1280 + (((lane & 7) ^ ((4 * (j & 1) + lg) & 7)) << 4);
#pragma unroll
    for (int k = 0; k < 3; ++k) {
      const unsigned xs = j * 10240 + (k + rot >= 10 ? k + rot - 10 : k + rot) * 128, xd = l0 + RB + k * XCH + j * 1024;
#pragma unroll
      for (int i = 0; i < 3; ++i) dma_df(rx, xvo, xs + i * 40960, xd + i * 4096);
      dma_df(rx, xvo, xs + 3 * 40960, j < 2 ? xd + 3 * 4096 : l0 + DUMMY);
    }
    wait_flag(c.tflags() + (lane & 31), __hip_atomic_load(c.tflags() + (lane & 31), RLX_AGENT), tag);
    u32x4 af[4][4];
    {
      QpState<2> q; u32x4 a2[10][2];
#pragma unroll
      for (int k = 0; k < 9; ++k) {
        const int ck = (k >> 1) + rot >= 10 ? (k >> 1) + rot - 10 : (k >> 1) + rot;
#pragma unroll
        for (int e = 0; e < 2; ++e) { a2[k][e] = __builtin_amdgcn_raw_buffer_load_b128(rqt, lv + e * 1024, (2 * ck + (k & 1)) * 24576 + j * 2048, 16); SB(); }
      }
      a2[9][0] = a2[9][1] = u32x4{0u, 0u, 0u, 0u};
      qp_init(q, lds + RB, lane);
      PST();
#pragma nounroll
      for (int cc = 0; cc < 10; cc += 5) {
        qp1_chunk<0>(q, a2, cc, j, rot, pc, pa, xvo, lv, lds, l0, rx, rqt); qp1_chunk<1>(q, a2, cc + 1, j, rot, pc, pa, xvo, lv, lds, l0, rx, rqt);
        qp1_chunk<2>(q, a2, cc + 2, j, rot, pc, pa, xvo, lv, lds, l0, rx, rqt); qp1_chunk<3>(q, a2, cc + 3, j, rot, pc, pa, xvo, lv, lds, l0, rx, rqt);
        qp1_chunk<4>(q, a2, cc + 4, j, rot, pc, pa, xvo, lv, lds, l0, rx, rqt);
      }
      qp_tail(q, a2[9]);
      parrive(pc, j, lds);
      qp2_first(af, rot, lv, (j + 2) * 4096, rqt);
      prog_wait<16, 0>(pa, pc);
      lptr qp = lds + RB + (8 * (j >> 1) + 2 * lg) * 256 + lr * 16 + (j & 1) * 4;
#pragma unroll
      for (int t = 0; t < 7; ++t)
#pragma unroll
        for (int r = 0; r < 4; ++r) *(LAS unsigned*)(qp + t * 4096 + (r >> 1) * 256 + (r & 1) * 8) = cvtpk(q.acc[0][t][r] * QSCALE, q.acc[1][t][r] * QSCALE);
      parrive(pc, j, lds);
      PST();
    }
    QpState<4> q;
    qp_init(q, lds + RC, lane);
#pragma nounroll
    for (int cc = 0; cc < 10; cc += 2) {
      qp2_chunk<0, 4, false>(q, af, cc, j, rot, pc, pa, xvo, lv, (j + 2) * 4096, lds, l0, rx, rqt);
      qp2_chunk<1, 4, false>(q, af, cc + 1, j, rot, pc, pa, xvo, lv, (j + 2) * 4096, lds, l0, rx, rqt);
      PST();
    }
    qp_tail(q, af[3]);
    parrive(pc, j, lds);
    u32x4 qf[7][2];
#pragma unroll
    for (int t = 0; t < 7; ++t)
#pragma unroll
      for (int hf = 0; hf < 2; ++hf) {
        qf[t][hf] = u32x4{cvtpk(q.acc[0][t][2 * hf] * QSCALE, q.acc[1][t][2 * hf] * QSCALE), cvtpk(q.acc[2][t][2 * hf] * QSCALE, q.acc[3][t][2 * hf] * QSCALE),
                          cvtpk(q.acc[0][t][2 * hf + 1] * QSCALE, q.acc[1][t][2 * hf + 1] * QSCALE), cvtpk(q.acc[2][t][2 * hf + 1] * QSCALE, q.acc[3][t][2 * hf + 1] * QSCALE)};
        PIN(qf[t][hf]);
      }
    prog_wait<16, 0>(pa, pc);
    prog_wait<32, 0>(pa, 1);
#pragma unroll
    for (int t = 0; t < 7; ++t)
#pragma unroll
      for (int hf = 0; hf < 2; ++hf) *(LAS u32x4*)(lds + ((j >> 1) ? RC : RA) + (4 * (j & 1) + lg) * 512 + lr * 16 + t * 4096 + hf * 256) = qf[t][hf];
    parrive(pc, j, lds);
    PST();
  }
  __builtin_amdgcn_s_waitcnt(0x0F70);
}

__global__ __launch_bounds__(512, 2) void attn_fused(const char* __restrict__ x, const char* __restrict__ K, const char* __restrict__ V,
                                                     const char* __restrict__ wq, const char* __restrict__ wo, char* __restrict__ ws, char* __restrict__ out) {
  extern __shared__ __attribute__((aligned(1024))) char smem[];
  const int w = rfl(threadIdx.x >> 6);
  unsigned long long pc0 = 0;
  if (CODEPF) asm volatile("s_getpc_b64 %0" : "=s"(pc0));
  const int bid = blockIdx.x;
  const int b = ((bid >> 1) & 3) * 8 + (bid >> 5), g = (bid & 1) * 4 + ((bid >> 3) & 3);
  const int rot = ROT ? (((b & 7) * 5) / 4 + ((g & 3) * 5) / 2) % 10 : 0;
  lptr lds = (lptr)smem;
  const unsigned l0 = (unsigned)(uintptr_t)lds;
  const Ctx c = {ws, b, g};
  const rsrc_t rx = mkrsrc(x + (size_t)b * 143360, 143360), rwq = mkrsrc(wq, 640 * 8192);
  int nst = 0; (void)nst;
  if (w < 4) {
    const size_t kvb = ((size_t)b * 32 + g * 4) * 131072;
    attn_wave(w, fresh_lane(), rot, lds, l0, mkrsrc(K + kvb, 524288), mkrsrc(V + kvb, 524288), rx, rwq, wo, c, nst);
    oproj_pass<3>(w, false, c, lds, l0, nst);
    ST(); ST();
  } else {
    if (CODEPF && w < 6) dma_df(mkrsrc((const void*)(pc0 & ~255ull), CODEPF), fresh_lane() * 16, ((blockIdx.x >> 3) * 2 + (w - 4)) * 1024, l0 + DUMMY + 1024);
    proj_wave(w - 4, fresh_lane(), c, rot, lds, l0, rx, nst);
    oproj_pass<5>(w - 4, w == 4, c, lds, l0, nst);
    oproj_pass<2>(w - 4, w == 4, c, lds, l0, nst);
    final_part(w - 4, c, lds, l0, out, nst);
    __builtin_amdgcn_s_waitcnt(0x0F70);
  }
}

extern "C" void kernel_launch(void* const* d_in, const int* in_sizes, int n_in, void* d_out, int out_size, void* d_ws, size_t ws_size, hipStream_t stream) {
  (void)hipFuncSetAttribute((const void*)attn_fused, hipFuncAttributeMaxDynamicSharedMemorySize, LDS_BYTES);
  attn_fused<<<256, 512, LDS_BYTES, stream>>>((const char*)d_in[0], (const char*)d_in[1], (const char*)d_in[2], (const char*)d_in[3], (const char*)d_in[4], (char*)d_ws + WSOFF, (char*)d_out);
}
```

```cpp
#include <hip/hip_runtime.h>
#include <stdint.h>
typedef __attribute__((ext_vector_type(8))) __bf16 bf16x8;
typedef __attribute__((ext_vector_type(2))) __bf16 bf16x2;
typedef __attribute__((ext_vector_type(4))) float f32x4;
typedef __attribute__((ext_vector_type(16))) float f32x16;
typedef __attribute__((ext_vector_type(2))) float f32x2;
typedef __attribute__((ext_vector_type(4))) unsigned u32x4;
typedef __attribute__((ext_vector_type(2))) unsigned u32x2;
typedef __attribute__((ext_vector_type(4))) short s16x4;
#define LAS __attribute__((address_space(3)))
#define DI __device__ __forceinline__
typedef LAS char* lptr;
typedef __amdgpu_buffer_rsrc_t rsrc_t;
typedef __attribute__((address_space(1))) unsigned long long gu64;
#define RLX_AGENT __ATOMIC_RELAXED, __HIP_MEMORY_SCOPE_AGENT

constexpr int SLOT = 16384, VOFF = 2 * SLOT, XCH = 14336, RA = 4 * SLOT, RB = RA + 2 * XCH, RC = RB + 2 * XCH, PBUF = SLOT + XCH, DUMMY = VOFF + 4 * PBUF,
              TAGW = DUMMY + 4096, CNT = TAGW + 16, APROG = CNT + 32, PPROG = APROG + 16, QPROG = PPROG + 16, KPROG = QPROG + 16, STMP = CNT + 128, ARR = STMP + 512, LDS_BYTES = ARR + 1024;
constexpr float QSCALE = 0.08838834764831845f * 1.4426950408889634f;
constexpr unsigned OOB = 0x40000000u;
constexpr size_t WS_TF = 8192, WS_OF = 16384, WS_XF = WS_OF + 131072, WS_O = 1u << 20, WS_Q = 40u << 20, WS_WT = 48u << 20, WS_QT = 56u << 20, WS_PX = 64u << 20, WS_ST = 128u << 20;
constexpr int OWN3 = 0, OWNP = 2 * XCH;
constexpr unsigned P_Q12 = 12, P_Q3 = 24;

template <int N> struct IC { static constexpr int value = N; };
DI unsigned rfl(unsigned x) { return __builtin_amdgcn_readfirstlane(x); }
DI unsigned cvtpk(float lo, float hi) { f32x2 v = {lo, hi}; bf16x2 b = __builtin_convertvector(v, bf16x2); return __builtin_bit_cast(unsigned, b); }
DI f32x4 mfma(u32x4 a, u32x4 b, f32x4 c) { return __builtin_amdgcn_mfma_f32_16x16x32_bf16(__builtin_bit_cast(bf16x8, a), __builtin_bit_cast(bf16x8, b), c, 0, 0, 0); }
DI void mfma_ip(f32x4& c, const u32x4& a, const u32x4& b) { asm volatile("v_mfma_f32_16x16x32_bf16 %0, %1, %2, %0" : "+v"(c) : "v"(a), "v"(b)); }
#define MFMA_DONE() asm volatile("s_nop 15")
DI f32x16 mfma32(u32x4 a, u32x4 b, f32x16 c) { return __builtin_amdgcn_mfma_f32_32x32x16_bf16(__builtin_bit_cast(bf16x8, a), __builtin_bit_cast(bf16x8, b), c, 0, 0, 0); }
DI u32x4 lds128(lptr p) { return *(const LAS u32x4*)p; }
DI int fresh_lane() { int l; asm volatile("v_mbcnt_lo_u32_b32 %0, -1, 0\n\tv_mbcnt_hi_u32_b32 %0, -1, %0" : "=v"(l)); return l; }
DI lptr lp(unsigned a) { return (lptr)(uintptr_t)a; }
DI u32x2 ldstr(lptr p) { return __builtin_bit_cast(u32x2, __builtin_amdgcn_ds_read_tr16_b64_v4i16((LAS s16x4*)p)); }
DI rsrc_t mkrsrc(const void* p, unsigned bytes) { return __builtin_amdgcn_make_buffer_rsrc((void*)p, 0, bytes, 0x00020000); }
DI float xmax(float m) {
  auto a = __builtin_amdgcn_permlane16_swap(__float_as_uint(m), __float_as_uint(m), false, false); m = fmaxf(__uint_as_float(a[0]), __uint_as_float(a[1]));
  auto b = __builtin_amdgcn_permlane32_swap(__float_as_uint(m), __float_as_uint(m), false, false); return fmaxf(__uint_as_float(b[0]), __uint_as_float(b[1]));
}
DI float xsum(float m) {
  auto a = __builtin_amdgcn_permlane16_swap(__float_as_uint(m), __float_as_uint(m), false, false); m = __uint_as_float(a[0]) + __uint_as_float(a[1]);
  auto b = __builtin_amdgcn_permlane32_swap(__float_as_uint(m), __float_as_uint(m), false, false); return __uint_as_float(b[0]) + __uint_as_float(b[1]);
}
#define DFPOL ""
#define TAUX 0
#define KVPOL "nt"
#define VPOL "nt"
template <int OFF, bool V = false> DI void dma_nt(rsrc_t r, unsigned voff, unsigned soff, unsigned ldsaddr) {
  if (V) asm volatile("s_mov_b32 m0, %3\n\ts_nop 0\n\tbuffer_load_dwordx4 %0, %1, %2 offen offset:%4 " VPOL " lds" :: "v"(voff), "s"(r), "s"(soff), "s"(ldsaddr), "i"(OFF) : "memory");
  else asm volatile("s_mov_b32 m0, %3\n\ts_nop 0\n\tbuffer_load_dwordx4 %0, %1, %2 offen offset:%4 " KVPOL " lds" :: "v"(voff), "s"(r), "s"(soff), "s"(ldsaddr), "i"(OFF) : "memory");
}
DI void dma_df(rsrc_t r, unsigned voff, unsigned soff, unsigned ldsaddr) {
  asm volatile("s_mov_b32 m0, %3\n\ts_nop 0\n\tbuffer_load_dwordx4 %0, %1, %2 offen " DFPOL "lds" :: "v"(voff), "s"(r), "s"(soff), "s"(ldsaddr) : "memory");
}
DI void dma_sc1(rsrc_t r, unsigned voff, unsigned soff, unsigned ldsaddr) {
  asm volatile("s_mov_b32 m0, %3\n\ts_nop 0\n\tbuffer_load_dwordx4 %0, %1, %2 offen sc1 lds" :: "v"(voff), "s"(r), "s"(soff), "s"(ldsaddr) : "memory");
}
DI void touch(rsrc_t r, unsigned voff, unsigned ldsaddr) {
  asm volatile("s_mov_b32 m0, %2\n\ts_nop 0\n\tbuffer_load_dword %0, %1, 0 offen lds" :: "v"(voff), "s"(r), "s"(ldsaddr) : "memory");
}
#define SB() __builtin_amdgcn_sched_barrier(0)
#define WAITV(N) do { SB(); asm volatile("s_waitcnt vmcnt(" #N ") lgkmcnt(0)" ::: "memory"); SB(); } while (0)
#define WAITVM(N) do { SB(); asm volatile("s_waitcnt vmcnt(" #N ")" ::: "memory"); SB(); } while (0)
#define WAITL() do { SB(); asm volatile("s_waitcnt lgkmcnt(0)" ::: "memory"); SB(); } while (0)
#define BAR_L() do { SB(); asm volatile("s_waitcnt lgkmcnt(0)\n\ts_barrier" ::: "memory"); SB(); } while (0)
#define ST() do {} while (0)
#define PST() do {} while (0)
#define PST2() do {} while (0)
#define PCAP 0
#define PC() do { if (PCAP) asm volatile("s_waitcnt vmcnt(%0)" :: "i"(PCAP - 1) : "memory"); } while (0)
#define PIN(x) asm volatile("" : "+v"(x))
#define NODMA 0
#define DPOS 2
#define DSTR 8
#define CAP 16
#define SAUX 16
#define PPRIO 0
#define ABL 0
#define TE0 2
#define XPRIO 0
#define PF 0
#define VP0 2
#define VST 4
#define KP0 18
#define KST 4
#define QPACE 10
#define OPACE 0
#define OP0 26
#define MAP 4
#define OSA 16
#define WSOFF 0
#define CODEPF 0
#define NH 2
#define SSL 0
#define DSL 0
#define LPRIO 3
#define ILV 0
#define TC 1
#define TN0 0, 9, 8, 8, 0, 4, 0, 5, 0, 0
#define LCAP 0
#define RPACE 0
#define WARM 0
#define TPRIO 0
#define ROT 1
#define QLD0 6
#define PFD 16
constexpr float PLIM = 1024.f;

template <int OFF> DI unsigned prog_read(unsigned pa) { return *(volatile LAS unsigned*)(lp(pa) + OFF); }
template <int OFF, int SLEEP> DI void prog_wait(unsigned pa, unsigned v, unsigned t) {
  unsigned n;
  asm volatile("s_mov_b32 %1, 0x1000000\n"
               "1:\n\tv_cmp_gt_u32 vcc, %3, %0\n\ts_cbranch_vccz 2f\n\ts_sleep %5\n\tds_read_b32 %0, %2 offset:%4\n\ts_sub_u32 %1, %1, 1\n\ts_waitcnt lgkmcnt(0)\n\ts_cbranch_scc0 1b\n2:"
               : "+v"(v), "=&s"(n) : "v"(pa), "s"(t), "i"(OFF), "i"(SLEEP) : "memory", "vcc", "scc");
}
template <int OFF, int SLEEP> DI void prog_wait(unsigned pa, unsigned t) { prog_wait<OFF, SLEEP>(pa, prog_read<OFF>(pa), t); }
DI void prog_post(lptr p, unsigned v) { asm volatile("" ::: "memory"); *(volatile LAS unsigned*)p = v; }

DI void publish(gu64* flag, lptr cnt, unsigned long long tag, int lane) {
  asm volatile("s_waitcnt vmcnt(0)" ::: "memory");
  if (lane == 0) {
    const unsigned old = __hip_atomic_fetch_add((LAS unsigned*)cnt, 1u, __ATOMIC_RELAXED, __HIP_MEMORY_SCOPE_WORKGROUP);
    asm volatile("" : "+s"(tag));
    if (old == 3u) __hip_atomic_store(flag, tag, RLX_AGENT);
  }
}
DI void wait_flag(gu64* f, unsigned long long v, unsigned long long tag) {
#pragma nounroll
  for (unsigned spins = 0; spins < (1u << 15); ++spins) {
    if (__all(v == tag)) break;
    __builtin_amdgcn_s_sleep(2);
    v = __hip_atomic_load(f, RLX_AGENT);
  }
  __builtin_amdgcn_s_waitcnt(0x0F70);
  __builtin_amdgcn_fence(__ATOMIC_ACQUIRE, "wavefront");
  asm volatile("" ::: "memory");
}

struct Ctx {
  char* ws; int b, g;
  DI Ctx fresh() const { Ctx c = *this; asm volatile("" : "+s"(c.b), "+s"(c.g)); return c; }
  DI gu64* epoch() const { return (gu64*)ws; }
  DI gu64* tflags() const { return (gu64*)(ws + WS_TF + g * 256); }
  DI gu64* xflag(int dst) const { return (gu64*)(ws + WS_XF + (b * 8 + dst) * 128); }
  DI rsrc_t rpx(int dst) const { return mkrsrc(ws + WS_PX + (size_t)(b * 8 + dst) * 143360, (ABL & 64) ? 0 : 143360); }
  DI rsrc_t rwt() const { return mkrsrc(ws + WS_WT + (size_t)g * 655360, 655360); }
  DI rsrc_t rqt() const { return mkrsrc(ws + WS_QT + (size_t)g * 491520, 491520); }
};
template <int NQ> struct AttnState {
  u32x4 q[NQ][4], fr[8], p[2][NQ];
  f32x4 o[8][NQ], negm[NQ], sc[2][4][NQ];
  float l[NQ], rs[NQ], inv[NQ];
  unsigned ka[4], va[8], kvo, vvo, pa, pb, pg, oad, last;
};

DI int qreg(int r) { return r == 3 ? RC : ((r & 1) ? RB : RA); }
template <int NQ> DI void sm_pair(AttnState<NQ>& a, const f32x4 (&x)[4][NQ], u32x4 (&p)[NQ], const int hf, const int n) {
  const int t = n >> 2, d = n & 3;
  if (t >= NQ) return;
  const float e0 = __builtin_amdgcn_exp2f(x[2 * hf + (d >> 1)][t][2 * (d & 1)]), e1 = __builtin_amdgcn_exp2f(x[2 * hf + (d >> 1)][t][2 * (d & 1) + 1]);
  a.rs[t] = d ? (a.rs[t] + e0) + e1 : e0 + e1;
  p[t][d] = cvtpk(e0, e1);
  PIN(p[t][d]); PIN(a.rs[t]);
}
template <int NQ, int HF, bool ADJ>
DI void fixup(AttnState<NQ>& a, f32x4 (&x)[4][NQ], f32x4 (&y)[4][NQ]) {
#pragma unroll
  for (int t = 0; t < NQ; ++t) {
    float mx = x[3][t][3];
#pragma unroll
    for (int i = 8 * HF; i < 15; ++i) mx = fmaxf(mx, x[i >> 2][t][i & 3]);
    const float dl = __builtin_ceilf(fmaxf(xmax(mx), 0.f)), sc = __builtin_amdgcn_exp2f(-dl);
    a.l[t] *= sc; a.negm[t] -= dl;
#pragma unroll
    for (int dt = 0; dt < 8; ++dt) a.o[dt][t] *= sc;
    if (HF) {
#pragma unroll
      for (int d = 0; d < 4; ++d) { const unsigned v = a.p[0][t][d]; a.p[0][t][d] = cvtpk(__uint_as_float(v << 16) * sc, __uint_as_float(v & 0xffff0000u) * sc); }
    }
#pragma unroll
    for (int kt = 2 * HF; kt < 4; ++kt) x[kt][t] -= dl;
    float r = 0.f;
#pragma unroll
    for (int d = 0; d < 4; ++d) { const float e0 = __builtin_amdgcn_exp2f(x[2 * HF + (d >> 1)][t][2 * (d & 1)]), e1 = __builtin_amdgcn_exp2f(x[2 * HF + (d >> 1)][t][2 * (d & 1) + 1]);
      r += e0 + e1; a.p[HF][t][d] = cvtpk(e0, e1); }
    a.rs[t] = r;
    if (ADJ) {
#pragma unroll
      for (int kt = 0; kt < 4; ++kt) y[kt][t] -= dl;
    }
  }
}
template <int NQ, int HF, bool CHECK, bool ADJ>
DI void sm_close(AttnState<NQ>& a, f32x4 (&x)[4][NQ], f32x4 (&y)[4][NQ]) {
  if (CHECK) {
    if (NQ == 1) PIN(a.p[HF][0]);
    float m = NQ == 2 ? a.rs[0] + a.rs[NQ - 1] : a.rs[0]; PIN(m);
    if (__builtin_expect(__any(!(m <= PLIM)), 0)) fixup<NQ, HF, ADJ>(a, x, y);
  }
#pragma unroll
  for (int t = 0; t < NQ; ++t) { PIN(a.rs[t]); a.l[t] += a.rs[t]; PIN(a.l[t]); }
}
template <int NQ> DI u32x4 o_frag(const AttnState<NQ>& a, const int m, const int t) {
  const f32x4 x = a.o[2 * m][t] * a.inv[t], y = a.o[2 * m + 1][t] * a.inv[t];
  return u32x4{cvtpk(x[0], x[1]), cvtpk(x[2], x[3]), cvtpk(y[0], y[1]), cvtpk(y[2], y[3])};
}
template <bool V, int NQ> DI void frag_read(AttnState<NQ>& a, const int m, const int par) {
  if (!V) a.fr[m & 7] = lds128(lp(a.ka[m & 3]) + par * SLOT + (m >> 2) * 4096);
  else { lptr p = lp(a.va[m & 7]) + par * SLOT + (m >> 3) * 8192; const u32x2 v0 = ldstr(p), v1 = ldstr(p + 4096); a.fr[m & 7] = u32x4{v0[0], v0[1], v1[0], v1[1]}; }
}
template <bool V, int NQ> DI void tile_issue(const AttnState<NQ>& a, rsrc_t r, const unsigned gsrc, const unsigned dst, const unsigned st = 4096) {
  if (NODMA) return;
#pragma unroll
  for (int i = 0; i < 4; ++i) {
    if (LCAP) asm volatile("s_waitcnt vmcnt(%0)" :: "i"(LCAP - 4) : "memory");
    dma_nt<0, V>(r, V ? a.vvo : a.kvo, gsrc + i * 4096, dst + i * st);          dma_nt<1024, V>(r, V ? a.vvo ^ 128 : a.kvo ^ 64, gsrc + i * 4096, dst + i * st);
    dma_nt<2048, V>(r, V ? a.vvo : a.kvo ^ 128, gsrc + i * 4096, dst + i * st); dma_nt<3072, V>(r, V ? a.vvo ^ 128 : a.kvo ^ 192, gsrc + i * 4096, dst + i * st);
  }
}
template <bool CV, int NQ> DI void phase_io(AttnState<NQ>& a, const int i, const int cp, const int np, const unsigned val, const int w, lptr lds) {
  if (i <= 10) frag_read<CV>(a, i + 5, cp);
  if (NQ == 2 && i == 10) {
    SB();
    prog_post(lds + APROG + w * 4, val);
    a.pg = prog_read<0>(a.pb);
  }
  if (NQ == 2 && i == 12) prog_wait<0, 0>(a.pb, a.pg, val);
  if (i == 13 || i == 14) { frag_read<!CV>(a, 2 * (i - 13), np); frag_read<!CV>(a, 2 * (i - 13) + 1, np); }
  if (i == 15) frag_read<!CV>(a, 4, np);
}
template <bool V, int NQ> DI void loader_turn(AttnState<NQ>& a, const unsigned val, lptr lds, rsrc_t r, const unsigned gsrc, const unsigned dst) {
  if (NQ == 1) {
    WAITVM(16);
    prog_post(lds + APROG + 12, val + 1);
    prog_wait<0, 1>(a.pb, val);
    if (RPACE) {
      asm volatile("1:\n\ts_memrealtime vcc\n\ts_waitcnt lgkmcnt(0)\n\ts_sub_u32 vcc_hi, vcc_lo, %0\n\ts_cmp_lt_u32 vcc_hi, %1\n\ts_cbranch_scc1 1b\n\ts_mov_b32 %0, vcc_lo"
                   : "+s"(a.last) : "i"(RPACE) : "memory", "scc", "vcc");
    }
    tile_issue<V>(a, r, gsrc, gsrc < 32 * SLOT ? dst : (unsigned)(uintptr_t)lds + DUMMY, gsrc < 32 * SLOT ? 4096u : 0u);
    SB();
  }
}
template <int KIND, int PAR, int NQ>
DI void attn_step(AttnState<NQ>& a, const int s, const int w, const int lane, lptr lds, const unsigned l0, rsrc_t rk, rsrc_t rv) {
  const f32x4 zero = {0.f, 0.f, 0.f, 0.f};
  f32x4 (&c)[4][NQ] = a.sc[PAR]; f32x4 (&d)[4][NQ] = a.sc[PAR ^ 1];
  SB();
  if (KIND == 1) {
    prog_wait<16, 1>(a.pa, s == 0 ? 0u : (s < 24 ? P_Q12 : P_Q3));
    const unsigned qa = l0 + qreg(s >> 3) + 2 * w * 4096 + lane * 16;
#pragma unroll
    for (int i = 0; i < 4; ++i)
#pragma unroll
      for (int t = 0; t < NQ; ++t) a.q[t][i] = lds128(lp(qa) + t * 4096 + i * 1024);
    a.oad = qa;
    SB();
  }
#pragma unroll
  for (int i = 0; i < 16; ++i) {
    constexpr int PA[16] = {-1, 0, 4, -1, 1, 5, -1, 2, 6, -1, -1, 3, 7, -1, -1, -1};
#pragma unroll
    for (int t = 0; t < NQ; ++t) {
      c[i >> 2][t] = mfma(a.fr[i & 7], a.q[t][i & 3], (i & 3) ? c[i >> 2][t] : (KIND == 1 ? zero : a.negm[t]));
      if (t == 0) { if (PA[i] >= 0) sm_pair(a, d, a.p[1], 1, PA[i]); SB(); }
    }
    if (i == 12) PIN(c[3][NQ - 1]);
    phase_io<false>(a, i, PAR, PAR ^ 1, 2 * s + 1, w, lds);
    if (KIND == 2 && i >= 8 && ((i - 8) >> 2) < NQ) *(LAS u32x4*)(lp(a.oad) + ((i - 8) >> 2) * 4096 + (i & 3) * 1024) = o_frag(a, i & 3, ((i - 8) >> 2) % NQ);
    SB();
  }
  if (KIND == 2) prog_post(lds + QPROG + w * 4, s);
  loader_turn<false>(a, 2 * s + 1, lds, rk, (s + 2) * SLOT, l0 + PAR * SLOT);
  sm_close<NQ, 1, KIND != 2, KIND == 0>(a, d, c);
  if (KIND == 1) {
#pragma unroll
    for (int t = 0; t < NQ; ++t) { a.inv[t] = __builtin_amdgcn_rcpf(xsum(a.l[t])); a.l[t] = 0.f; }
  }
  SB();
  u32x4 pn[NQ];
#pragma unroll
  for (int i = 0; i < 16; ++i) {
    constexpr int PB[16] = {-1, -1, -1, 0, -1, 4, -1, 1, -1, 5, -1, 2, 6, -1, 3, 7};
#pragma unroll
    for (int t = 0; t < NQ; ++t) {
      a.o[i & 7][t] = mfma(a.fr[i & 7], a.p[i >> 3][t], (KIND == 2 && i < 8) ? zero : a.o[i & 7][t]);
      if (t == 0) { if (PB[i] >= 0) sm_pair(a, c, pn, 0, PB[i]); SB(); }
    }
    if (i == 12) PIN(a.o[4][NQ - 1]);
    phase_io<true>(a, i, PAR ^ 1, PAR ^ 1, 2 * s + 2, w, lds);
    if (KIND == 1 && (i == 1 || i == 2) && i - 1 < NQ) {
      const int t = (i - 1) % NQ;
      float m0 = c[3][t][3];
#pragma unroll
      for (int e = 0; e < 15; ++e) m0 = fmaxf(m0, c[e >> 2][t][e & 3]);
      m0 = xmax(m0);
#pragma unroll
      for (int kt = 0; kt < 4; ++kt) { c[kt][t] -= m0; PIN(c[kt][t]); }
      a.negm[t] = f32x4{-m0, -m0, -m0, -m0}; PIN(a.negm[t]);
    }
    SB();
  }
#pragma unroll
  for (int t = 0; t < NQ; ++t) a.p[0][t] = pn[t];
  loader_turn<true>(a, 2 * s + 2, lds, rv, (s + 1) * SLOT, l0 + VOFF + (PAR ^ 1) * SLOT);
  sm_close<NQ, 0, KIND != 1, false>(a, c, c);
}

template <int NQ>
DI void attn_main(const int w, lptr lds, const unsigned l0, rsrc_t rk, rsrc_t rv, const Ctx& c0, int& nst) {
  const int lane = fresh_lane(), lg = lane >> 4, lr = lane & 15;
  AttnState<NQ> a;
  a.kvo = lg * 256 + ((lr ^ lg) << 4);
  a.vvo = lg * 256 + ((lr ^ (lg << 1)) << 4);
  if (NQ == 1) { tile_issue<false>(a, rk, 0, l0); }
  BAR_L();
  if (NQ == 1) { tile_issue<false>(a, rk, SLOT, l0 + SLOT); }
  BAR_L();
  if (NQ == 1) { tile_issue<true>(a, rv, 0, l0 + VOFF); prog_post(lds + APROG + 12, 1); __builtin_amdgcn_s_setprio(LPRIO); }
#pragma unroll
  for (int i = 0; i < 4; ++i) { a.ka[i] = l0 + lr * 256 + (((4 * i + lg) ^ lr) << 4); PIN(a.ka[i]); }
#pragma unroll
  for (int i = 0; i < 8; ++i) { a.va[i] = l0 + VOFF + (lane >> 2) * 256 + ((i ^ ((lane >> 2) & 7)) << 5) + (lane & 3) * 8; PIN(a.va[i]); }
  a.pg = 0; a.pa = l0 + APROG + (lane & 3) * 4; a.pb = l0 + APROG + (NQ == 2 ? 12 : ((lane & 3) == 3 ? 0 : (lane & 3) * 4)); a.oad = 0; a.last = RPACE ? (unsigned)__builtin_amdgcn_s_memrealtime() - 1000u : 0u;
#pragma unroll
  for (int t = 0; t < NQ; ++t) {
    a.l[t] = 0.f; a.inv[t] = 0.f; a.rs[t] = 0.f; a.p[0][t] = a.p[1][t] = u32x4{0u, 0u, 0u, 0u}; a.negm[t] = f32x4{0.f, 0.f, 0.f, 0.f};
#pragma unroll
    for (int i = 0; i < 8; ++i) { a.o[i][t] = f32x4{0.f, 0.f, 0.f, 0.f}; a.sc[1][i & 3][t] = f32x4{0.f, 0.f, 0.f, 0.f}; }
  }
  WAITVM(32);
  BAR_L(); ST();
#pragma unroll
  for (int m = 0; m < 5; ++m) frag_read<false>(a, m, 0);
#pragma nounroll
  for (int r = 0; r < 4; ++r) {
    attn_step<1, 0>(a, 8 * r, w, lane, lds, l0, rk, rv); ST();
    attn_step<2, 1>(a, 8 * r + 1, w, lane, lds, l0, rk, rv); ST();
#pragma nounroll
    for (int t = 2; t < 8; t += 2) {
      attn_step<0, 0>(a, 8 * r + t, w, lane, lds, l0, rk, rv); ST();
      attn_step<0, 1>(a, 8 * r + t + 1, w, lane, lds, l0, rk, rv); ST();
    }
  }
  {
#pragma unroll
    for (int i = 0; i < 8; ++i) sm_pair(a, a.sc[1], a.p[1], 1, i);
    if (NQ == 2) prog_wait<0, 0>(a.pb, 65);
#pragma unroll
    for (int m = 0; m < 8; ++m) frag_read<true>(a, m, 1);
    sm_close<NQ, 1, true, false>(a, a.sc[1], a.sc[1]);
    SB();
#pragma unroll
    for (int i = 0; i < 16; ++i) {
#pragma unroll
      for (int t = 0; t < NQ; ++t) a.o[i & 7][t] = mfma(a.fr[i & 7], a.p[i >> 3][t], a.o[i & 7][t]);
      if (i < 8) frag_read<true>(a, i + 8, 1);
      SB();
    }
#pragma unroll
    for (int t = 0; t < NQ; ++t) a.inv[t] = __builtin_amdgcn_rcpf(xsum(a.l[t]));
  }
#pragma unroll
  for (int t = 0; t < NQ; ++t)
#pragma unroll
    for (int m = 0; m < 4; ++m) *(LAS u32x4*)(lds + OWN3 + (2 * w + t) * 4096 + m * 1024 + lane * 16) = o_frag(a, m, t);
  prog_post(lds + QPROG + w * 4, 33);
  __builtin_amdgcn_s_setprio(0);
  ST();
}

DI void attn_wave(const int w, const int lane, const int rot, lptr lds, const unsigned l0, rsrc_t rk, rsrc_t rv, rsrc_t rx, rsrc_t rwq, const char* wo, const Ctx& c0, int& nst) {
  const int lg = lane >> 4, lr = lane & 15;
  ST();
  {
    const unsigned wvo = lg * 8192 + ((lr ^ (lg << 2)) << 4) + c0.g * 1024;
    const unsigned xvo = (lane >> 3) * 1280 + (((lane & 7) ^ ((4 * (w & 1) + lg) & 7)) << 4);
    auto issue = [&](int c) {
      const int cr = c + rot >= 10 ? c + rot - 10 : c + rot;
      const unsigned wd = l0 + VOFF + (c & 3) * PBUF + w * 1024, xd = wd + SLOT, xs = w * 10240 + cr * 128, ws_ = (cr * 64 + 4 * w) * 8192;
#pragma unroll
      for (int i = 0; i < 3; ++i) dma_df(rx, xvo, xs + i * 40960, xd + i * 4096);
      dma_df(rx, xvo, xs + 3 * 40960, w < 2 ? xd + 3 * 4096 : l0 + DUMMY);
#pragma unroll
      for (int i = 0; i < 4; ++i) dma_df(rwq, wvo, ws_ + i * 131072, wd + i * 4096);
    };
    issue(0); issue(1); issue(2);
    if (w == 0) { *(LAS u32x4*)(lds + CNT + (lane & 7) * 16) = u32x4{0u, 0u, 0u, 0u}; }
    const int b = c0.b, g = c0.g; const rsrc_t rwo = mkrsrc(wo, 4096 * 1280);
    u32x2 rw[8], rq2[8]; unsigned short r5[8]; unsigned long long e1;
    const unsigned rb = 4 * lg * 1280 + (b >> 4) * 640, wv0 = rb + w * 128 + lr * 8, wv1 = rb + 512 + w * 32 + lr * 2;
    const unsigned s2 = ((4 * g + ((b >> 2) & 3)) * 128 + (b & 3) * 32) * 1280;
    const int ti = (b - 20) * 4 + w, tk = b < 20 ? b : ti >> 1, tu = b < 20 ? w : 4 + (ti & 1);
    const unsigned qv = lg * 8 * 8192 + lr * 8 + g * 1024 + 256, sq = tk * 32 * 8192 + tu * 128;
    constexpr int TN[13] = {0, 0, 0, TN0};
#pragma unroll
    for (int c = 0; c < 9; ++c) {
      SB(); asm volatile("s_waitcnt vmcnt(%0) lgkmcnt(0)\n\ts_barrier" :: "i"((c < 8 ? 16 : 8) + TN[c] + TN[c + 1] + TN[c + 2]) : "memory"); SB();
      if (c == TC + 4) { _Pragma("unroll") for (int i = 0; i < 8; ++i) PIN(rq2[i]); }
      if (c == TC + 6) { _Pragma("unroll") for (int i = 0; i < 8; ++i) { PIN(rw[i]); PIN(r5[i]); } }
      SB();
      if (c < 7) issue(c + 3);
      SB();
      if (c == TC) {
        e1 = __hip_atomic_load(c0.epoch(), RLX_AGENT) + 1;
#pragma unroll
        for (int jj = 0; jj < 8; ++jj) rq2[jj] = __builtin_amdgcn_raw_buffer_load_b64(rwq, qv, sq + jj * 8192, 0);
      }
      if (c == TC + 1) {
#pragma unroll
        for (int jj = 0; jj < 8; ++jj) rw[jj] = __builtin_amdgcn_raw_buffer_load_b64(rwo, wv0 + (jj & 3) * 1280, s2 + (jj >> 2) * 16 * 1280, 0);
      }
      if (c == TC + 2) {
#pragma unroll
        for (int jj = 0; jj < 8; ++jj) r5[jj] = __builtin_amdgcn_raw_buffer_load_b16(rwo, wv1 + (jj & 3) * 1280, s2 + (jj >> 2) * 16 * 1280, 0);
      }
      if (c == TC + 4) {
#pragma unroll
        for (int e = 0; e < 4; ++e) { u32x4 f;
#pragma unroll
          for (int pp = 0; pp < 4; ++pp) f[pp] = __builtin_amdgcn_perm(rq2[2 * pp + 1][e >> 1], rq2[2 * pp][e >> 1], (e & 1) ? 0x07060302u : 0x05040100u);
          __builtin_amdgcn_raw_buffer_store_b128(f, c0.rqt(), (tk * 6 + tu) * 4096 + e * 1024 + lane * 16, 0, 16); }
      }
      if (c == TC + 6) {
#pragma unroll
        for (int e = 0; e < 5; ++e) { u32x4 f;
#pragma unroll
          for (int pp = 0; pp < 4; ++pp) f[pp] = e < 4 ? __builtin_amdgcn_perm(rw[2 * pp + 1][(e >> 1) & 1], rw[2 * pp][(e >> 1) & 1], (e & 1) ? 0x07060302u : 0x05040100u)
                                                       : __builtin_amdgcn_perm((unsigned)r5[2 * pp + 1], (unsigned)r5[2 * pp], 0x05040100u);
          __builtin_amdgcn_raw_buffer_store_b128(f, c0.rwt(), (b * 4 + w) * 5120 + e * 1024 + lane * 16, 0, 16); }
      }
    }
    {
      const unsigned long long tag = ((unsigned long long)rfl((unsigned)(e1 >> 32)) << 32) | rfl((unsigned)e1);
      *(LAS unsigned long long*)(lds + TAGW) = tag;
      publish(c0.tflags() + b, lds + CNT + 16, tag, lane);
    }
  }
  if (w < 3) attn_main<2>(w, lds, l0, rk, rv, c0, nst); else attn_main<1>(w, lds, l0, rk, rv, c0, nst);
}

constexpr int LAG = 3;
DI void parrive(unsigned& pc, const int j, lptr lds) { WAITL(); ++pc; prog_post(lds + PPROG + j * 4, pc); }
template <int NE> constexpr int efrag(int e) { return NE == 5 ? e : (NE == 3 ? (e < 2 ? e : 4) : e + 2); }
template <int NE> DI u32x4 wload(rsrc_t rwt, const unsigned lv, const unsigned so, const int e) {
  return __builtin_amdgcn_raw_buffer_load_b128(rwt, lv + (efrag<NE>(e) & 3) * 1024, so + (efrag<NE>(e) >> 2) * 4096, 16);
}
DI void send_flag(const Ctx& c, const int dst, const int part, const unsigned lv, lptr lds) {
  const unsigned long long tag = *(const LAS unsigned long long*)(lds + TAGW);
  asm volatile("s_waitcnt vmcnt(0)" ::: "memory");
  if (lv == 0) __hip_atomic_store(c.xflag(dst) + c.g + part, tag, RLX_AGENT);
}
template <int NE>
DI void oproj_pass(const int j, const bool pstv, const Ctx& c0, lptr lds, const unsigned l0, int& nst) {
  const int lane = fresh_lane();
  const Ctx c = c0.fresh();
  const int mrot = ROT ? c.b : 0, side = (c.g ^ (NE != 5)) & 1;
  const rsrc_t rwt = c.rwt();
  const unsigned pa = l0 + APROG + (lane & 3) * 4, lv = lane * 16, tb = (side * 64 + j) * 5120;
  f32x4 acc[NE][7]; u32x4 af[4][NE], bf[4];
#pragma unroll
  for (int k = 0; k < 3; ++k)
#pragma unroll
    for (int e = 0; e < NE; ++e) { af[k][e] = wload<NE>(rwt, lv, tb + ((k + mrot) & 3) * 20480, e); SB(); }
#pragma unroll
  for (int e = 0; e < NE; ++e)
#pragma unroll
    for (int t = 0; t < 7; ++t) { acc[e][t] = f32x4{0.f, 0.f, 0.f, 0.f}; PIN(acc[e][t]); }
  asm volatile("s_nop 1"); SB();
#pragma nounroll
  for (int r = 0; r < 3; ++r) {
    const unsigned reg = qreg(r + 1);
    if (NE != 2) prog_wait<32, 2>(pa, 8 * r + 9);
    if (NE == 2 && r == 2) send_flag(c0.fresh(), 2 * j + (side ^ 1), 0, lv, lds);
    PST();
    auto baddr = [&](int gi) { return lp(l0 + reg + ((gi / 7 + mrot) & 3) * 1024 + lv) + (gi % 7) * 4096; };
#pragma unroll
    for (int gi = 0; gi < LAG; ++gi) bf[gi] = lds128(baddr(gi));
    SB();
#pragma unroll
    for (int k = 0; k < 4; ++k) {
      int r2 = r; asm volatile("" : "+s"(r2));
      const unsigned so = tb + ((k ? r2 + 1 : r2) * 4 + ((k + 3 + mrot) & 3)) * 20480;
#pragma unroll
      for (int t = 0; t < 7; ++t) {
        const int gi = 7 * k + t;
#pragma unroll
        for (int e = 0; e < NE; ++e) {
          mfma_ip(acc[e][t], af[k][e], bf[gi & 3]);
          if (e == 0 && gi + LAG < 28) bf[(gi + LAG) & 3] = lds128(baddr(gi + LAG));
          if (e == NE / 2 && t >= 1 && t <= NE) af[(k + 3) & 3][t - 1] = wload<NE>(rwt, lv, so, t - 1);
          SB();
        }
      }
    }
    PST();
  }
#pragma unroll
  for (int e = 0; e < NE; ++e) af[3][e] = wload<NE>(rwt, lv, tb + (12 + ((3 + mrot) & 3)) * 20480, e);
  if (NE == 5 && WARM) {
    prog_wait<0, 8>(pa, WARM);
#pragma unroll
    for (int i = 0; i < 10; ++i) dma_sc1(rwt, lv, (side ^ 1) * 327680 + ((c.b & 7) * 40 + j * 10 + i) * 1024, l0 + DUMMY);
  }
  if (NE != 2) prog_wait<32, 1>(pa, 33);
  if (NE == 5) __builtin_amdgcn_s_setprio(2);
  PST();
  const Ctx c2 = c0.fresh(); const int dst = 2 * j + side;
  const bool keep = NE == 5 && dst == c2.g;
  const rsrc_t rpx = c2.rpx(dst); const unsigned po = c2.g * 17920;
  unsigned ba[4];
#pragma unroll
  for (int k = 0; k < 4; ++k) ba[k] = l0 + OWN3 + ((k + mrot) & 3) * 1024 + lv;
  auto out = [&](int t, int part) {
    if (keep) { *(LAS f32x4*)(lp(l0 + OWNP + lv) + (t * 5 + part) * 1024) = acc[part % NE][t]; if (part == 3) *(LAS f32x4*)(lp(l0 + OWNP + lv) + (t * 5 + 4) * 1024) = acc[NE - 1][t]; }
    else if (part < NE / 2) {
      const f32x4 x = acc[2 * part][t], y = acc[2 * part + 1][t];
      __builtin_amdgcn_raw_buffer_store_b128(u32x4{cvtpk(x[0], y[0]), cvtpk(x[1], y[1]), cvtpk(x[2], y[2]), cvtpk(x[3], y[3])}, rpx, po + lv + t * 2560 + efrag<NE>(2 * part) * 512, 0, 16);
    } else if (part == 2 && (NE & 1)) { const f32x4 x = acc[NE - 1][t];
      __builtin_amdgcn_raw_buffer_store_b64(u32x2{cvtpk(x[0], x[1]), cvtpk(x[2], x[3])}, rpx, po + (lv >> 1) + t * 2560 + 2048, 0, 16); }
  };
#pragma unroll
  for (int gi = 0; gi < LAG; ++gi) bf[gi] = lds128(lp(ba[gi & 3]) + (gi >> 2) * 4096);
  SB();
#pragma unroll
  for (int gi = 0; gi < 28; ++gi) {
    const int t = gi >> 2, k = gi & 3;
#pragma unroll
    for (int e = 0; e < NE; ++e) {
      mfma_ip(acc[e][t], af[k][e], bf[gi & 3]);
      if (e == 0 && gi + LAG < 28) bf[(gi + LAG) & 3] = lds128(lp(ba[(gi + LAG) & 3]) + ((gi + LAG) >> 2) * 4096);
      if (e == NE - 1 && k == 0 && t > 0) {
        asm volatile("s_nop %0" :: "i"(8 - NE));
#pragma unroll
        for (int e2 = 0; e2 < NE; ++e2) PIN(acc[e2][t - 1]);
      }
      if (e == NE - 1 && t > 0) out(t - 1, k);
      SB();
    }
  }
  MFMA_DONE();
#pragma unroll
  for (int e2 = 0; e2 < NE; ++e2) PIN(acc[e2][6]);
  SB();
#pragma unroll
  for (int k = 0; k < 4; ++k) out(6, k);
  if (NE == 5) __builtin_amdgcn_s_setprio(TPRIO);
  if (keep) prog_post(lds + KPROG, 1);
  if (NE != 5) { send_flag(c2, dst, NE == 2 ? 8 : 0, lv, lds); __builtin_amdgcn_s_waitcnt(0x0F70); }
  PST();
}

DI void oproj_res(const int j, const Ctx& c0, lptr lds, const unsigned l0, int& nst) {
  const int lane = fresh_lane();
  const bool pstv = j == 0; (void)pstv;
  const Ctx c = c0.fresh();
  const int side = (c.g ^ 1) & 1, dst = 2 * j + side;
  const rsrc_t rwt = c.rwt(), rpx = c.rpx(dst);
  const unsigned lv = lane * 16, tb = (side * 64 + j) * 5120, po = c.g * 17920 + 1024 + lv;
  f32x4 acc[2][7]; u32x4 af[16][2], bf[4];
#pragma unroll
  for (int i = 0; i < 16; ++i)
#pragma unroll
    for (int e = 0; e < 2; ++e) { af[i][e] = wload<2>(rwt, lv, tb + i * 20480, e); SB(); }
  auto brd = [&](int n) { const int r = n / 28, gi = n % 28, k = r < 3 ? gi / 7 : gi & 3, t = r < 3 ? gi % 7 : gi >> 2;
    const int off = (r == 0 ? RB : (r == 1 ? RA : (r == 2 ? RC : OWN3))) + t * 4096 + k * 1024;
    bf[n & 3] = lds128(lp(l0 + lv + (off & ~0xFFFF)) + (off & 0xFFFF)); };
  auto out = [&](int t) { const f32x4 x = acc[0][t], y = acc[1][t];
    __builtin_amdgcn_raw_buffer_store_b128(u32x4{cvtpk(x[0], y[0]), cvtpk(x[1], y[1]), cvtpk(x[2], y[2]), cvtpk(x[3], y[3])}, rpx, po + t * 2560, 0, 16); };
#pragma unroll
  for (int t = 0; t < 7; ++t) { acc[0][t] = acc[1][t] = f32x4{0.f, 0.f, 0.f, 0.f}; PIN(acc[0][t]); PIN(acc[1][t]); }
#pragma unroll
  for (int n = 0; n < LAG; ++n) brd(n);
  asm volatile("s_nop 1"); SB();
  auto head = [&](auto R) {
#pragma unroll
    for (int gi = 0; gi < 28; ++gi) {
      constexpr int r = decltype(R)::value;
      const int n = 28 * r + gi, k = r < 3 ? gi / 7 : gi & 3, t = r < 3 ? gi % 7 : gi >> 2;
      if (n == 28) send_flag(c0.fresh(), 2 * j + (side ^ 1), 0, lv, lds);
      mfma_ip(acc[0][t], af[4 * r + k][0], bf[n & 3]);
      if (n + LAG < 112) brd(n + LAG);
      SB();
      mfma_ip(acc[1][t], af[4 * r + k][1], bf[n & 3]);
      if (r == 3 && k == 1 && t > 0) { asm volatile("s_nop 3"); PIN(acc[0][t - 1]); PIN(acc[1][t - 1]); out(t - 1); }
      SB();
    }
    PST();
  };
  head(IC<0>{}); head(IC<1>{}); head(IC<2>{}); head(IC<3>{});
  MFMA_DONE(); PIN(acc[0][6]); PIN(acc[1][6]); SB();
  out(6);
  send_flag(c0.fresh(), dst, 8, lv, lds);
  PST();
}

DI void final_part(const int j, const Ctx& c0, lptr lds, const unsigned l0, char* out, int& nst) {
  const Ctx c = c0.fresh(); const int b = c.b, g = c.g; const rsrc_t rpx = c.rpx(g);
  const int lane = fresh_lane(), p = g >> 1, lg = lane >> 4, lr = lane & 15;
  const bool pstv = false; (void)pstv;
  const unsigned long long tg = *(const LAS unsigned long long*)(lds + TAGW), tag = ((unsigned long long)rfl((unsigned)(tg >> 32)) << 32) | rfl((unsigned)tg);
  auto lo = [](unsigned v) { return __uint_as_float(v << 16); };
  auto hi = [](unsigned v) { return __uint_as_float(v & 0xffff0000u); };
  f32x4 a[2][5];
#pragma unroll
  for (int st = 0; st < 3; ++st) {
    const int ns = st ? 4 : 3;
    {
      const int k = lane & 3;
      gu64* f = c.xflag(g) + (st ? 2 * k + 1 - (g & 1) + (st == 2 ? 8 : 0) : 2 * (k == p ? (p + 1) & 3 : k) + (g & 1));
      wait_flag(f, __hip_atomic_load(f, RLX_AGENT), tag);
    }
    u32x4 in[2][4][2]; u32x2 in2[2][4];
#pragma unroll
    for (int i = 0; i < 2; ++i)
#pragma unroll
      for (int k = 0; k < ns; ++k) {
        const int src = st ? 2 * k + 1 - (g & 1) : 2 * (k + (k >= p)) + (g & 1);
        const unsigned o = (i == 1 && j == 3) ? OOB : src * 17920 + (j + 4 * i) * 2560;
        if (st != 2) in[i][k][0] = __builtin_amdgcn_raw_buffer_load_b128(rpx, lane * 16 + o, 0, 16);
        if (st != 1) in[i][k][1] = __builtin_amdgcn_raw_buffer_load_b128(rpx, lane * 16 + o + 1024, 0, 16);
        if (st != 2) in2[i][k] = __builtin_amdgcn_raw_buffer_load_b64(rpx, lane * 8 + o + 2048, 0, 16);
      }
    if (st == 0) {
      prog_wait<0, 1>(l0 + KPROG, 1);
#pragma unroll
      for (int i = 0; i < 2; ++i)
#pragma unroll
        for (int e = 0; e < 5; ++e) a[i][e] = *(LAS f32x4*)(lds + OWNP + (((i == 1 && j == 3) ? 0 : j + 4 * i) * 5 + e) * 1024 + lane * 16);
    }
#pragma unroll
    for (int i = 0; i < 2; ++i)
#pragma unroll
      for (int k = 0; k < ns; ++k) {
#pragma unroll
        for (int d = (st == 2 ? 4 : 0); d < (st == 1 ? 4 : 8); ++d) { const int reg = d & 3, e = 2 * (d >> 2); a[i][e][reg] += lo(in[i][k][d >> 2][d & 3]); a[i][e + 1][reg] += hi(in[i][k][d >> 2][d & 3]); }
        if (st != 2) {
#pragma unroll
          for (int d = 0; d < 2; ++d) { a[i][4][2 * d] += lo(in2[i][k][d]); a[i][4][2 * d + 1] += hi(in2[i][k][d]); }
        }
      }
    PST();
  }
  if (j == 0 && g == 0 && b == 0 && lane == 0) __hip_atomic_store(c.epoch(), tag, RLX_AGENT);
  const rsrc_t rout = mkrsrc(out + (size_t)b * 143360, 143360);
#pragma unroll
  for (int i = 0; i < 2; ++i) {
    const unsigned ob = (i == 1 && j == 3) ? OOB : ((j + 4 * i) * 16 + lr) * 1280 + (g & 1) * 640;
#pragma unroll
    for (int hf = 0; hf < 2; ++hf)
      __builtin_amdgcn_raw_buffer_store_b128(u32x4{cvtpk(a[i][0][2 * hf], a[i][1][2 * hf]), cvtpk(a[i][2][2 * hf], a[i][3][2 * hf]), cvtpk(a[i][0][2 * hf + 1], a[i][1][2 * hf + 1]),
                                                   cvtpk(a[i][2][2 * hf + 1], a[i][3][2 * hf + 1])}, rout, ob + p * 128 + lg * 32 + hf * 16, 0, 0);
    __builtin_amdgcn_raw_buffer_store_b64(u32x2{cvtpk(a[i][4][0], a[i][4][1]), cvtpk(a[i][4][2], a[i][4][3])}, rout, ob + 512 + p * 32 + lg * 8, 0, 0);
  }
}

template <int NE> struct QpState { f32x4 acc[NE][7]; u32x4 xf[7]; lptr xa0, xa1; };
template <int NE> DI void qp_init(QpState<NE>& q, lptr xb, int lane) {
  const int lg = lane >> 4, lr = lane & 15;
  q.xa0 = xb + lr * 128 + ((lg ^ (lr >> 1)) << 4); q.xa1 = xb + lr * 128 + (((4 + lg) ^ (lr >> 1)) << 4);
#pragma unroll
  for (int t = 0; t < 7; ++t) { q.xf[t] = u32x4{0u, 0u, 0u, 0u}; PIN(q.xf[t]);
#pragma unroll
    for (int e = 0; e < NE; ++e) { q.acc[e][t] = f32x4{0.f, 0.f, 0.f, 0.f}; PIN(q.acc[e][t]); } }
  asm volatile("s_nop 1"); SB();
}
template <int PAR, int NE, bool DEEP>
DI void qp2_chunk(QpState<NE>& q, u32x4 (&af)[4][NE], const int c, const int j, const int rot, unsigned& pc, const unsigned pa, const unsigned xvo, const unsigned lv, const unsigned tb,
                  lptr lds, const unsigned l0, rsrc_t rx, rsrc_t rqt) {
  SB(); asm volatile("s_waitcnt vmcnt(%0) lgkmcnt(0)" :: "i"(DEEP ? 6 * NE + 8 : 2 * NE) : "memory"); SB();
  ++pc; prog_post(lds + PPROG + j * 4, pc);
  const unsigned pg = prog_read<16>(pa);
  const int c1 = c + 1 + rot >= 10 ? c + 1 + rot - 10 : c + 1 + rot, c2 = c + 2 + rot >= 10 ? c + 2 + rot - 10 : c + 2 + rot;
  const unsigned s1 = c < 9 ? (2 * c1 + 1) * 24576 + tb : OOB, s2 = c < 8 ? (2 * c2) * 24576 + tb : OOB;
  lptr x0 = q.xa0 + (DEEP ? (c & 2) * XCH : 0), x1 = q.xa1 + (DEEP ? (c & 2) * XCH : 0);
  SB();
#pragma unroll
  for (int u = 0; u < 14; ++u) {
    const int v = (u + 14 - LAG) % 14, t = v % 7, set = u < LAG ? 2 * (PAR ^ 1) + 1 : 2 * PAR + v / 7;
#pragma unroll
    for (int e = 0; e < NE; ++e) {
      mfma_ip(q.acc[e][t], af[set][e], q.xf[t]);
      if (e == 0 && u >= 2) q.xf[u % 7] = lds128((u >= 7 ? x1 : x0) + PAR * XCH + (u % 7) * 2048);
      if (e == NE / 2 && u >= LAG && u < LAG + NE) af[2 * (PAR ^ 1) + 1][u - LAG] = __builtin_amdgcn_raw_buffer_load_b128(rqt, lv + (u - LAG) * 1024, s1, 16);
      if (e == NE / 2 && u >= 10 && u < 10 + NE) af[2 * PAR][u - 10] = __builtin_amdgcn_raw_buffer_load_b128(rqt, lv + (u - 10) * 1024, s2, 16);
      SB();
    }
    if (u == 0) { prog_wait<16, 0>(pa, pg, pc); q.xf[0] = lds128(x0 + PAR * XCH); q.xf[1] = lds128(x0 + PAR * XCH + 2048); SB(); }
    if (u == 1) {
      const bool on = DEEP ? c < 8 : c < 9;
      const int cx = !DEEP ? c1 : (c + 3 + rot >= 10 ? c + 3 + rot - 10 : c + 3 + rot);
      const unsigned xs = on ? j * 10240 + cx * 128 : OOB, xd = l0 + (on ? (DEEP ? RB + ((c + 3) & 3) * XCH : RC + (PAR ^ 1) * XCH) + j * 1024 : DUMMY), st = on ? 4096u : 0u;
#pragma unroll
      for (int i = 0; i < 3; ++i) dma_df(rx, xvo, xs + i * 40960, xd + i * st);
      dma_df(rx, xvo, xs + 3 * 40960, j < 2 ? xd + 3 * st : l0 + DUMMY);
      SB();
    }
  }
}
template <int NE> DI void qp2_first(u32x4 (&af)[4][NE], const int rot, const unsigned lv, const unsigned tb, rsrc_t rqt) {
  const int r1 = rot == 9 ? 0 : rot + 1;
#pragma unroll
  for (int k = 0; k < 3; ++k)
#pragma unroll
    for (int e = 0; e < NE; ++e) { af[k][e] = __builtin_amdgcn_raw_buffer_load_b128(rqt, lv + e * 1024, (k < 2 ? 2 * rot + k : 2 * r1) * 24576 + tb, 16); SB(); }
#pragma unroll
  for (int e = 0; e < NE; ++e) { af[3][e] = u32x4{0u, 0u, 0u, 0u}; PIN(af[3][e]); }
}
template <int NE> DI void qp_tail(QpState<NE>& q, const u32x4 (&a)[NE]) {
#pragma unroll
  for (int t = 7 - LAG; t < 7; ++t)
#pragma unroll
    for (int e = 0; e < NE; ++e) mfma_ip(q.acc[e][t], a[e], q.xf[t]);
  MFMA_DONE(); SB();
}

DI void proj_wave(const int j, const int lane, const Ctx& c0, const int rot, lptr lds, const unsigned l0, rsrc_t rx, int& nst) {
  unsigned long long tag;
  const bool pstv = j == 0; (void)pstv;
  const int lg = lane >> 4, lr = lane & 15;
  {
    QpState<2> q;
    u32x4 af[2][2];
    af[1][0] = af[1][1] = u32x4{0u, 0u, 0u, 0u}; PIN(af[1][0]); PIN(af[1][1]);
    qp_init(q, lds + VOFF + SLOT, lane);
    const int q4 = lr >> 2;
    const unsigned wa = l0 + VOFF + (8 * lg + q4) * 256 + (((j ^ q4) * 4 + (lane & 3)) << 4);
#pragma nounroll
    for (int c = 0; c < 10; ++c) {
      const int bo = (c & 3) * PBUF;
      BAR_L();
#pragma unroll
      for (int u = 0; u < 14; ++u) {
        const int v = (u + 14 - LAG) % 14, t = v % 7, set = u < LAG ? 1 : v / 7;
        auto ard = [&](int ks, int e) { lptr p = lp(wa + bo) + ks * 8192 + e * 8; const u32x2 lo = ldstr(p), hi = ldstr(p + 1024); af[ks][e] = u32x4{lo[0], lo[1], hi[0], hi[1]}; };
        if (u == 0) { ard(0, 0); q.xf[0] = lds128(q.xa0 + bo); ard(0, 1); SB(); }
#pragma unroll
        for (int e = 0; e < 2; ++e) {
          mfma_ip(q.acc[e][t], af[set][e], q.xf[t]);
          if (e == 0 && u >= 1) q.xf[u % 7] = lds128((u >= 7 ? q.xa1 : q.xa0) + bo + (u % 7) * 2048);
          if (e == 1 && (u == 4 || u == 5)) ard(1, u - 4);
          SB();
        }
      }
      PST();
    }
    qp_tail(q, af[1]);
    BAR_L();
#pragma unroll
    for (int t = 0; t < 7; ++t)
      *(LAS u32x4*)(lds + RA + t * 4096 + j * 1024 + lane * 16) = u32x4{cvtpk(q.acc[0][t][0] * QSCALE, q.acc[0][t][1] * QSCALE), cvtpk(q.acc[0][t][2] * QSCALE, q.acc[0][t][3] * QSCALE),
                                                                        cvtpk(q.acc[1][t][0] * QSCALE, q.acc[1][t][1] * QSCALE), cvtpk(q.acc[1][t][2] * QSCALE, q.acc[1][t][3] * QSCALE)};
    { const unsigned long long tg = *(const LAS unsigned long long*)(lds + TAGW); tag = ((unsigned long long)rfl((unsigned)(tg >> 32)) << 32) | rfl((unsigned)tg); }
    BAR_L();
    PST();
  }
  unsigned pc = 0;
  {
    const Ctx c = c0.fresh();
    const rsrc_t rqt = c.rqt();
    const unsigned pa = l0 + APROG + (lane & 3) * 4, lv = lane * 16;
    const unsigned xvo = (lane >> 3) * 1280 + (((lane & 7) ^ ((4 * (j & 1) + lg) & 7)) << 4);
#pragma unroll
    for (int k = 0; k < 3; ++k) {
      const unsigned xs = j * 10240 + (k + rot >= 10 ? k + rot - 10 : k + rot) * 128, xd = l0 + RB + k * XCH + j * 1024;
#pragma unroll
      for (int i = 0; i < 3; ++i) dma_df(rx, xvo, xs + i * 40960, xd + i * 4096);
      dma_df(rx, xvo, xs + 3 * 40960, j < 2 ? xd + 3 * 4096 : l0 + DUMMY);
    }
    wait_flag(c.tflags() + (lane & 31), __hip_atomic_load(c.tflags() + (lane & 31), RLX_AGENT), tag);
    u32x4 a2[4][2];
    {
      QpState<4> q; u32x4 af[4][4];
      qp2_first(af, rot, lv, j * 4096, rqt);
      qp_init(q, lds + RB, lane);
      PST();
#pragma nounroll
      for (int cc = 0; cc < 10; cc += 2) {
        qp2_chunk<0, 4, true>(q, af, cc, j, rot, pc, pa, xvo, lv, j * 4096, lds, l0, rx, rqt);
        qp2_chunk<1, 4, true>(q, af, cc + 1, j, rot, pc, pa, xvo, lv, j * 4096, lds, l0, rx, rqt);
        PST();
      }
      qp_tail(q, af[3]);
      parrive(pc, j, lds);
      __builtin_amdgcn_s_setprio(XPRIO);
      u32x4 qf[7][2];
#pragma unroll
      for (int t = 0; t < 7; ++t)
#pragma unroll
        for (int hf = 0; hf < 2; ++hf) {
          qf[t][hf] = u32x4{cvtpk(q.acc[0][t][2 * hf] * QSCALE, q.acc[1][t][2 * hf] * QSCALE), cvtpk(q.acc[2][t][2 * hf] * QSCALE, q.acc[3][t][2 * hf] * QSCALE),
                            cvtpk(q.acc[0][t][2 * hf + 1] * QSCALE, q.acc[1][t][2 * hf + 1] * QSCALE), cvtpk(q.acc[2][t][2 * hf + 1] * QSCALE, q.acc[3][t][2 * hf + 1] * QSCALE)};
          PIN(qf[t][hf]);
        }
      prog_wait<16, 0>(pa, pc);
      prog_wait<32, 0>(pa, 1);
#pragma unroll
      for (int t = 0; t < 7; ++t)
#pragma unroll
        for (int hf = 0; hf < 2; ++hf) *(LAS u32x4*)(lds + ((j >> 1) ? RA : RB) + (4 * (j & 1) + lg) * 512 + lr * 16 + t * 4096 + hf * 256) = qf[t][hf];
      parrive(pc, j, lds);
      __builtin_amdgcn_s_setprio(0);
      qp2_first(a2, rot, lv, 16384 + j * 2048, rqt);
      PST();
    }
    QpState<2> q;
    qp_init(q, lds + RC, lane);
#pragma nounroll
    for (int cc = 0; cc < 10; cc += 2) {
      qp2_chunk<0, 2, false>(q, a2, cc, j, rot, pc, pa, xvo, lv, 16384 + j * 2048, lds, l0, rx, rqt);
      qp2_chunk<1, 2, false>(q, a2, cc + 1, j, rot, pc, pa, xvo, lv, 16384 + j * 2048, lds, l0, rx, rqt);
    }
    qp_tail(q, a2[3]);
    parrive(pc, j, lds);
    prog_wait<16, 0>(pa, pc);
    lptr qp = lds + RC + (8 * (j >> 1) + 2 * lg) * 256 + lr * 16 + (j & 1) * 4;
#pragma unroll
    for (int t = 0; t < 7; ++t)
#pragma unroll
      for (int r = 0; r < 4; ++r) *(LAS unsigned*)(qp + t * 4096 + (r >> 1) * 256 + (r & 1) * 8) = cvtpk(q.acc[0][t][r] * QSCALE, q.acc[1][t][r] * QSCALE);
    parrive(pc, j, lds);
    PST();
  }
  __builtin_amdgcn_s_waitcnt(0x0F70);
}

__global__ __launch_bounds__(512, 2) void attn_fused(const char* __restrict__ x, const char* __restrict__ K, const char* __restrict__ V,
                                                     const char* __restrict__ wq, const char* __restrict__ wo, char* __restrict__ ws, char* __restrict__ out) {
  extern __shared__ __attribute__((aligned(1024))) char smem[];
  const int w = rfl(threadIdx.x >> 6);
  unsigned long long pc0 = 0;
  if (CODEPF) asm volatile("s_getpc_b64 %0" : "=s"(pc0));
  const int bid = blockIdx.x;
  const int b = ((bid >> 1) & 3) * 8 + (bid >> 5), g = (bid & 1) * 4 + ((bid >> 3) & 3);
  const int rot = ROT ? (((b & 7) * 5) / 4 + ((g & 3) * 5) / 2) % 10 : 0;
  lptr lds = (lptr)smem;
  const unsigned l0 = (unsigned)(uintptr_t)lds;
  const Ctx c = {ws, b, g};
  const rsrc_t rx = mkrsrc(x + (size_t)b * 143360, 143360), rwq = mkrsrc(wq, 640 * 8192);
  int nst = 0; (void)nst;
  if (w < 4) {
    const size_t kvb = ((size_t)b * 32 + g * 4) * 131072;
    attn_wave(w, fresh_lane(), rot, lds, l0, mkrsrc(K + kvb, 524288), mkrsrc(V + kvb, 524288), rx, rwq, wo, c, nst);
    oproj_pass<3>(w, false, c, lds, l0, nst);
    ST(); ST();
    final_part(w, c, lds, l0, out, nst);
  } else {
    if (CODEPF && w < 6) dma_df(mkrsrc((const void*)(pc0 & ~255ull), CODEPF), fresh_lane() * 16, ((blockIdx.x >> 3) * 2 + (w - 4)) * 1024, l0 + DUMMY + 1024);
    proj_wave(w - 4, fresh_lane(), c, rot, lds, l0, rx, nst);
    oproj_pass<5>(w - 4, w == 4, c, lds, l0, nst);
    oproj_res(w - 4, c, lds, l0, nst);
    __builtin_amdgcn_s_waitcnt(0x0F70);
  }
}

extern "C" void kernel_launch(void* const* d_in, const int* in_sizes, int n_in, void* d_out, int out_size, void* d_ws, size_t ws_size, hipStream_t stream) {
  (void)hipFuncSetAttribute((const void*)attn_fused, hipFuncAttributeMaxDynamicSharedMemorySize, LDS_BYTES);
  attn_fused<<<256, 512, LDS_BYTES, stream>>>((const char*)d_in[0], (const char*)d_in[1], (const char*)d_in[2], (const char*)d_in[3], (const char*)d_in[4], (char*)d_ws + WSOFF, (char*)d_out);
}
```

```cpp
#include <hip/hip_runtime.h>
#include <stdint.h>
typedef __attribute__((ext_vector_type(8))) __bf16 bf16x8;
typedef __attribute__((ext_vector_type(2))) __bf16 bf16x2;
typedef __attribute__((ext_vector_type(4))) float f32x4;
typedef __attribute__((ext_vector_type(16))) float f32x16;
typedef __attribute__((ext_vector_type(2))) float f32x2;
typedef __attribute__((ext_vector_type(4))) unsigned u32x4;
typedef __attribute__((ext_vector_type(2))) unsigned u32x2;
typedef __attribute__((ext_vector_type(4))) short s16x4;
#define LAS __attribute__((address_space(3)))
#define DI __device__ __forceinline__
typedef LAS char* lptr;
typedef __amdgpu_buffer_rsrc_t rsrc_t;
typedef __attribute__((address_space(1))) unsigned long long gu64;
#define RLX_AGENT __ATOMIC_RELAXED, __HIP_MEMORY_SCOPE_AGENT

constexpr int SLOT = 16384, VOFF = 2 * SLOT, XCH = 14336, RA = 4 * SLOT, RB = RA + 2 * XCH, RC = RB + 2 * XCH, PBUF = SLOT + XCH, DUMMY = VOFF + 4 * PBUF,
              TAGW = DUMMY + 4096, CNT = TAGW + 16, APROG = CNT + 32, PPROG = APROG + 16, QPROG = PPROG + 16, KPROG = QPROG + 16, STMP = CNT + 128, ARR = STMP + 512, LDS_BYTES = ARR + 1024;
constexpr float QSCALE = 0.08838834764831845f * 1.4426950408889634f;
constexpr unsigned OOB = 0x40000000u;
constexpr size_t WS_TF = 8192, WS_OF = 16384, WS_XF = WS_OF + 131072, WS_O = 1u << 20, WS_Q = 40u << 20, WS_WT = 48u << 20, WS_QT = 56u << 20, WS_PX = 64u << 20, WS_ST = 128u << 20;
constexpr int OWN3 = 0, OWNP = 2 * XCH;
constexpr unsigned P_Q12 = 12, P_Q3 = 24;

template <int N> struct IC { static constexpr int value = N; };
DI unsigned rfl(unsigned x) { return __builtin_amdgcn_readfirstlane(x); }
DI unsigned cvtpk(float lo, float hi) { f32x2 v = {lo, hi}; bf16x2 b = __builtin_convertvector(v, bf16x2); return __builtin_bit_cast(unsigned, b); }
DI f32x4 mfma(u32x4 a, u32x4 b, f32x4 c) { return __builtin_amdgcn_mfma_f32_16x16x32_bf16(__builtin_bit_cast(bf16x8, a), __builtin_bit_cast(bf16x8, b), c, 0, 0, 0); }
DI void mfma_ip(f32x4& c, const u32x4& a, const u32x4& b) { asm volatile("v_mfma_f32_16x16x32_bf16 %0, %1, %2, %0" : "+v"(c) : "v"(a), "v"(b)); }
#define MFMA_DONE() asm volatile("s_nop 15")
DI f32x16 mfma32(u32x4 a, u32x4 b, f32x16 c) { return __builtin_amdgcn_mfma_f32_32x32x16_bf16(__builtin_bit_cast(bf16x8, a), __builtin_bit_cast(bf16x8, b), c, 0, 0, 0); }
DI u32x4 lds128(lptr p) { return *(const LAS u32x4*)p; }
DI int fresh_lane() { int l; asm volatile("v_mbcnt_lo_u32_b32 %0, -1, 0\n\tv_mbcnt_hi_u32_b32 %0, -1, %0" : "=v"(l)); return l; }
DI lptr lp(unsigned a) { return (lptr)(uintptr_t)a; }
DI u32x2 ldstr(lptr p) { return __builtin_bit_cast(u32x2, __builtin_amdgcn_ds_read_tr16_b64_v4i16((LAS s16x4*)p)); }
DI rsrc_t mkrsrc(const void* p, unsigned bytes) { return __builtin_amdgcn_make_buffer_rsrc((void*)p, 0, bytes, 0x00020000); }
DI float xmax(float m) {
  auto a = __builtin_amdgcn_permlane16_swap(__float_as_uint(m), __float_as_uint(m), false, false); m = fmaxf(__uint_as_float(a[0]), __uint_as_float(a[1]));
  auto b = __builtin_amdgcn_permlane32_swap(__float_as_uint(m), __float_as_uint(m), false, false); return fmaxf(__uint_as_float(b[0]), __uint_as_float(b[1]));
}
DI float xsum(float m) {
  auto a = __builtin_amdgcn_permlane16_swap(__float_as_uint(m), __float_as_uint(m), false, false); m = __uint_as_float(a[0]) + __uint_as_float(a[1]);
  auto b = __builtin_amdgcn_permlane32_swap(__float_as_uint(m), __float_as_uint(m), false, false); return __uint_as_float(b[0]) + __uint_as_float(b[1]);
}
#define DFPOL ""
#define TAUX 0
#define KVPOL "nt"
#define VPOL "nt"
template <int OFF, bool V = false> DI void dma_nt(rsrc_t r, unsigned voff, unsigned soff, unsigned ldsaddr) {
  if (V) asm volatile("s_mov_b32 m0, %3\n\ts_nop 0\n\tbuffer_load_dwordx4 %0, %1, %2 offen offset:%4 " VPOL " lds" :: "v"(voff), "s"(r), "s"(soff), "s"(ldsaddr), "i"(OFF) : "memory");
  else asm volatile("s_mov_b32 m0, %3\n\ts_nop 0\n\tbuffer_load_dwordx4 %0, %1, %2 offen offset:%4 " KVPOL " lds" :: "v"(voff), "s"(r), "s"(soff), "s"(ldsaddr), "i"(OFF) : "memory");
}
DI void dma_df(rsrc_t r, unsigned voff, unsigned soff, unsigned ldsaddr) {
  asm volatile("s_mov_b32 m0, %3\n\ts_nop 0\n\tbuffer_load_dwordx4 %0, %1, %2 offen " DFPOL "lds" :: "v"(voff), "s"(r), "s"(soff), "s"(ldsaddr) : "memory");
}
DI void dma_sc1(rsrc_t r, unsigned voff, unsigned soff, unsigned ldsaddr) {
  asm volatile("s_mov_b32 m0, %3\n\ts_nop 0\n\tbuffer_load_dwordx4 %0, %1, %2 offen sc1 lds" :: "v"(voff), "s"(r), "s"(soff), "s"(ldsaddr) : "memory");
}
DI void touch(rsrc_t r, unsigned voff, unsigned ldsaddr) {
  asm volatile("s_mov_b32 m0, %2\n\ts_nop 0\n\tbuffer_load_dword %0, %1, 0 offen lds" :: "v"(voff), "s"(r), "s"(ldsaddr) : "memory");
}
#define SB() __builtin_amdgcn_sched_barrier(0)
#define WAITV(N) do { SB(); asm volatile("s_waitcnt vmcnt(" #N ") lgkmcnt(0)" ::: "memory"); SB(); } while (0)
#define WAITVM(N) do { SB(); asm volatile("s_waitcnt vmcnt(" #N ")" ::: "memory"); SB(); } while (0)
#define WAITL() do { SB(); asm volatile("s_waitcnt lgkmcnt(0)" ::: "memory"); SB(); } while (0)
#define BAR_L() do { SB(); asm volatile("s_waitcnt lgkmcnt(0)\n\ts_barrier" ::: "memory"); SB(); } while (0)
#define ST() do {} while (0)
#define PST() do {} while (0)
#define PST2() do {} while (0)
#define PCAP 0
#define PC() do { if (PCAP) asm volatile("s_waitcnt vmcnt(%0)" :: "i"(PCAP - 1) : "memory"); } while (0)
#define PIN(x) asm volatile("" : "+v"(x))
#define NODMA 0
#define DPOS 2
#define DSTR 8
#define CAP 16
#define SAUX 16
#define PPRIO 0
#define ABL 0
#define TE0 2
#define XPRIO 0
#define PF 0
#define VP0 2
#define VST 4
#define KP0 18
#define KST 4
#define QPACE 10
#define OPACE 0
#define OP0 26
#define MAP 4
#define OSA 16
#define WSOFF 0
#define CODEPF 0
#define NH 2
#define SSL 0
#define DSL 0
#define LPRIO 3
#define ILV 0
#define TC 1
#define TN0 0, 9, 8, 8, 0, 4, 0, 5, 0, 0
#define LCAP 0
#define RPACE 0
#define WARM 0
#define TPRIO 0
#define OSL 0
#define HSL 0
#define ROT 1
#define QLD0 6
#define PFD 16
constexpr float PLIM = 1024.f;

template <int OFF> DI unsigned prog_read(unsigned pa) { return *(volatile LAS unsigned*)(lp(pa) + OFF); }
template <int OFF, int SLEEP> DI void prog_wait(unsigned pa, unsigned v, unsigned t) {
  unsigned n;
  asm volatile("s_mov_b32 %1, 0x1000000\n"
               "1:\n\tv_cmp_gt_u32 vcc, %3, %0\n\ts_cbranch_vccz 2f\n\ts_sleep %5\n\tds_read_b32 %0, %2 offset:%4\n\ts_sub_u32 %1, %1, 1\n\ts_waitcnt lgkmcnt(0)\n\ts_cbranch_scc0 1b\n2:"
               : "+v"(v), "=&s"(n) : "v"(pa), "s"(t), "i"(OFF), "i"(SLEEP) : "memory", "vcc", "scc");
}
template <int OFF, int SLEEP> DI void prog_wait(unsigned pa, unsigned t) { prog_wait<OFF, SLEEP>(pa, prog_read<OFF>(pa), t); }
DI void prog_post(lptr p, unsigned v) { asm volatile("" ::: "memory"); *(volatile LAS unsigned*)p = v; }

DI void publish(gu64* flag, lptr cnt, unsigned long long tag, int lane) {
  asm volatile("s_waitcnt vmcnt(0)" ::: "memory");
  if (lane == 0) {
    const unsigned old = __hip_atomic_fetch_add((LAS unsigned*)cnt, 1u, __ATOMIC_RELAXED, __HIP_MEMORY_SCOPE_WORKGROUP);
    asm volatile("" : "+s"(tag));
    if (old == 3u) __hip_atomic_store(flag, tag, RLX_AGENT);
  }
}
DI void wait_flag(gu64* f, unsigned long long v, unsigned long long tag) {
#pragma nounroll
  for (unsigned spins = 0; spins < (1u << 15); ++spins) {
    if (__all(v == tag)) break;
    __builtin_amdgcn_s_sleep(2);
    v = __hip_atomic_load(f, RLX_AGENT);
  }
  __builtin_amdgcn_s_waitcnt(0x0F70);
  __builtin_amdgcn_fence(__ATOMIC_ACQUIRE, "wavefront");
  asm volatile("" ::: "memory");
}

struct Ctx {
  char* ws; int b, g;
  DI Ctx fresh() const { Ctx c = *this; asm volatile("" : "+s"(c.b), "+s"(c.g)); return c; }
  DI gu64* epoch() const { return (gu64*)ws; }
  DI gu64* tflags() const { return (gu64*)(ws + WS_TF + g * 512); }
  DI gu64* xflag(int dst) const { return (gu64*)(ws + WS_XF + (b * 8 + dst) * 128); }
  DI rsrc_t rpx(int dst) const { return mkrsrc(ws + WS_PX + (size_t)(b * 8 + dst) * 143360, (ABL & 64) ? 0 : 143360); }
  DI rsrc_t rwt() const { return mkrsrc(ws + WS_WT + (size_t)g * 655360, 655360); }
  DI rsrc_t rqt() const { return mkrsrc(ws + WS_QT + (size_t)g * 491520, 491520); }
};
template <int NQ> struct AttnState {
  u32x4 q[NQ][4], fr[8], p[2][NQ];
  f32x4 o[8][NQ], negm[NQ], sc[2][4][NQ];
  float l[NQ], rs[NQ], inv[NQ];
  unsigned ka[4], va[8], kvo, vvo, pa, pb, pg, oad, last;
};

DI int qreg(int r) { return r == 3 ? RC : ((r & 1) ? RB : RA); }
template <int NQ> DI void sm_pair(AttnState<NQ>& a, const f32x4 (&x)[4][NQ], u32x4 (&p)[NQ], const int hf, const int n) {
  const int t = n >> 2, d = n & 3;
  if (t >= NQ) return;
  const float e0 = __builtin_amdgcn_exp2f(x[2 * hf + (d >> 1)][t][2 * (d & 1)]), e1 = __builtin_amdgcn_exp2f(x[2 * hf + (d >> 1)][t][2 * (d & 1) + 1]);
  a.rs[t] = d ? (a.rs[t] + e0) + e1 : e0 + e1;
  p[t][d] = cvtpk(e0, e1);
  PIN(p[t][d]); PIN(a.rs[t]);
}
template <int NQ, int HF, bool ADJ>
DI void fixup(AttnState<NQ>& a, f32x4 (&x)[4][NQ], f32x4 (&y)[4][NQ]) {
#pragma unroll
  for (int t = 0; t < NQ; ++t) {
    float mx = x[3][t][3];
#pragma unroll
    for (int i = 8 * HF; i < 15; ++i) mx = fmaxf(mx, x[i >> 2][t][i & 3]);
    const float dl = __builtin_ceilf(fmaxf(xmax(mx), 0.f)), sc = __builtin_amdgcn_exp2f(-dl);
    a.l[t] *= sc; a.negm[t] -= dl;
#pragma unroll
    for (int dt = 0; dt < 8; ++dt) a.o[dt][t] *= sc;
    if (HF) {
#pragma unroll
      for (int d = 0; d < 4; ++d) { const unsigned v = a.p[0][t][d]; a.p[0][t][d] = cvtpk(__uint_as_float(v << 16) * sc, __uint_as_float(v & 0xffff0000u) * sc); }
    }
#pragma unroll
    for (int kt = 2 * HF; kt < 4; ++kt) x[kt][t] -= dl;
    float r = 0.f;
#pragma unroll
    for (int d = 0; d < 4; ++d) { const float e0 = __builtin_amdgcn_exp2f(x[2 * HF + (d >> 1)][t][2 * (d & 1)]), e1 = __builtin_amdgcn_exp2f(x[2 * HF + (d >> 1)][t][2 * (d & 1) + 1]);
      r += e0 + e1; a.p[HF][t][d] = cvtpk(e0, e1); }
    a.rs[t] = r;
    if (ADJ) {
#pragma unroll
      for (int kt = 0; kt < 4; ++kt) y[kt][t] -= dl;
    }
  }
}
template <int NQ, int HF, bool CHECK, bool ADJ>
DI void sm_close(AttnState<NQ>& a, f32x4 (&x)[4][NQ], f32x4 (&y)[4][NQ]) {
  if (CHECK) {
    if (NQ == 1) PIN(a.p[HF][0]);
    float m = NQ == 2 ? a.rs[0] + a.rs[NQ - 1] : a.rs[0]; PIN(m);
    if (__builtin_expect(__any(!(m <= PLIM)), 0)) fixup<NQ, HF, ADJ>(a, x, y);
  }
#pragma unroll
  for (int t = 0; t < NQ; ++t) { PIN(a.rs[t]); a.l[t] += a.rs[t]; PIN(a.l[t]); }
}
template <int NQ> DI u32x4 o_frag(const AttnState<NQ>& a, const int m, const int t) {
  const f32x4 x = a.o[2 * m][t] * a.inv[t], y = a.o[2 * m + 1][t] * a.inv[t];
  return u32x4{cvtpk(x[0], x[1]), cvtpk(x[2], x[3]), cvtpk(y[0], y[1]), cvtpk(y[2], y[3])};
}
template <bool V, int NQ> DI void frag_read(AttnState<NQ>& a, const int m, const int par) {
  if (!V) a.fr[m & 7] = lds128(lp(a.ka[m & 3]) + par * SLOT + (m >> 2) * 4096);
  else { lptr p = lp(a.va[m & 7]) + par * SLOT + (m >> 3) * 8192; const u32x2 v0 = ldstr(p), v1 = ldstr(p + 4096); a.fr[m & 7] = u32x4{v0[0], v0[1], v1[0], v1[1]}; }
}
template <bool V, int NQ> DI void tile_issue(const AttnState<NQ>& a, rsrc_t r, const unsigned gsrc, const unsigned dst, const unsigned st = 4096) {
  if (NODMA) return;
#pragma unroll
  for (int i = 0; i < 4; ++i) {
    if (LCAP) asm volatile("s_waitcnt vmcnt(%0)" :: "i"(LCAP - 4) : "memory");
    dma_nt<0, V>(r, V ? a.vvo : a.kvo, gsrc + i * 4096, dst + i * st);          dma_nt<1024, V>(r, V ? a.vvo ^ 128 : a.kvo ^ 64, gsrc + i * 4096, dst + i * st);
    dma_nt<2048, V>(r, V ? a.vvo : a.kvo ^ 128, gsrc + i * 4096, dst + i * st); dma_nt<3072, V>(r, V ? a.vvo ^ 128 : a.kvo ^ 192, gsrc + i * 4096, dst + i * st);
  }
}
template <bool CV, int NQ> DI void phase_io(AttnState<NQ>& a, const int i, const int cp, const int np, const unsigned val, const int w, lptr lds) {
  if (i <= 10) frag_read<CV>(a, i + 5, cp);
  if (NQ == 2 && i == 10) {
    SB();
    prog_post(lds + APROG + w * 4, val);
    a.pg = prog_read<0>(a.pb);
  }
  if (NQ == 2 && i == 12) prog_wait<0, HSL>(a.pb, a.pg, val);
  if (i == 13 || i == 14) { frag_read<!CV>(a, 2 * (i - 13), np); frag_read<!CV>(a, 2 * (i - 13) + 1, np); }
  if (i == 15) frag_read<!CV>(a, 4, np);
}
template <bool V, int NQ> DI void loader_turn(AttnState<NQ>& a, const unsigned val, lptr lds, rsrc_t r, const unsigned gsrc, const unsigned dst) {
  if (NQ == 1) {
    WAITVM(16);
    prog_post(lds + APROG + 12, val + 1);
    prog_wait<0, 1>(a.pb, val);
    if (RPACE) {
      asm volatile("1:\n\ts_memrealtime vcc\n\ts_waitcnt lgkmcnt(0)\n\ts_sub_u32 vcc_hi, vcc_lo, %0\n\ts_cmp_lt_u32 vcc_hi, %1\n\ts_cbranch_scc1 1b\n\ts_mov_b32 %0, vcc_lo"
                   : "+s"(a.last) : "i"(RPACE) : "memory", "scc", "vcc");
    }
    tile_issue<V>(a, r, gsrc, gsrc < 32 * SLOT ? dst : (unsigned)(uintptr_t)lds + DUMMY, gsrc < 32 * SLOT ? 4096u : 0u);
    SB();
  }
}
template <int KIND, int PAR, int NQ>
DI void attn_step(AttnState<NQ>& a, const int s, const int w, const int lane, lptr lds, const unsigned l0, rsrc_t rk, rsrc_t rv) {
  const f32x4 zero = {0.f, 0.f, 0.f, 0.f};
  f32x4 (&c)[4][NQ] = a.sc[PAR]; f32x4 (&d)[4][NQ] = a.sc[PAR ^ 1];
  SB();
  if (KIND == 1) {
    prog_wait<16, 1>(a.pa, s == 0 ? 0u : (s < 24 ? P_Q12 : P_Q3));
    const unsigned qa = l0 + qreg(s >> 3) + 2 * w * 4096 + lane * 16;
#pragma unroll
    for (int i = 0; i < 4; ++i)
#pragma unroll
      for (int t = 0; t < NQ; ++t) a.q[t][i] = lds128(lp(qa) + t * 4096 + i * 1024);
    a.oad = qa;
    SB();
  }
#pragma unroll
  for (int i = 0; i < 16; ++i) {
    constexpr int PA[16] = {-1, 0, 4, -1, 1, 5, -1, 2, 6, -1, -1, 3, 7, -1, -1, -1};
#pragma unroll
    for (int t = 0; t < NQ; ++t) {
      c[i >> 2][t] = mfma(a.fr[i & 7], a.q[t][i & 3], (i & 3) ? c[i >> 2][t] : (KIND == 1 ? zero : a.negm[t]));
      if (t == 0) { if (PA[i] >= 0) sm_pair(a, d, a.p[1], 1, PA[i]); SB(); }
    }
    if (i == 12) PIN(c[3][NQ - 1]);
    phase_io<false>(a, i, PAR, PAR ^ 1, 2 * s + 1, w, lds);
    if (KIND == 2 && i >= 8 && ((i - 8) >> 2) < NQ) *(LAS u32x4*)(lp(a.oad) + ((i - 8) >> 2) * 4096 + (i & 3) * 1024) = o_frag(a, i & 3, ((i - 8) >> 2) % NQ);
    SB();
  }
  if (KIND == 2) prog_post(lds + QPROG + w * 4, s);
  loader_turn<false>(a, 2 * s + 1, lds, rk, (s + 2) * SLOT, l0 + PAR * SLOT);
  sm_close<NQ, 1, KIND != 2, KIND == 0>(a, d, c);
  if (KIND == 1) {
#pragma unroll
    for (int t = 0; t < NQ; ++t) { a.inv[t] = __builtin_amdgcn_rcpf(xsum(a.l[t])); a.l[t] = 0.f; }
  }
  SB();
  u32x4 pn[NQ];
#pragma unroll
  for (int i = 0; i < 16; ++i) {
    constexpr int PB[16] = {-1, -1, -1, 0, -1, 4, -1, 1, -1, 5, -1, 2, 6, -1, 3, 7};
#pragma unroll
    for (int t = 0; t < NQ; ++t) {
      a.o[i & 7][t] = mfma(a.fr[i & 7], a.p[i >> 3][t], (KIND == 2 && i < 8) ? zero : a.o[i & 7][t]);
      if (t == 0) { if (PB[i] >= 0) sm_pair(a, c, pn, 0, PB[i]); SB(); }
    }
    if (i == 12) PIN(a.o[4][NQ - 1]);
    phase_io<true>(a, i, PAR ^ 1, PAR ^ 1, 2 * s + 2, w, lds);
    if (KIND == 1 && (i == 1 || i == 2) && i - 1 < NQ) {
      const int t = (i - 1) % NQ;
      float m0 = c[3][t][3];
#pragma unroll
      for (int e = 0; e < 15; ++e) m0 = fmaxf(m0, c[e >> 2][t][e & 3]);
      m0 = xmax(m0);
#pragma unroll
      for (int kt = 0; kt < 4; ++kt) { c[kt][t] -= m0; PIN(c[kt][t]); }
      a.negm[t] = f32x4{-m0, -m0, -m0, -m0}; PIN(a.negm[t]);
    }
    SB();
  }
#pragma unroll
  for (int t = 0; t < NQ; ++t) a.p[0][t] = pn[t];
  loader_turn<true>(a, 2 * s + 2, lds, rv, (s + 1) * SLOT, l0 + VOFF + (PAR ^ 1) * SLOT);
  sm_close<NQ, 0, KIND != 1, false>(a, c, c);
}

template <int NQ>
DI void attn_main(const int w, lptr lds, const unsigned l0, rsrc_t rk, rsrc_t rv, const Ctx& c0, int& nst) {
  const int lane = fresh_lane(), lg = lane >> 4, lr = lane & 15;
  AttnState<NQ> a;
  a.kvo = lg * 256 + ((lr ^ lg) << 4);
  a.vvo = lg * 256 + ((lr ^ (lg << 1)) << 4);
  BAR_L();
  if (NQ == 1) { tile_issue<true>(a, rv, 0, l0 + VOFF); prog_post(lds + APROG + 12, 1); __builtin_amdgcn_s_setprio(LPRIO); }
#pragma unroll
  for (int i = 0; i < 4; ++i) { a.ka[i] = l0 + lr * 256 + (((4 * i + lg) ^ lr) << 4); PIN(a.ka[i]); }
#pragma unroll
  for (int i = 0; i < 8; ++i) { a.va[i] = l0 + VOFF + (lane >> 2) * 256 + ((i ^ ((lane >> 2) & 7)) << 5) + (lane & 3) * 8; PIN(a.va[i]); }
  a.pg = 0; a.pa = l0 + APROG + (lane & 3) * 4; a.pb = l0 + APROG + (NQ == 2 ? 12 : ((lane & 3) == 3 ? 0 : (lane & 3) * 4)); a.oad = 0; a.last = RPACE ? (unsigned)__builtin_amdgcn_s_memrealtime() - 1000u : 0u;
#pragma unroll
  for (int t = 0; t < NQ; ++t) {
    a.l[t] = 0.f; a.inv[t] = 0.f; a.rs[t] = 0.f; a.p[0][t] = a.p[1][t] = u32x4{0u, 0u, 0u, 0u}; a.negm[t] = f32x4{0.f, 0.f, 0.f, 0.f};
#pragma unroll
    for (int i = 0; i < 8; ++i) { a.o[i][t] = f32x4{0.f, 0.f, 0.f, 0.f}; a.sc[1][i & 3][t] = f32x4{0.f, 0.f, 0.f, 0.f}; }
  }
  if (NQ == 1) WAITVM(16); else WAITVM(0);
  BAR_L(); ST();
#pragma unroll
  for (int m = 0; m < 5; ++m) frag_read<false>(a, m, 0);
#pragma nounroll
  for (int r = 0; r < 4; ++r) {
    attn_step<1, 0>(a, 8 * r, w, lane, lds, l0, rk, rv); ST();
    attn_step<2, 1>(a, 8 * r + 1, w, lane, lds, l0, rk, rv); ST();
#pragma nounroll
    for (int t = 2; t < 8; t += 2) {
      attn_step<0, 0>(a, 8 * r + t, w, lane, lds, l0, rk, rv); ST();
      attn_step<0, 1>(a, 8 * r + t + 1, w, lane, lds, l0, rk, rv); ST();
    }
  }
  {
#pragma unroll
    for (int i = 0; i < 8; ++i) sm_pair(a, a.sc[1], a.p[1], 1, i);
    if (NQ == 2) prog_wait<0, 0>(a.pb, 65);
#pragma unroll
    for (int m = 0; m < 8; ++m) frag_read<true>(a, m, 1);
    sm_close<NQ, 1, true, false>(a, a.sc[1], a.sc[1]);
    SB();
#pragma unroll
    for (int i = 0; i < 16; ++i) {
#pragma unroll
      for (int t = 0; t < NQ; ++t) a.o[i & 7][t] = mfma(a.fr[i & 7], a.p[i >> 3][t], a.o[i & 7][t]);
      if (i < 8) frag_read<true>(a, i + 8, 1);
      SB();
    }
#pragma unroll
    for (int t = 0; t < NQ; ++t) a.inv[t] = __builtin_amdgcn_rcpf(xsum(a.l[t]));
  }
#pragma unroll
  for (int t = 0; t < NQ; ++t)
#pragma unroll
    for (int m = 0; m < 4; ++m) *(LAS u32x4*)(lds + OWN3 + (2 * w + t) * 4096 + m * 1024 + lane * 16) = o_frag(a, m, t);
  prog_post(lds + QPROG + w * 4, 33);
  __builtin_amdgcn_s_setprio(0);
  ST();
}

DI void attn_wave(const int w, const int lane, const int rot, lptr lds, const unsigned l0, rsrc_t rk, rsrc_t rv, rsrc_t rx, rsrc_t rwq, const Ctx& c0, int& nst) {
  const int lg = lane >> 4, lr = lane & 15;
  ST();
  {
    const unsigned wvo = lg * 8192 + ((lr ^ (lg << 2)) << 4) + c0.g * 1024;
    const unsigned xvo = (lane >> 3) * 1280 + (((lane & 7) ^ ((4 * (w & 1) + lg) & 7)) << 4);
    auto issue = [&](int c) {
      const int cr = c + rot >= 10 ? c + rot - 10 : c + rot;
      const unsigned wd = l0 + VOFF + (c & 3) * PBUF + w * 1024, xd = wd + SLOT, xs = w * 10240 + cr * 128, ws_ = (cr * 64 + 4 * w) * 8192;
#pragma unroll
      for (int i = 0; i < 3; ++i) dma_df(rx, xvo, xs + i * 40960, xd + i * 4096);
      dma_df(rx, xvo, xs + 3 * 40960, w < 2 ? xd + 3 * 4096 : l0 + DUMMY);
#pragma unroll
      for (int i = 0; i < 4; ++i) dma_df(rwq, wvo, ws_ + i * 131072, wd + i * 4096);
    };
    issue(0); issue(1); issue(2);
    if (w == 0) { *(LAS u32x4*)(lds + CNT + (lane & 7) * 16) = u32x4{0u, 0u, 0u, 0u}; }
    const int b = c0.b, g = c0.g;
    u32x2 rq2[8]; unsigned long long e1;
    const int ti = (b - 20) * 4 + w, tk = b < 20 ? b : ti >> 1, tu = b < 20 ? w : 4 + (ti & 1);
    const unsigned qv = lg * 8 * 8192 + lr * 8 + g * 1024 + 256, sq = tk * 32 * 8192 + tu * 128;
    constexpr int TN[11] = {0, 0, 0, 9, 0, 0, 0, 4, 0, 0, 0};
#pragma unroll
    for (int c = 0; c < 8; ++c) {
      SB(); asm volatile("s_waitcnt vmcnt(%0) lgkmcnt(0)\n\ts_barrier" :: "i"(16 + TN[c] + TN[c + 1] + TN[c + 2]) : "memory"); SB();
      if (c == 4) {
#pragma unroll
        for (int i = 0; i < 8; ++i) PIN(rq2[i]);
        PIN(e1);
      }
      SB();
      if (c < 7) issue(c + 3);
      SB();
      if (c == 0) {
        asm volatile("global_load_dwordx2 %0, %1, %2 sc1" : "=v"(e1) : "v"(0u), "s"(c0.epoch()) : "memory");
#pragma unroll
        for (int jj = 0; jj < 8; ++jj) asm volatile("buffer_load_dwordx2 %0, %1, %2, %3 offen" : "=v"(rq2[jj]) : "v"(qv), "s"(rwq), "s"(sq + jj * 8192) : "memory");
      }
      if (c == 4) {
#pragma unroll
        for (int e = 0; e < 4; ++e) { u32x4 f;
#pragma unroll
          for (int pp = 0; pp < 4; ++pp) f[pp] = __builtin_amdgcn_perm(rq2[2 * pp + 1][e >> 1], rq2[2 * pp][e >> 1], (e & 1) ? 0x07060302u : 0x05040100u);
          __builtin_amdgcn_raw_buffer_store_b128(f, c0.rqt(), (tk * 6 + tu) * 4096 + e * 1024 + lane * 16, 0, 16); }
      }
    }
    {
      const unsigned long long tag = (((unsigned long long)rfl((unsigned)(e1 >> 32)) << 32) | rfl((unsigned)e1)) + 1;
      *(LAS unsigned long long*)(lds + TAGW) = tag;
      publish(c0.tflags() + b, lds + CNT + 16, tag, lane);
    }
    const unsigned kvo = lg * 256 + ((lr ^ lg) << 4);
#pragma unroll
    for (int t = 0; t < 2; ++t) {
      const unsigned o = t * SLOT + w * 4096;
      dma_nt<0>(rk, kvo, o, l0 + o); dma_nt<1024>(rk, kvo ^ 64, o, l0 + o); dma_nt<2048>(rk, kvo ^ 128, o, l0 + o); dma_nt<3072>(rk, kvo ^ 192, o, l0 + o);
      BAR_L();
    }
  }
  if (w < 3) attn_main<2>(w, lds, l0, rk, rv, c0, nst); else attn_main<1>(w, lds, l0, rk, rv, c0, nst);
}

constexpr int LAG = 3;
DI void parrive(unsigned& pc, const int j, lptr lds) { WAITL(); ++pc; prog_post(lds + PPROG + j * 4, pc); }
template <int NE> constexpr int efrag(int e) { return NE == 5 ? e : (NE == 3 ? (e < 2 ? e : 4) : e + 2); }
template <int NE> DI u32x4 wload(rsrc_t rwt, const unsigned lv, const unsigned so, const int e) {
  return __builtin_amdgcn_raw_buffer_load_b128(rwt, lv + (efrag<NE>(e) & 3) * 1024, so + (efrag<NE>(e) >> 2) * 4096, 16);
}
DI void send_flag(const Ctx& c, const int dst, const int part, const unsigned lv, lptr lds) {
  const unsigned long long tag = *(const LAS unsigned long long*)(lds + TAGW);
  asm volatile("s_waitcnt vmcnt(0)" ::: "memory");
  if (lv == 0) __hip_atomic_store(c.xflag(dst) + c.g + part, tag, RLX_AGENT);
}
template <int NE>
DI void oproj_pass(const int j, const bool pstv, const Ctx& c0, lptr lds, const unsigned l0, int& nst) {
  const int lane = fresh_lane();
  const Ctx c = c0.fresh();
  const int mrot = ROT ? c.b : 0, side = (c.g ^ (NE != 5)) & 1;
  const rsrc_t rwt = c.rwt();
  const unsigned pa = l0 + APROG + (lane & 3) * 4, lv = lane * 16, tb = (side * 64 + j) * 5120;
  f32x4 acc[NE][7]; u32x4 af[4][NE], bf[4];
#pragma unroll
  for (int k = 0; k < 3; ++k)
#pragma unroll
    for (int e = 0; e < NE; ++e) { af[k][e] = wload<NE>(rwt, lv, tb + ((k + mrot) & 3) * 20480, e); SB(); }
#pragma unroll
  for (int e = 0; e < NE; ++e)
#pragma unroll
    for (int t = 0; t < 7; ++t) { acc[e][t] = f32x4{0.f, 0.f, 0.f, 0.f}; PIN(acc[e][t]); }
  asm volatile("s_nop 1"); SB();
#pragma nounroll
  for (int r = 0; r < 3; ++r) {
    const unsigned reg = qreg(r + 1);
    if (NE != 2) prog_wait<32, 2>(pa, 8 * r + 9);
    if (NE == 2 && r == 2) send_flag(c0.fresh(), 2 * j + (side ^ 1), 0, lv, lds);
    PST();
    auto baddr = [&](int gi) { return lp(l0 + reg + ((gi / 7 + mrot) & 3) * 1024 + lv) + (gi % 7) * 4096; };
#pragma unroll
    for (int gi = 0; gi < LAG; ++gi) bf[gi] = lds128(baddr(gi));
    SB();
#pragma unroll
    for (int k = 0; k < 4; ++k) {
      int r2 = r; asm volatile("" : "+s"(r2));
      const unsigned so = tb + ((k ? r2 + 1 : r2) * 4 + ((k + 3 + mrot) & 3)) * 20480;
#pragma unroll
      for (int t = 0; t < 7; ++t) {
        const int gi = 7 * k + t;
#pragma unroll
        for (int e = 0; e < NE; ++e) {
          mfma_ip(acc[e][t], af[k][e], bf[gi & 3]);
          if (e == 0 && gi + LAG < 28) bf[(gi + LAG) & 3] = lds128(baddr(gi + LAG));
          if (e == NE / 2 && t >= 1 && t <= NE) af[(k + 3) & 3][t - 1] = wload<NE>(rwt, lv, so, t - 1);
          if (NE == 5 && OSL && e == 4) __builtin_amdgcn_s_sleep(OSL);
          SB();
        }
      }
    }
    PST();
  }
#pragma unroll
  for (int e = 0; e < NE; ++e) af[3][e] = wload<NE>(rwt, lv, tb + (12 + ((3 + mrot) & 3)) * 20480, e);
  if (NE == 5 && WARM) {
    prog_wait<0, 8>(pa, WARM);
#pragma unroll
    for (int i = 0; i < 10; ++i) dma_sc1(rwt, lv, (side ^ 1) * 327680 + ((c.b & 7) * 40 + j * 10 + i) * 1024, l0 + DUMMY);
  }
  if (NE != 2) prog_wait<32, 1>(pa, 33);
  if (NE == 5) __builtin_amdgcn_s_setprio(2);
  PST();
  const Ctx c2 = c0.fresh(); const int dst = 2 * j + side;
  const bool keep = NE == 5 && dst == c2.g;
  const rsrc_t rpx = c2.rpx(dst); const unsigned po = c2.g * 17920;
  unsigned ba[4];
#pragma unroll
  for (int k = 0; k < 4; ++k) ba[k] = l0 + OWN3 + ((k + mrot) & 3) * 1024 + lv;
  auto out = [&](int t, int part) {
    if (keep) { *(LAS f32x4*)(lp(l0 + OWNP + lv) + (t * 5 + part) * 1024) = acc[part % NE][t]; if (part == 3) *(LAS f32x4*)(lp(l0 + OWNP + lv) + (t * 5 + 4) * 1024) = acc[NE - 1][t]; }
    else if (part < NE / 2) {
      const f32x4 x = acc[2 * part][t], y = acc[2 * part + 1][t];
      __builtin_amdgcn_raw_buffer_store_b128(u32x4{cvtpk(x[0], y[0]), cvtpk(x[1], y[1]), cvtpk(x[2], y[2]), cvtpk(x[3], y[3])}, rpx, po + lv + t * 2560 + efrag<NE>(2 * part) * 512, 0, 16);
    } else if (part == 2 && (NE & 1)) { const f32x4 x = acc[NE - 1][t];
      __builtin_amdgcn_raw_buffer_store_b64(u32x2{cvtpk(x[0], x[1]), cvtpk(x[2], x[3])}, rpx, po + (lv >> 1) + t * 2560 + 2048, 0, 16); }
  };
#pragma unroll
  for (int gi = 0; gi < LAG; ++gi) bf[gi] = lds128(lp(ba[gi & 3]) + (gi >> 2) * 4096);
  SB();
#pragma unroll
  for (int gi = 0; gi < 28; ++gi) {
    const int t = gi >> 2, k = gi & 3;
#pragma unroll
    for (int e = 0; e < NE; ++e) {
      mfma_ip(acc[e][t], af[k][e], bf[gi & 3]);
      if (e == 0 && gi + LAG < 28) bf[(gi + LAG) & 3] = lds128(lp(ba[(gi + LAG) & 3]) + ((gi + LAG) >> 2) * 4096);
      if (e == NE - 1 && k == 0 && t > 0) {
        asm volatile("s_nop %0" :: "i"(8 - NE));
#pragma unroll
        for (int e2 = 0; e2 < NE; ++e2) PIN(acc[e2][t - 1]);
      }
      if (e == NE - 1 && t > 0) out(t - 1, k);
      SB();
    }
  }
  MFMA_DONE();
#pragma unroll
  for (int e2 = 0; e2 < NE; ++e2) PIN(acc[e2][6]);
  SB();
#pragma unroll
  for (int k = 0; k < 4; ++k) out(6, k);
  if (NE == 5) __builtin_amdgcn_s_setprio(TPRIO);
  if (keep) prog_post(lds + KPROG, 1);
  if (NE != 5) { send_flag(c2, dst, NE == 2 ? 8 : 0, lv, lds); __builtin_amdgcn_s_waitcnt(0x0F70); }
  PST();
}

DI void oproj_res(const int j, const Ctx& c0, lptr lds, const unsigned l0, int& nst) {
  const int lane = fresh_lane();
  const bool pstv = j == 0; (void)pstv;
  const Ctx c = c0.fresh();
  const int side = (c.g ^ 1) & 1, dst = 2 * j + side;
  const rsrc_t rwt = c.rwt(), rpx = c.rpx(dst);
  const unsigned lv = lane * 16, tb = (side * 64 + j) * 5120, po = c.g * 17920 + 1024 + lv;
  f32x4 acc[2][7]; u32x4 af[16][2], bf[4];
#pragma unroll
  for (int i = 0; i < 16; ++i)
#pragma unroll
    for (int e = 0; e < 2; ++e) { af[i][e] = wload<2>(rwt, lv, tb + i * 20480, e); SB(); }
  auto brd = [&](int n) { const int r = n / 28, gi = n % 28, k = r < 3 ? gi / 7 : gi & 3, t = r < 3 ? gi % 7 : gi >> 2;
    const int off = (r == 0 ? RB : (r == 1 ? RA : (r == 2 ? RC : OWN3))) + t * 4096 + k * 1024;
    bf[n & 3] = lds128(lp(l0 + lv + (off & ~0xFFFF)) + (off & 0xFFFF)); };
  auto out = [&](int t) { const f32x4 x = acc[0][t], y = acc[1][t];
    __builtin_amdgcn_raw_buffer_store_b128(u32x4{cvtpk(x[0], y[0]), cvtpk(x[1], y[1]), cvtpk(x[2], y[2]), cvtpk(x[3], y[3])}, rpx, po + t * 2560, 0, 16); };
#pragma unroll
  for (int t = 0; t < 7; ++t) { acc[0][t] = acc[1][t] = f32x4{0.f, 0.f, 0.f, 0.f}; PIN(acc[0][t]); PIN(acc[1][t]); }
#pragma unroll
  for (int n = 0; n < LAG; ++n) brd(n);
  asm volatile("s_nop 1"); SB();
  auto head = [&](auto R) {
#pragma unroll
    for (int gi = 0; gi < 28; ++gi) {
      constexpr int r = decltype(R)::value;
      const int n = 28 * r + gi, k = r < 3 ? gi / 7 : gi & 3, t = r < 3 ? gi % 7 : gi >> 2;
      if (n == 28) send_flag(c0.fresh(), 2 * j + (side ^ 1), 0, lv, lds);
      mfma_ip(acc[0][t], af[4 * r + k][0], bf[n & 3]);
      if (n + LAG < 112) brd(n + LAG);
      SB();
      mfma_ip(acc[1][t], af[4 * r + k][1], bf[n & 3]);
      if (r == 3 && k == 1 && t > 0) { asm volatile("s_nop 3"); PIN(acc[0][t - 1]); PIN(acc[1][t - 1]); out(t - 1); }
      SB();
    }
    PST();
  };
  head(IC<0>{}); head(IC<1>{}); head(IC<2>{}); head(IC<3>{});
  MFMA_DONE(); PIN(acc[0][6]); PIN(acc[1][6]); SB();
  out(6);
  send_flag(c0.fresh(), dst, 8, lv, lds);
  PST();
}

DI void final_part(const int j, const Ctx& c0, lptr lds, const unsigned l0, char* out, int& nst) {
  const Ctx c = c0.fresh(); const int b = c.b, g = c.g; const rsrc_t rpx = c.rpx(g);
  const int lane = fresh_lane(), p = g >> 1, lg = lane >> 4, lr = lane & 15;
  const bool pstv = false; (void)pstv;
  const unsigned long long tg = *(const LAS unsigned long long*)(lds + TAGW), tag = ((unsigned long long)rfl((unsigned)(tg >> 32)) << 32) | rfl((unsigned)tg);
  auto lo = [](unsigned v) { return __uint_as_float(v << 16); };
  auto hi = [](unsigned v) { return __uint_as_float(v & 0xffff0000u); };
  f32x4 a[2][5];
#pragma unroll
  for (int st = 0; st < 3; ++st) {
    const int ns = st ? 4 : 3;
    {
      const int k = lane & 3;
      gu64* f = c.xflag(g) + (st ? 2 * k + 1 - (g & 1) + (st == 2 ? 8 : 0) : 2 * (k == p ? (p + 1) & 3 : k) + (g & 1));
      wait_flag(f, __hip_atomic_load(f, RLX_AGENT), tag);
    }
    u32x4 in[2][4][2]; u32x2 in2[2][4];
#pragma unroll
    for (int i = 0; i < 2; ++i)
#pragma unroll
      for (int k = 0; k < ns; ++k) {
        const int src = st ? 2 * k + 1 - (g & 1) : 2 * (k + (k >= p)) + (g & 1);
        const unsigned o = (i == 1 && j == 3) ? OOB : src * 17920 + (j + 4 * i) * 2560;
        if (st != 2) in[i][k][0] = __builtin_amdgcn_raw_buffer_load_b128(rpx, lane * 16 + o, 0, 16);
        if (st != 1) in[i][k][1] = __builtin_amdgcn_raw_buffer_load_b128(rpx, lane * 16 + o + 1024, 0, 16);
        if (st != 2) in2[i][k] = __builtin_amdgcn_raw_buffer_load_b64(rpx, lane * 8 + o + 2048, 0, 16);
      }
    if (st == 0) {
      prog_wait<0, 1>(l0 + KPROG, 1);
#pragma unroll
      for (int i = 0; i < 2; ++i)
#pragma unroll
        for (int e = 0; e < 5; ++e) a[i][e] = *(LAS f32x4*)(lds + OWNP + (((i == 1 && j == 3) ? 0 : j + 4 * i) * 5 + e) * 1024 + lane * 16);
    }
#pragma unroll
    for (int i = 0; i < 2; ++i)
#pragma unroll
      for (int k = 0; k < ns; ++k) {
#pragma unroll
        for (int d = (st == 2 ? 4 : 0); d < (st == 1 ? 4 : 8); ++d) { const int reg = d & 3, e = 2 * (d >> 2); a[i][e][reg] += lo(in[i][k][d >> 2][d & 3]); a[i][e + 1][reg] += hi(in[i][k][d >> 2][d & 3]); }
        if (st != 2) {
#pragma unroll
          for (int d = 0; d < 2; ++d) { a[i][4][2 * d] += lo(in2[i][k][d]); a[i][4][2 * d + 1] += hi(in2[i][k][d]); }
        }
      }
    PST();
  }
  if (j == 0 && g == 0 && b == 0 && lane == 0) __hip_atomic_store(c.epoch(), tag, RLX_AGENT);
  const rsrc_t rout = mkrsrc(out + (size_t)b * 143360, 143360);
#pragma unroll
  for (int i = 0; i < 2; ++i) {
    const unsigned ob = (i == 1 && j == 3) ? OOB : ((j + 4 * i) * 16 + lr) * 1280 + (g & 1) * 640;
#pragma unroll
    for (int hf = 0; hf < 2; ++hf)
      __builtin_amdgcn_raw_buffer_store_b128(u32x4{cvtpk(a[i][0][2 * hf], a[i][1][2 * hf]), cvtpk(a[i][2][2 * hf], a[i][3][2 * hf]), cvtpk(a[i][0][2 * hf + 1], a[i][1][2 * hf + 1]),
                                                   cvtpk(a[i][2][2 * hf + 1], a[i][3][2 * hf + 1])}, rout, ob + p * 128 + lg * 32 + hf * 16, 0, 0);
    __builtin_amdgcn_raw_buffer_store_b64(u32x2{cvtpk(a[i][4][0], a[i][4][1]), cvtpk(a[i][4][2], a[i][4][3])}, rout, ob + 512 + p * 32 + lg * 8, 0, 0);
  }
}

template <int NE> struct QpState { f32x4 acc[NE][7]; u32x4 xf[7]; lptr xa0, xa1; };
template <int NE> DI void qp_init(QpState<NE>& q, lptr xb, int lane) {
  const int lg = lane >> 4, lr = lane & 15;
  q.xa0 = xb + lr * 128 + ((lg ^ (lr >> 1)) << 4); q.xa1 = xb + lr * 128 + (((4 + lg) ^ (lr >> 1)) << 4);
#pragma unroll
  for (int t = 0; t < 7; ++t) { q.xf[t] = u32x4{0u, 0u, 0u, 0u}; PIN(q.xf[t]);
#pragma unroll
    for (int e = 0; e < NE; ++e) { q.acc[e][t] = f32x4{0.f, 0.f, 0.f, 0.f}; PIN(q.acc[e][t]); } }
  asm volatile("s_nop 1"); SB();
}
template <int PAR, int NE, bool DEEP>
DI void qp2_chunk(QpState<NE>& q, u32x4 (&af)[4][NE], const int c, const int j, const int rot, unsigned& pc, const unsigned pa, const unsigned xvo, const unsigned lv, const unsigned tb,
                  lptr lds, const unsigned l0, rsrc_t rx, rsrc_t rqt) {
  SB(); asm volatile("s_waitcnt vmcnt(%0) lgkmcnt(0)" :: "i"(DEEP ? 6 * NE + 8 : 2 * NE) : "memory"); SB();
  if (DEEP) asm volatile("s_cmp_gt_u32 %0, 1\n\ts_cbranch_scc1 1f\n\ts_waitcnt vmcnt(12)\n1:" :: "s"(c) : "memory", "scc");
  ++pc; prog_post(lds + PPROG + j * 4, pc);
  const unsigned pg = prog_read<16>(pa);
  const int c1 = c + 1 + rot >= 10 ? c + 1 + rot - 10 : c + 1 + rot, c2 = c + 2 + rot >= 10 ? c + 2 + rot - 10 : c + 2 + rot;
  const unsigned s1 = c < 9 ? (2 * c1 + 1) * 24576 + tb : OOB, s2 = c < 8 ? (2 * c2) * 24576 + tb : OOB;
  lptr x0 = q.xa0 + (DEEP ? (c & 2) * XCH : 0), x1 = q.xa1 + (DEEP ? (c & 2) * XCH : 0);
  SB();
#pragma unroll
  for (int u = 0; u < 14; ++u) {
    const int v = (u + 14 - LAG) % 14, t = v % 7, set = u < LAG ? 2 * (PAR ^ 1) + 1 : 2 * PAR + v / 7;
#pragma unroll
    for (int e = 0; e < NE; ++e) {
      mfma_ip(q.acc[e][t], af[set][e], q.xf[t]);
      if (e == 0 && u >= 2) q.xf[u % 7] = lds128((u >= 7 ? x1 : x0) + PAR * XCH + (u % 7) * 2048);
      if (e == NE / 2 && u >= LAG && u < LAG + NE) af[2 * (PAR ^ 1) + 1][u - LAG] = __builtin_amdgcn_raw_buffer_load_b128(rqt, lv + (u - LAG) * 1024, s1, 16);
      if (e == NE / 2 && u >= 10 && u < 10 + NE) af[2 * PAR][u - 10] = __builtin_amdgcn_raw_buffer_load_b128(rqt, lv + (u - 10) * 1024, s2, 16);
      SB();
    }
    if (u == 0) { prog_wait<16, 0>(pa, pg, pc); q.xf[0] = lds128(x0 + PAR * XCH); q.xf[1] = lds128(x0 + PAR * XCH + 2048); SB(); }
    if (u == 1) {
      const bool on = DEEP ? c < 8 : c < 9;
      const int cx = !DEEP ? c1 : (c + 3 + rot >= 10 ? c + 3 + rot - 10 : c + 3 + rot);
      const unsigned xs = on ? j * 10240 + cx * 128 : OOB, xd = l0 + (on ? (DEEP ? RB + ((c + 3) & 3) * XCH : RC + (PAR ^ 1) * XCH) + j * 1024 : DUMMY), st = on ? 4096u : 0u;
#pragma unroll
      for (int i = 0; i < 3; ++i) dma_df(rx, xvo, xs + i * 40960, xd + i * st);
      dma_df(rx, xvo, xs + 3 * 40960, j < 2 ? xd + 3 * st : l0 + DUMMY);
      SB();
    }
  }
}
template <int NE> DI void qp2_first(u32x4 (&af)[4][NE], const int rot, const unsigned lv, const unsigned tb, rsrc_t rqt) {
  const int r1 = rot == 9 ? 0 : rot + 1;
#pragma unroll
  for (int k = 0; k < 3; ++k)
#pragma unroll
    for (int e = 0; e < NE; ++e) { af[k][e] = __builtin_amdgcn_raw_buffer_load_b128(rqt, lv + e * 1024, (k < 2 ? 2 * rot + k : 2 * r1) * 24576 + tb, 16); SB(); }
#pragma unroll
  for (int e = 0; e < NE; ++e) { af[3][e] = u32x4{0u, 0u, 0u, 0u}; PIN(af[3][e]); }
}
template <int NE> DI void qp_tail(QpState<NE>& q, const u32x4 (&a)[NE]) {
#pragma unroll
  for (int t = 7 - LAG; t < 7; ++t)
#pragma unroll
    for (int e = 0; e < NE; ++e) mfma_ip(q.acc[e][t], a[e], q.xf[t]);
  MFMA_DONE(); SB();
}

DI void proj_wave(const int j, const int lane, const Ctx& c0, const int rot, lptr lds, const unsigned l0, rsrc_t rx, const char* wo, int& nst) {
  unsigned long long tag, tv;
  const bool pstv = j == 0; (void)pstv;
  const int lg = lane >> 4, lr = lane & 15;
  {
    QpState<2> q;
    u32x4 af[2][2];
    af[1][0] = af[1][1] = u32x4{0u, 0u, 0u, 0u}; PIN(af[1][0]); PIN(af[1][1]);
    qp_init(q, lds + VOFF + SLOT, lane);
    const int q4 = lr >> 2;
    const unsigned wa = l0 + VOFF + (8 * lg + q4) * 256 + (((j ^ q4) * 4 + (lane & 3)) << 4);
#pragma nounroll
    for (int c = 0; c < 10; ++c) {
      const int bo = (c & 3) * PBUF;
      BAR_L();
#pragma unroll
      for (int u = 0; u < 14; ++u) {
        const int v = (u + 14 - LAG) % 14, t = v % 7, set = u < LAG ? 1 : v / 7;
        auto ard = [&](int ks, int e) { lptr p = lp(wa + bo) + ks * 8192 + e * 8; const u32x2 lo = ldstr(p), hi = ldstr(p + 1024); af[ks][e] = u32x4{lo[0], lo[1], hi[0], hi[1]}; };
        if (u == 0) { ard(0, 0); q.xf[0] = lds128(q.xa0 + bo); ard(0, 1); SB(); }
#pragma unroll
        for (int e = 0; e < 2; ++e) {
          mfma_ip(q.acc[e][t], af[set][e], q.xf[t]);
          if (e == 0 && u >= 1) q.xf[u % 7] = lds128((u >= 7 ? q.xa1 : q.xa0) + bo + (u % 7) * 2048);
          if (e == 1 && (u == 4 || u == 5)) ard(1, u - 4);
          SB();
        }
      }
      PST();
    }
    tv = __hip_atomic_load(c0.fresh().tflags() + (lane & 31), RLX_AGENT);
    qp_tail(q, af[1]);
    BAR_L();
#pragma unroll
    for (int t = 0; t < 7; ++t)
      *(LAS u32x4*)(lds + RA + t * 4096 + j * 1024 + lane * 16) = u32x4{cvtpk(q.acc[0][t][0] * QSCALE, q.acc[0][t][1] * QSCALE), cvtpk(q.acc[0][t][2] * QSCALE, q.acc[0][t][3] * QSCALE),
                                                                        cvtpk(q.acc[1][t][0] * QSCALE, q.acc[1][t][1] * QSCALE), cvtpk(q.acc[1][t][2] * QSCALE, q.acc[1][t][3] * QSCALE)};
    { const unsigned long long tg = *(const LAS unsigned long long*)(lds + TAGW); tag = ((unsigned long long)rfl((unsigned)(tg >> 32)) << 32) | rfl((unsigned)tg); }
    BAR_L();
    PST();
  }
  unsigned pc = 0;
  {
    const Ctx c = c0.fresh();
    const rsrc_t rqt = c.rqt();
    const unsigned pa = l0 + APROG + (lane & 3) * 4, lv = lane * 16;
    const unsigned xvo = (lane >> 3) * 1280 + (((lane & 7) ^ ((4 * (j & 1) + lg) & 7)) << 4);
    auto xfirst = [&](int k) {
      const unsigned xs = j * 10240 + (k + rot >= 10 ? k + rot - 10 : k + rot) * 128, xd = l0 + RB + k * XCH + j * 1024;
#pragma unroll
      for (int i = 0; i < 3; ++i) dma_df(rx, xvo, xs + i * 40960, xd + i * 4096);
      dma_df(rx, xvo, xs + 3 * 40960, j < 2 ? xd + 3 * 4096 : l0 + DUMMY);
    };
    wait_flag(c.tflags() + (lane & 31), tv, tag);
    u32x4 a2[4][2];
    {
      QpState<4> q; u32x4 af[4][4];
      xfirst(0);
      qp2_first(af, rot, lv, j * 4096, rqt);
      xfirst(1); xfirst(2);
      qp_init(q, lds + RB, lane);
      PST();
#pragma nounroll
      for (int cc = 0; cc < 10; cc += 2) {
        qp2_chunk<0, 4, true>(q, af, cc, j, rot, pc, pa, xvo, lv, j * 4096, lds, l0, rx, rqt);
        qp2_chunk<1, 4, true>(q, af, cc + 1, j, rot, pc, pa, xvo, lv, j * 4096, lds, l0, rx, rqt);
        PST();
      }
      qp_tail(q, af[3]);
      parrive(pc, j, lds);
      __builtin_amdgcn_s_setprio(XPRIO);
      u32x4 qf[7][2];
#pragma unroll
      for (int t = 0; t < 7; ++t)
#pragma unroll
        for (int hf = 0; hf < 2; ++hf) {
          qf[t][hf] = u32x4{cvtpk(q.acc[0][t][2 * hf] * QSCALE, q.acc[1][t][2 * hf] * QSCALE), cvtpk(q.acc[2][t][2 * hf] * QSCALE, q.acc[3][t][2 * hf] * QSCALE),
                            cvtpk(q.acc[0][t][2 * hf + 1] * QSCALE, q.acc[1][t][2 * hf + 1] * QSCALE), cvtpk(q.acc[2][t][2 * hf + 1] * QSCALE, q.acc[3][t][2 * hf + 1] * QSCALE)};
          PIN(qf[t][hf]);
        }
      prog_wait<16, 0>(pa, pc);
      prog_wait<32, 0>(pa, 1);
#pragma unroll
      for (int t = 0; t < 7; ++t)
#pragma unroll
        for (int hf = 0; hf < 2; ++hf) *(LAS u32x4*)(lds + ((j >> 1) ? RA : RB) + (4 * (j & 1) + lg) * 512 + lr * 16 + t * 4096 + hf * 256) = qf[t][hf];
      parrive(pc, j, lds);
      __builtin_amdgcn_s_setprio(0);
      {
        const Ctx cc = c0.fresh(); const int b = cc.b, g = cc.g; const rsrc_t rwo = mkrsrc(wo, 4096 * 1280);
        u32x2 rw[8]; unsigned short r5[8];
        const unsigned rb = 4 * lg * 1280 + (b >> 4) * 640, wv0 = rb + j * 128 + lr * 8, wv1 = rb + 512 + j * 32 + lr * 2;
        const unsigned s2 = ((4 * g + ((b >> 2) & 3)) * 128 + (b & 3) * 32) * 1280;
#pragma unroll
        for (int jj = 0; jj < 8; ++jj) rw[jj] = __builtin_amdgcn_raw_buffer_load_b64(rwo, wv0 + (jj & 3) * 1280, s2 + (jj >> 2) * 16 * 1280, 0);
#pragma unroll
        for (int jj = 0; jj < 8; ++jj) r5[jj] = __builtin_amdgcn_raw_buffer_load_b16(rwo, wv1 + (jj & 3) * 1280, s2 + (jj >> 2) * 16 * 1280, 0);
#pragma unroll
        for (int e = 0; e < 5; ++e) { u32x4 f;
#pragma unroll
          for (int pp = 0; pp < 4; ++pp) f[pp] = e < 4 ? __builtin_amdgcn_perm(rw[2 * pp + 1][(e >> 1) & 1], rw[2 * pp][(e >> 1) & 1], (e & 1) ? 0x07060302u : 0x05040100u)
                                                       : __builtin_amdgcn_perm((unsigned)r5[2 * pp + 1], (unsigned)r5[2 * pp], 0x05040100u);
          __builtin_amdgcn_raw_buffer_store_b128(f, cc.rwt(), (b * 4 + j) * 5120 + e * 1024 + lv, 0, 16); }
        qp2_first(a2, rot, lv, 16384 + j * 2048, rqt);
        publish(cc.tflags() + 32 + b, lds + CNT + 20, tag, lane);
      }
      PST();
    }
    QpState<2> q;
    qp_init(q, lds + RC, lane);
#pragma nounroll
    for (int cc = 0; cc < 10; cc += 2) {
      qp2_chunk<0, 2, false>(q, a2, cc, j, rot, pc, pa, xvo, lv, 16384 + j * 2048, lds, l0, rx, rqt);
      qp2_chunk<1, 2, false>(q, a2, cc + 1, j, rot, pc, pa, xvo, lv, 16384 + j * 2048, lds, l0, rx, rqt);
    }
    qp_tail(q, a2[3]);
    parrive(pc, j, lds);
    prog_wait<16, 0>(pa, pc);
    lptr qp = lds + RC + (8 * (j >> 1) + 2 * lg) * 256 + lr * 16 + (j & 1) * 4;
#pragma unroll
    for (int t = 0; t < 7; ++t)
#pragma unroll
      for (int r = 0; r < 4; ++r) *(LAS unsigned*)(qp + t * 4096 + (r >> 1) * 256 + (r & 1) * 8) = cvtpk(q.acc[0][t][r] * QSCALE, q.acc[1][t][r] * QSCALE);
    wait_flag(c.tflags() + 32 + (lane & 31), __hip_atomic_load(c.tflags() + 32 + (lane & 31), RLX_AGENT), tag);
    parrive(pc, j, lds);
    PST();
  }
  __builtin_amdgcn_s_waitcnt(0x0F70);
}

__global__ __launch_bounds__(512, 2) void attn_fused(const char* __restrict__ x, const char* __restrict__ K, const char* __restrict__ V,
                                                     const char* __restrict__ wq, const char* __restrict__ wo, char* __restrict__ ws, char* __restrict__ out) {
  extern __shared__ __attribute__((aligned(1024))) char smem[];
  const int w = rfl(threadIdx.x >> 6);
  unsigned long long pc0 = 0;
  if (CODEPF) asm volatile("s_getpc_b64 %0" : "=s"(pc0));
  const int bid = blockIdx.x;
  const int b = ((bid >> 1) & 3) * 8 + (bid >> 5), g = (bid & 1) * 4 + ((bid >> 3) & 3);
  const int rot = ROT ? (((b & 7) * 5) / 4 + ((g & 3) * 5) / 2) % 10 : 0;
  lptr lds = (lptr)smem;
  const unsigned l0 = (unsigned)(uintptr_t)lds;
  const Ctx c = {ws, b, g};
  const rsrc_t rx = mkrsrc(x + (size_t)b * 143360, 143360), rwq = mkrsrc(wq, 640 * 8192);
  int nst = 0; (void)nst;
  if (w < 4) {
    const size_t kvb = ((size_t)b * 32 + g * 4) * 131072;
    attn_wave(w, fresh_lane(), rot, lds, l0, mkrsrc(K + kvb, 524288), mkrsrc(V + kvb, 524288), rx, rwq, c, nst);
    oproj_pass<3>(w, false, c, lds, l0, nst);
    ST(); ST();
    final_part(w, c, lds, l0, out, nst);
  } else {
    if (CODEPF && w < 6) dma_df(mkrsrc((const void*)(pc0 & ~255ull), CODEPF), fresh_lane() * 16, ((blockIdx.x >> 3) * 2 + (w - 4)) * 1024, l0 + DUMMY + 1024);
    proj_wave(w - 4, fresh_lane(), c, rot, lds, l0, rx, wo, nst);
    oproj_pass<5>(w - 4, w == 4, c, lds, l0, nst);
    oproj_res(w - 4, c, lds, l0, nst);
    __builtin_amdgcn_s_waitcnt(0x0F70);
  }
}

extern "C" void kernel_launch(void* const* d_in, const int* in_sizes, int n_in, void* d_out, int out_size, void* d_ws, size_t ws_size, hipStream_t stream) {
  (void)hipFuncSetAttribute((const void*)attn_fused, hipFuncAttributeMaxDynamicSharedMemorySize, LDS_BYTES);
  attn_fused<<<256, 512, LDS_BYTES, stream>>>((const char*)d_in[0], (const char*)d_in[1], (const char*)d_in[2], (const char*)d_in[3], (const char*)d_in[4], (char*)d_ws + WSOFF, (char*)d_out);
}
```

```cpp
#include <hip/hip_runtime.h>
#include <stdint.h>
typedef __attribute__((ext_vector_type(8))) __bf16 bf16x8;
typedef __attribute__((ext_vector_type(2))) __bf16 bf16x2;
typedef __attribute__((ext_vector_type(4))) float f32x4;
typedef __attribute__((ext_vector_type(16))) float f32x16;
typedef __attribute__((ext_vector_type(2))) float f32x2;
typedef __attribute__((ext_vector_type(4))) unsigned u32x4;
typedef __attribute__((ext_vector_type(2))) unsigned u32x2;
typedef __attribute__((ext_vector_type(4))) short s16x4;
#define LAS __attribute__((address_space(3)))
#define DI __device__ __forceinline__
typedef LAS char* lptr;
typedef __amdgpu_buffer_rsrc_t rsrc_t;
typedef __attribute__((address_space(1))) unsigned long long gu64;
#define RLX_AGENT __ATOMIC_RELAXED, __HIP_MEMORY_SCOPE_AGENT

constexpr int SLOT = 16384, VOFF = 2 * SLOT, XCH = 14336, RA = 4 * SLOT, RB = RA + 2 * XCH, RC = RB + 2 * XCH, PBUF = SLOT + XCH, DUMMY = VOFF + 4 * PBUF,
              TAGW = DUMMY + 4096, CNT = TAGW + 16, APROG = CNT + 32, PPROG = APROG + 16, QPROG = PPROG + 16, KPROG = QPROG + 16, STMP = CNT + 128, ARR = STMP + 512, LDS_BYTES = ARR + 1024;
constexpr float QSCALE = 0.08838834764831845f * 1.4426950408889634f;
constexpr unsigned OOB = 0x40000000u;
constexpr size_t WS_TF = 8192, WS_OF = 16384, WS_XF = WS_OF + 131072, WS_O = 1u << 20, WS_Q = 40u << 20, WS_WT = 48u << 20, WS_QT = 56u << 20, WS_PX = 64u << 20, WS_ST = 128u << 20;
constexpr int OWN3 = 0, OWNP = 2 * XCH;
constexpr unsigned P_Q12 = 12, P_Q3 = 24;

template <int N> struct IC { static constexpr int value = N; };
DI unsigned rfl(unsigned x) { return __builtin_amdgcn_readfirstlane(x); }
DI unsigned cvtpk(float lo, float hi) { f32x2 v = {lo, hi}; bf16x2 b = __builtin_convertvector(v, bf16x2); return __builtin_bit_cast(unsigned, b); }
DI f32x4 mfma(u32x4 a, u32x4 b, f32x4 c) { return __builtin_amdgcn_mfma_f32_16x16x32_bf16(__builtin_bit_cast(bf16x8, a), __builtin_bit_cast(bf16x8, b), c, 0, 0, 0); }
DI void mfma_ip(f32x4& c, const u32x4& a, const u32x4& b) { asm volatile("v_mfma_f32_16x16x32_bf16 %0, %1, %2, %0" : "+v"(c) : "v"(a), "v"(b)); }
#define MFMA_DONE() asm volatile("s_nop 15")
DI f32x16 mfma32(u32x4 a, u32x4 b, f32x16 c) { return __builtin_amdgcn_mfma_f32_32x32x16_bf16(__builtin_bit_cast(bf16x8, a), __builtin_bit_cast(bf16x8, b), c, 0, 0, 0); }
DI u32x4 lds128(lptr p) { return *(const LAS u32x4*)p; }
DI int fresh_lane() { int l; asm volatile("v_mbcnt_lo_u32_b32 %0, -1, 0\n\tv_mbcnt_hi_u32_b32 %0, -1, %0" : "=v"(l)); return l; }
DI lptr lp(unsigned a) { return (lptr)(uintptr_t)a; }
DI u32x2 ldstr(lptr p) { return __builtin_bit_cast(u32x2, __builtin_amdgcn_ds_read_tr16_b64_v4i16((LAS s16x4*)p)); }
DI rsrc_t mkrsrc(const void* p, unsigned bytes) { return __builtin_amdgcn_make_buffer_rsrc((void*)p, 0, bytes, 0x00020000); }
DI float xmax(float m) {
  auto a = __builtin_amdgcn_permlane16_swap(__float_as_uint(m), __float_as_uint(m), false, false); m = fmaxf(__uint_as_float(a[0]), __uint_as_float(a[1]));
  auto b = __builtin_amdgcn_permlane32_swap(__float_as_uint(m), __float_as_uint(m), false, false); return fmaxf(__uint_as_float(b[0]), __uint_as_float(b[1]));
}
DI float xsum(float m) {
  auto a = __builtin_amdgcn_permlane16_swap(__float_as_uint(m), __float_as_uint(m), false, false); m = __uint_as_float(a[0]) + __uint_as_float(a[1]);
  auto b = __builtin_amdgcn_permlane32_swap(__float_as_uint(m), __float_as_uint(m), false, false); return __uint_as_float(b[0]) + __uint_as_float(b[1]);
}
#define DFPOL ""
#define TAUX 0
#define KVPOL "nt"
#define VPOL "nt"
template <int OFF, bool V = false> DI void dma_nt(rsrc_t r, unsigned voff, unsigned soff, unsigned ldsaddr) {
  if (V) asm volatile("s_mov_b32 m0, %3\n\ts_nop 0\n\tbuffer_load_dwordx4 %0, %1, %2 offen offset:%4 " VPOL " lds" :: "v"(voff), "s"(r), "s"(soff), "s"(ldsaddr), "i"(OFF) : "memory");
  else asm volatile("s_mov_b32 m0, %3\n\ts_nop 0\n\tbuffer_load_dwordx4 %0, %1, %2 offen offset:%4 " KVPOL " lds" :: "v"(voff), "s"(r), "s"(soff), "s"(ldsaddr), "i"(OFF) : "memory");
}
DI void dma_df(rsrc_t r, unsigned voff, unsigned soff, unsigned ldsaddr) {
  asm volatile("s_mov_b32 m0, %3\n\ts_nop 0\n\tbuffer_load_dwordx4 %0, %1, %2 offen " DFPOL "lds" :: "v"(voff), "s"(r), "s"(soff), "s"(ldsaddr) : "memory");
}
DI void dma_sc1(rsrc_t r, unsigned voff, unsigned soff, unsigned ldsaddr) {
  asm volatile("s_mov_b32 m0, %3\n\ts_nop 0\n\tbuffer_load_dwordx4 %0, %1, %2 offen sc1 lds" :: "v"(voff), "s"(r), "s"(soff), "s"(ldsaddr) : "memory");
}
DI void touch(rsrc_t r, unsigned voff, unsigned ldsaddr) {
  asm volatile("s_mov_b32 m0, %2\n\ts_nop 0\n\tbuffer_load_dword %0, %1, 0 offen sc1 lds" :: "v"(voff), "s"(r), "s"(ldsaddr) : "memory");
}
#define SB() __builtin_amdgcn_sched_barrier(0)
#define WAITV(N) do { SB(); asm volatile("s_waitcnt vmcnt(" #N ") lgkmcnt(0)" ::: "memory"); SB(); } while (0)
#define WAITVM(N) do { SB(); asm volatile("s_waitcnt vmcnt(" #N ")" ::: "memory"); SB(); } while (0)
#define WAITL() do { SB(); asm volatile("s_waitcnt lgkmcnt(0)" ::: "memory"); SB(); } while (0)
#define BAR_L() do { SB(); asm volatile("s_waitcnt lgkmcnt(0)\n\ts_barrier" ::: "memory"); SB(); } while (0)
#define ST() do {} while (0)
#define PST() do {} while (0)
#define PST2() do {} while (0)
#define PCAP 0
#define PC() do { if (PCAP) asm volatile("s_waitcnt vmcnt(%0)" :: "i"(PCAP - 1) : "memory"); } while (0)
#define PIN(x) asm volatile("" : "+v"(x))
#define NODMA 0
#define DPOS 2
#define DSTR 8
#define CAP 16
#define SAUX 16
#define PPRIO 0
#define ABL 0
#define TE0 2
#define XPRIO 0
#define PF 0
#define VP0 2
#define VST 4
#define KP0 18
#define KST 4
#define QPACE 10
#define OPACE 0
#define OP0 26
#define MAP 4
#define OSA 16
#define WSOFF 0
#define CODEPF 0
#define NH 2
#define SSL 0
#define DSL 0
#define LPRIO 3
#define ILV 0
#define TC 1
#define TN0 0, 9, 8, 8, 0, 4, 0, 5, 0, 0
#define LCAP 0
#define RPACE 0
#define WARM 0
#define TPRIO 0
#define OSL 0
#define HSL 0
#define TOUCH 1
#define ROT 1
#define QLD0 6
#define PFD 16
constexpr float PLIM = 1024.f;

template <int OFF> DI unsigned prog_read(unsigned pa) { return *(volatile LAS unsigned*)(lp(pa) + OFF); }
template <int OFF, int SLEEP> DI void prog_wait(unsigned pa, unsigned v, unsigned t) {
  unsigned n;
  asm volatile("s_mov_b32 %1, 0x1000000\n"
               "1:\n\tv_cmp_gt_u32 vcc, %3, %0\n\ts_cbranch_vccz 2f\n\ts_sleep %5\n\tds_read_b32 %0, %2 offset:%4\n\ts_sub_u32 %1, %1, 1\n\ts_waitcnt lgkmcnt(0)\n\ts_cbranch_scc0 1b\n2:"
               : "+v"(v), "=&s"(n) : "v"(pa), "s"(t), "i"(OFF), "i"(SLEEP) : "memory", "vcc", "scc");
}
template <int OFF, int SLEEP> DI void prog_wait(unsigned pa, unsigned t) { prog_wait<OFF, SLEEP>(pa, prog_read<OFF>(pa), t); }
DI void prog_post(lptr p, unsigned v) { asm volatile("" ::: "memory"); *(volatile LAS unsigned*)p = v; }

DI void publish(gu64* flag, lptr cnt, unsigned long long tag, int lane) {
  asm volatile("s_waitcnt vmcnt(0)" ::: "memory");
  if (lane == 0) {
    const unsigned old = __hip_atomic_fetch_add((LAS unsigned*)cnt, 1u, __ATOMIC_RELAXED, __HIP_MEMORY_SCOPE_WORKGROUP);
    asm volatile("" : "+s"(tag));
    if (old == 3u) __hip_atomic_store(flag, tag, RLX_AGENT);
  }
}
DI void wait_flag(gu64* f, unsigned long long v, unsigned long long tag) {
#pragma nounroll
  for (unsigned spins = 0; spins < (1u << 15); ++spins) {
    if (__all(v == tag)) break;
    __builtin_amdgcn_s_sleep(2);
    v = __hip_atomic_load(f, RLX_AGENT);
  }
  __builtin_amdgcn_s_waitcnt(0x0F70);
  __builtin_amdgcn_fence(__ATOMIC_ACQUIRE, "wavefront");
  asm volatile("" ::: "memory");
}

struct Ctx {
  char* ws; char* out; int b, g;
  DI Ctx fresh() const { Ctx c = *this; asm volatile("" : "+s"(c.b), "+s"(c.g)); return c; }
  DI gu64* epoch() const { return (gu64*)ws; }
  DI gu64* tflags() const { return (gu64*)(ws + WS_TF + g * 512); }
  DI gu64* xflag(int dst) const { return (gu64*)(ws + WS_XF + (b * 8 + dst) * 128); }
  DI rsrc_t rpx(int dst) const { return mkrsrc(ws + WS_PX + (size_t)(b * 8 + dst) * 143360, (ABL & 64) ? 0 : 143360); }
  DI rsrc_t rwt() const { return mkrsrc(ws + WS_WT + (size_t)g * 655360, 655360); }
  DI rsrc_t rqt() const { return mkrsrc(ws + WS_QT + (size_t)g * 491520, 491520); }
};
template <int NQ> struct AttnState {
  u32x4 q[NQ][4], fr[8], p[2][NQ];
  f32x4 o[8][NQ], negm[NQ], sc[2][4][NQ];
  float l[NQ], rs[NQ], inv[NQ];
  unsigned ka[4], va[8], kvo, vvo, pa, pb, pg, oad, last;
};

DI int qreg(int r) { return r == 3 ? RC : ((r & 1) ? RB : RA); }
template <int NQ> DI void sm_pair(AttnState<NQ>& a, const f32x4 (&x)[4][NQ], u32x4 (&p)[NQ], const int hf, const int n) {
  const int t = n >> 2, d = n & 3;
  if (t >= NQ) return;
  const float e0 = __builtin_amdgcn_exp2f(x[2 * hf + (d >> 1)][t][2 * (d & 1)]), e1 = __builtin_amdgcn_exp2f(x[2 * hf + (d >> 1)][t][2 * (d & 1) + 1]);
  a.rs[t] = d ? (a.rs[t] + e0) + e1 : e0 + e1;
  p[t][d] = cvtpk(e0, e1);
  PIN(p[t][d]); PIN(a.rs[t]);
}
template <int NQ, int HF, bool ADJ>
DI void fixup(AttnState<NQ>& a, f32x4 (&x)[4][NQ], f32x4 (&y)[4][NQ]) {
#pragma unroll
  for (int t = 0; t < NQ; ++t) {
    float mx = x[3][t][3];
#pragma unroll
    for (int i = 8 * HF; i < 15; ++i) mx = fmaxf(mx, x[i >> 2][t][i & 3]);
    const float dl = __builtin_ceilf(fmaxf(xmax(mx), 0.f)), sc = __builtin_amdgcn_exp2f(-dl);
    a.l[t] *= sc; a.negm[t] -= dl;
#pragma unroll
    for (int dt = 0; dt < 8; ++dt) a.o[dt][t] *= sc;
    if (HF) {
#pragma unroll
      for (int d = 0; d < 4; ++d) { const unsigned v = a.p[0][t][d]; a.p[0][t][d] = cvtpk(__uint_as_float(v << 16) * sc, __uint_as_float(v & 0xffff0000u) * sc); }
    }
#pragma unroll
    for (int kt = 2 * HF; kt < 4; ++kt) x[kt][t] -= dl;
    float r = 0.f;
#pragma unroll
    for (int d = 0; d < 4; ++d) { const float e0 = __builtin_amdgcn_exp2f(x[2 * HF + (d >> 1)][t][2 * (d & 1)]), e1 = __builtin_amdgcn_exp2f(x[2 * HF + (d >> 1)][t][2 * (d & 1) + 1]);
      r += e0 + e1; a.p[HF][t][d] = cvtpk(e0, e1); }
    a.rs[t] = r;
    if (ADJ) {
#pragma unroll
      for (int kt = 0; kt < 4; ++kt) y[kt][t] -= dl;
    }
  }
}
template <int NQ, int HF, bool CHECK, bool ADJ>
DI void sm_close(AttnState<NQ>& a, f32x4 (&x)[4][NQ], f32x4 (&y)[4][NQ]) {
  if (CHECK) {
    if (NQ == 1) PIN(a.p[HF][0]);
    float m = NQ == 2 ? a.rs[0] + a.rs[NQ - 1] : a.rs[0]; PIN(m);
    if (__builtin_expect(__any(!(m <= PLIM)), 0)) fixup<NQ, HF, ADJ>(a, x, y);
  }
#pragma unroll
  for (int t = 0; t < NQ; ++t) { PIN(a.rs[t]); a.l[t] += a.rs[t]; PIN(a.l[t]); }
}
template <int NQ> DI u32x4 o_frag(const AttnState<NQ>& a, const int m, const int t) {
  const f32x4 x = a.o[2 * m][t] * a.inv[t], y = a.o[2 * m + 1][t] * a.inv[t];
  return u32x4{cvtpk(x[0], x[1]), cvtpk(x[2], x[3]), cvtpk(y[0], y[1]), cvtpk(y[2], y[3])};
}
template <bool V, int NQ> DI void frag_read(AttnState<NQ>& a, const int m, const int par) {
  if (!V) a.fr[m & 7] = lds128(lp(a.ka[m & 3]) + par * SLOT + (m >> 2) * 4096);
  else { lptr p = lp(a.va[m & 7]) + par * SLOT + (m >> 3) * 8192; const u32x2 v0 = ldstr(p), v1 = ldstr(p + 4096); a.fr[m & 7] = u32x4{v0[0], v0[1], v1[0], v1[1]}; }
}
template <bool V, int NQ> DI void tile_issue(const AttnState<NQ>& a, rsrc_t r, const unsigned gsrc, const unsigned dst, const unsigned st = 4096) {
  if (NODMA) return;
#pragma unroll
  for (int i = 0; i < 4; ++i) {
    if (LCAP) asm volatile("s_waitcnt vmcnt(%0)" :: "i"(LCAP - 4) : "memory");
    dma_nt<0, V>(r, V ? a.vvo : a.kvo, gsrc + i * 4096, dst + i * st);          dma_nt<1024, V>(r, V ? a.vvo ^ 128 : a.kvo ^ 64, gsrc + i * 4096, dst + i * st);
    dma_nt<2048, V>(r, V ? a.vvo : a.kvo ^ 128, gsrc + i * 4096, dst + i * st); dma_nt<3072, V>(r, V ? a.vvo ^ 128 : a.kvo ^ 192, gsrc + i * 4096, dst + i * st);
  }
}
template <bool CV, int NQ> DI void phase_io(AttnState<NQ>& a, const int i, const int cp, const int np, const unsigned val, const int w, lptr lds) {
  if (i <= 10) frag_read<CV>(a, i + 5, cp);
  if (NQ == 2 && i == 10) {
    SB();
    prog_post(lds + APROG + w * 4, val);
    a.pg = prog_read<0>(a.pb);
  }
  if (NQ == 2 && i == 12) prog_wait<0, HSL>(a.pb, a.pg, val);
  if (i == 13 || i == 14) { frag_read<!CV>(a, 2 * (i - 13), np); frag_read<!CV>(a, 2 * (i - 13) + 1, np); }
  if (i == 15) frag_read<!CV>(a, 4, np);
}
template <bool V, int NQ> DI void loader_turn(AttnState<NQ>& a, const unsigned val, lptr lds, rsrc_t r, const unsigned gsrc, const unsigned dst) {
  if (NQ == 1) {
    WAITVM(16);
    prog_post(lds + APROG + 12, val + 1);
    prog_wait<0, 1>(a.pb, val);
    if (RPACE) {
      asm volatile("1:\n\ts_memrealtime vcc\n\ts_waitcnt lgkmcnt(0)\n\ts_sub_u32 vcc_hi, vcc_lo, %0\n\ts_cmp_lt_u32 vcc_hi, %1\n\ts_cbranch_scc1 1b\n\ts_mov_b32 %0, vcc_lo"
                   : "+s"(a.last) : "i"(RPACE) : "memory", "scc", "vcc");
    }
    tile_issue<V>(a, r, gsrc, gsrc < 32 * SLOT ? dst : (unsigned)(uintptr_t)lds + DUMMY, gsrc < 32 * SLOT ? 4096u : 0u);
    SB();
  }
}
template <int KIND, int PAR, int NQ>
DI void attn_step(AttnState<NQ>& a, const int s, const int w, const int lane, lptr lds, const unsigned l0, rsrc_t rk, rsrc_t rv) {
  const f32x4 zero = {0.f, 0.f, 0.f, 0.f};
  f32x4 (&c)[4][NQ] = a.sc[PAR]; f32x4 (&d)[4][NQ] = a.sc[PAR ^ 1];
  SB();
  if (KIND == 1) {
    prog_wait<16, 1>(a.pa, s == 0 ? 0u : (s < 24 ? P_Q12 : P_Q3));
    const unsigned qa = l0 + qreg(s >> 3) + 2 * w * 4096 + lane * 16;
#pragma unroll
    for (int i = 0; i < 4; ++i)
#pragma unroll
      for (int t = 0; t < NQ; ++t) a.q[t][i] = lds128(lp(qa) + t * 4096 + i * 1024);
    a.oad = qa;
    SB();
  }
#pragma unroll
  for (int i = 0; i < 16; ++i) {
    constexpr int PA[16] = {-1, 0, 4, -1, 1, 5, -1, 2, 6, -1, -1, 3, 7, -1, -1, -1};
#pragma unroll
    for (int t = 0; t < NQ; ++t) {
      c[i >> 2][t] = mfma(a.fr[i & 7], a.q[t][i & 3], (i & 3) ? c[i >> 2][t] : (KIND == 1 ? zero : a.negm[t]));
      if (t == 0) { if (PA[i] >= 0) sm_pair(a, d, a.p[1], 1, PA[i]); SB(); }
    }
    if (i == 12) PIN(c[3][NQ - 1]);
    phase_io<false>(a, i, PAR, PAR ^ 1, 2 * s + 1, w, lds);
    if (KIND == 2 && i >= 8 && ((i - 8) >> 2) < NQ) *(LAS u32x4*)(lp(a.oad) + ((i - 8) >> 2) * 4096 + (i & 3) * 1024) = o_frag(a, i & 3, ((i - 8) >> 2) % NQ);
    SB();
  }
  if (KIND == 2) prog_post(lds + QPROG + w * 4, s);
  loader_turn<false>(a, 2 * s + 1, lds, rk, (s + 2) * SLOT, l0 + PAR * SLOT);
  sm_close<NQ, 1, KIND != 2, KIND == 0>(a, d, c);
  if (KIND == 1) {
#pragma unroll
    for (int t = 0; t < NQ; ++t) { a.inv[t] = __builtin_amdgcn_rcpf(xsum(a.l[t])); a.l[t] = 0.f; }
  }
  SB();
  u32x4 pn[NQ];
#pragma unroll
  for (int i = 0; i < 16; ++i) {
    constexpr int PB[16] = {-1, -1, -1, 0, -1, 4, -1, 1, -1, 5, -1, 2, 6, -1, 3, 7};
#pragma unroll
    for (int t = 0; t < NQ; ++t) {
      a.o[i & 7][t] = mfma(a.fr[i & 7], a.p[i >> 3][t], (KIND == 2 && i < 8) ? zero : a.o[i & 7][t]);
      if (t == 0) { if (PB[i] >= 0) sm_pair(a, c, pn, 0, PB[i]); SB(); }
    }
    if (i == 12) PIN(a.o[4][NQ - 1]);
    phase_io<true>(a, i, PAR ^ 1, PAR ^ 1, 2 * s + 2, w, lds);
    if (KIND == 1 && (i == 1 || i == 2) && i - 1 < NQ) {
      const int t = (i - 1) % NQ;
      float m0 = c[3][t][3];
#pragma unroll
      for (int e = 0; e < 15; ++e) m0 = fmaxf(m0, c[e >> 2][t][e & 3]);
      m0 = xmax(m0);
#pragma unroll
      for (int kt = 0; kt < 4; ++kt) { c[kt][t] -= m0; PIN(c[kt][t]); }
      a.negm[t] = f32x4{-m0, -m0, -m0, -m0}; PIN(a.negm[t]);
    }
    SB();
  }
#pragma unroll
  for (int t = 0; t < NQ; ++t) a.p[0][t] = pn[t];
  loader_turn<true>(a, 2 * s + 2, lds, rv, (s + 1) * SLOT, l0 + VOFF + (PAR ^ 1) * SLOT);
  sm_close<NQ, 0, KIND != 1, false>(a, c, c);
}

template <int NQ>
DI void attn_main(const int w, lptr lds, const unsigned l0, rsrc_t rk, rsrc_t rv, const Ctx& c0, int& nst) {
  const int lane = fresh_lane(), lg = lane >> 4, lr = lane & 15;
  AttnState<NQ> a;
  a.kvo = lg * 256 + ((lr ^ lg) << 4);
  a.vvo = lg * 256 + ((lr ^ (lg << 1)) << 4);
  BAR_L();
  if (NQ == 1) { tile_issue<true>(a, rv, 0, l0 + VOFF); prog_post(lds + APROG + 12, 1); __builtin_amdgcn_s_setprio(LPRIO); }
#pragma unroll
  for (int i = 0; i < 4; ++i) { a.ka[i] = l0 + lr * 256 + (((4 * i + lg) ^ lr) << 4); PIN(a.ka[i]); }
#pragma unroll
  for (int i = 0; i < 8; ++i) { a.va[i] = l0 + VOFF + (lane >> 2) * 256 + ((i ^ ((lane >> 2) & 7)) << 5) + (lane & 3) * 8; PIN(a.va[i]); }
  a.pg = 0; a.pa = l0 + APROG + (lane & 3) * 4; a.pb = l0 + APROG + (NQ == 2 ? 12 : ((lane & 3) == 3 ? 0 : (lane & 3) * 4)); a.oad = 0; a.last = RPACE ? (unsigned)__builtin_amdgcn_s_memrealtime() - 1000u : 0u;
#pragma unroll
  for (int t = 0; t < NQ; ++t) {
    a.l[t] = 0.f; a.inv[t] = 0.f; a.rs[t] = 0.f; a.p[0][t] = a.p[1][t] = u32x4{0u, 0u, 0u, 0u}; a.negm[t] = f32x4{0.f, 0.f, 0.f, 0.f};
#pragma unroll
    for (int i = 0; i < 8; ++i) { a.o[i][t] = f32x4{0.f, 0.f, 0.f, 0.f}; a.sc[1][i & 3][t] = f32x4{0.f, 0.f, 0.f, 0.f}; }
  }
  if (NQ == 1) WAITVM(16); else WAITVM(0);
  BAR_L(); ST();
#pragma unroll
  for (int m = 0; m < 5; ++m) frag_read<false>(a, m, 0);
#pragma nounroll
  for (int r = 0; r < 4; ++r) {
    attn_step<1, 0>(a, 8 * r, w, lane, lds, l0, rk, rv); ST();
    attn_step<2, 1>(a, 8 * r + 1, w, lane, lds, l0, rk, rv); ST();
#pragma nounroll
    for (int t = 2; t < 8; t += 2) {
      attn_step<0, 0>(a, 8 * r + t, w, lane, lds, l0, rk, rv); ST();
      attn_step<0, 1>(a, 8 * r + t + 1, w, lane, lds, l0, rk, rv); ST();
    }
  }
  {
#pragma unroll
    for (int i = 0; i < 8; ++i) sm_pair(a, a.sc[1], a.p[1], 1, i);
    if (NQ == 2) prog_wait<0, 0>(a.pb, 65);
#pragma unroll
    for (int m = 0; m < 8; ++m) frag_read<true>(a, m, 1);
    sm_close<NQ, 1, true, false>(a, a.sc[1], a.sc[1]);
    SB();
#pragma unroll
    for (int i = 0; i < 16; ++i) {
#pragma unroll
      for (int t = 0; t < NQ; ++t) a.o[i & 7][t] = mfma(a.fr[i & 7], a.p[i >> 3][t], a.o[i & 7][t]);
      if (i < 8) frag_read<true>(a, i + 8, 1);
      SB();
    }
#pragma unroll
    for (int t = 0; t < NQ; ++t) a.inv[t] = __builtin_amdgcn_rcpf(xsum(a.l[t]));
  }
#pragma unroll
  for (int t = 0; t < NQ; ++t)
#pragma unroll
    for (int m = 0; m < 4; ++m) *(LAS u32x4*)(lds + OWN3 + (2 * w + t) * 4096 + m * 1024 + lane * 16) = o_frag(a, m, t);
  prog_post(lds + QPROG + w * 4, 33);
  __builtin_amdgcn_s_setprio(0);
  ST();
}

DI void attn_wave(const int w, const int lane, const int rot, lptr lds, const unsigned l0, rsrc_t rk, rsrc_t rv, rsrc_t rx, rsrc_t rwq, const Ctx& c0, int& nst) {
  const int lg = lane >> 4, lr = lane & 15;
  ST();
  {
    const unsigned wvo = lg * 8192 + ((lr ^ (lg << 2)) << 4) + c0.g * 1024;
    const unsigned xvo = (lane >> 3) * 1280 + (((lane & 7) ^ ((4 * (w & 1) + lg) & 7)) << 4);
    auto issue = [&](int c) {
      const int cr = c + rot >= 10 ? c + rot - 10 : c + rot;
      const unsigned wd = l0 + VOFF + (c & 3) * PBUF + w * 1024, xd = wd + SLOT, xs = w * 10240 + cr * 128, ws_ = (cr * 64 + 4 * w) * 8192;
#pragma unroll
      for (int i = 0; i < 3; ++i) dma_df(rx, xvo, xs + i * 40960, xd + i * 4096);
      dma_df(rx, xvo, xs + 3 * 40960, w < 2 ? xd + 3 * 4096 : l0 + DUMMY);
#pragma unroll
      for (int i = 0; i < 4; ++i) dma_df(rwq, wvo, ws_ + i * 131072, wd + i * 4096);
    };
    issue(0); issue(1); issue(2);
    if (w == 0) { *(LAS u32x4*)(lds + CNT + (lane & 7) * 16) = u32x4{0u, 0u, 0u, 0u}; }
    const int b = c0.b, g = c0.g;
    u32x2 rq2[8]; unsigned long long e1;
    const int ti = (b - 20) * 4 + w, tk = b < 20 ? b : ti >> 1, tu = b < 20 ? w : 4 + (ti & 1);
    const unsigned qv = lg * 8 * 8192 + lr * 8 + g * 1024 + 256, sq = tk * 32 * 8192 + tu * 128;
    constexpr int TN[11] = {0, 0, 0, 9, 0, 0, 0, 4, 0, 0, 0};
#pragma unroll
    for (int c = 0; c < 8; ++c) {
      SB(); asm volatile("s_waitcnt vmcnt(%0) lgkmcnt(0)\n\ts_barrier" :: "i"(16 + TN[c] + TN[c + 1] + TN[c + 2]) : "memory"); SB();
      if (c == 4) {
#pragma unroll
        for (int i = 0; i < 8; ++i) PIN(rq2[i]);
        PIN(e1);
      }
      SB();
      if (c < 7) issue(c + 3);
      SB();
      if (c == 0) {
        asm volatile("global_load_dwordx2 %0, %1, %2 sc1" : "=v"(e1) : "v"(0u), "s"(c0.epoch()) : "memory");
#pragma unroll
        for (int jj = 0; jj < 8; ++jj) asm volatile("buffer_load_dwordx2 %0, %1, %2, %3 offen" : "=v"(rq2[jj]) : "v"(qv), "s"(rwq), "s"(sq + jj * 8192) : "memory");
      }
      if (c == 4) {
#pragma unroll
        for (int e = 0; e < 4; ++e) { u32x4 f;
#pragma unroll
          for (int pp = 0; pp < 4; ++pp) f[pp] = __builtin_amdgcn_perm(rq2[2 * pp + 1][e >> 1], rq2[2 * pp][e >> 1], (e & 1) ? 0x07060302u : 0x05040100u);
          __builtin_amdgcn_raw_buffer_store_b128(f, c0.rqt(), (tk * 6 + tu) * 4096 + e * 1024 + lane * 16, 0, 16); }
      }
    }
    {
      const unsigned long long tag = (((unsigned long long)rfl((unsigned)(e1 >> 32)) << 32) | rfl((unsigned)e1)) + 1;
      *(LAS unsigned long long*)(lds + TAGW) = tag;
      publish(c0.tflags() + b, lds + CNT + 16, tag, lane);
    }
    const unsigned kvo = lg * 256 + ((lr ^ lg) << 4);
#pragma unroll
    for (int t = 0; t < 2; ++t) {
      const unsigned o = t * SLOT + w * 4096;
      dma_nt<0>(rk, kvo, o, l0 + o); dma_nt<1024>(rk, kvo ^ 64, o, l0 + o); dma_nt<2048>(rk, kvo ^ 128, o, l0 + o); dma_nt<3072>(rk, kvo ^ 192, o, l0 + o);
      BAR_L();
    }
  }
  if (w < 3) attn_main<2>(w, lds, l0, rk, rv, c0, nst); else attn_main<1>(w, lds, l0, rk, rv, c0, nst);
}

constexpr int LAG = 3;
DI void parrive(unsigned& pc, const int j, lptr lds) { WAITL(); ++pc; prog_post(lds + PPROG + j * 4, pc); }
template <int NE> constexpr int efrag(int e) { return NE == 5 ? e : (NE == 3 ? (e < 2 ? e : 4) : e + 2); }
template <int NE> DI u32x4 wload(rsrc_t rwt, const unsigned lv, const unsigned so, const int e) {
  return __builtin_amdgcn_raw_buffer_load_b128(rwt, lv + (efrag<NE>(e) & 3) * 1024, so + (efrag<NE>(e) >> 2) * 4096, 16);
}
DI void send_flag(const Ctx& c, const int dst, const int part, const unsigned lv, lptr lds) {
  const unsigned long long tag = *(const LAS unsigned long long*)(lds + TAGW);
  asm volatile("s_waitcnt vmcnt(0)" ::: "memory");
  if (lv == 0) __hip_atomic_store(c.xflag(dst) + c.g + part, tag, RLX_AGENT);
}
template <int NE>
DI void oproj_pass(const int j, const bool pstv, const Ctx& c0, lptr lds, const unsigned l0, int& nst) {
  const int lane = fresh_lane();
  const Ctx c = c0.fresh();
  const int mrot = ROT ? c.b : 0, side = (c.g ^ (NE != 5)) & 1;
  const rsrc_t rwt = c.rwt();
  const unsigned pa = l0 + APROG + (lane & 3) * 4, lv = lane * 16, tb = (side * 64 + j) * 5120;
  f32x4 acc[NE][7]; u32x4 af[4][NE], bf[4];
#pragma unroll
  for (int k = 0; k < 3; ++k)
#pragma unroll
    for (int e = 0; e < NE; ++e) { af[k][e] = wload<NE>(rwt, lv, tb + ((k + mrot) & 3) * 20480, e); SB(); }
#pragma unroll
  for (int e = 0; e < NE; ++e)
#pragma unroll
    for (int t = 0; t < 7; ++t) { acc[e][t] = f32x4{0.f, 0.f, 0.f, 0.f}; PIN(acc[e][t]); }
  asm volatile("s_nop 1"); SB();
#pragma nounroll
  for (int r = 0; r < 3; ++r) {
    const unsigned reg = qreg(r + 1);
    if (NE != 2) prog_wait<32, 2>(pa, 8 * r + 9);
    if (NE == 2 && r == 2) send_flag(c0.fresh(), 2 * j + (side ^ 1), 0, lv, lds);
    PST();
    auto baddr = [&](int gi) { return lp(l0 + reg + ((gi / 7 + mrot) & 3) * 1024 + lv) + (gi % 7) * 4096; };
#pragma unroll
    for (int gi = 0; gi < LAG; ++gi) bf[gi] = lds128(baddr(gi));
    SB();
#pragma unroll
    for (int k = 0; k < 4; ++k) {
      int r2 = r; asm volatile("" : "+s"(r2));
      const unsigned so = tb + ((k ? r2 + 1 : r2) * 4 + ((k + 3 + mrot) & 3)) * 20480;
#pragma unroll
      for (int t = 0; t < 7; ++t) {
        const int gi = 7 * k + t;
#pragma unroll
        for (int e = 0; e < NE; ++e) {
          mfma_ip(acc[e][t], af[k][e], bf[gi & 3]);
          if (e == 0 && gi + LAG < 28) bf[(gi + LAG) & 3] = lds128(baddr(gi + LAG));
          if (e == NE / 2 && t >= 1 && t <= NE) af[(k + 3) & 3][t - 1] = wload<NE>(rwt, lv, so, t - 1);
          if (NE == 5 && OSL && e == 4) __builtin_amdgcn_s_sleep(OSL);
          SB();
        }
      }
    }
    PST();
  }
#pragma unroll
  for (int e = 0; e < NE; ++e) af[3][e] = wload<NE>(rwt, lv, tb + (12 + ((3 + mrot) & 3)) * 20480, e);
  if (NE == 5 && WARM) {
    prog_wait<0, 8>(pa, WARM);
#pragma unroll
    for (int i = 0; i < 10; ++i) dma_sc1(rwt, lv, (side ^ 1) * 327680 + ((c.b & 7) * 40 + j * 10 + i) * 1024, l0 + DUMMY);
  }
  if (NE == 5 && TOUCH) {
    if (j == 0) touch(mkrsrc(c.ws + WS_PX + (size_t)c.b * 8 * 143360, 8 * 143360), lv * 2048, l0 + DUMMY);
    if (j == 1) touch(mkrsrc(c.out + (size_t)c.b * 143360, 143360), lv * 2048, l0 + DUMMY);
    if (j == 2) touch(rwt, lv * 2048, l0 + DUMMY);
  }
  if (NE != 2) prog_wait<32, 1>(pa, 33);
  if (NE == 5) __builtin_amdgcn_s_setprio(2);
  PST();
  const Ctx c2 = c0.fresh(); const int dst = 2 * j + side;
  const bool keep = NE == 5 && dst == c2.g;
  const rsrc_t rpx = c2.rpx(dst); const unsigned po = c2.g * 17920;
  unsigned ba[4];
#pragma unroll
  for (int k = 0; k < 4; ++k) ba[k] = l0 + OWN3 + ((k + mrot) & 3) * 1024 + lv;
  auto out = [&](int t, int part) {
    if (keep) { *(LAS f32x4*)(lp(l0 + OWNP + lv) + (t * 5 + part) * 1024) = acc[part % NE][t]; if (part == 3) *(LAS f32x4*)(lp(l0 + OWNP + lv) + (t * 5 + 4) * 1024) = acc[NE - 1][t]; }
    else if (part < NE / 2) {
      const f32x4 x = acc[2 * part][t], y = acc[2 * part + 1][t];
      __builtin_amdgcn_raw_buffer_store_b128(u32x4{cvtpk(x[0], y[0]), cvtpk(x[1], y[1]), cvtpk(x[2], y[2]), cvtpk(x[3], y[3])}, rpx, po + lv + t * 2560 + efrag<NE>(2 * part) * 512, 0, 16);
    } else if (part == 2 && (NE & 1)) { const f32x4 x = acc[NE - 1][t];
      __builtin_amdgcn_raw_buffer_store_b64(u32x2{cvtpk(x[0], x[1]), cvtpk(x[2], x[3])}, rpx, po + (lv >> 1) + t * 2560 + 2048, 0, 16); }
  };
#pragma unroll
  for (int gi = 0; gi < LAG; ++gi) bf[gi] = lds128(lp(ba[gi & 3]) + (gi >> 2) * 4096);
  SB();
#pragma unroll
  for (int gi = 0; gi < 28; ++gi) {
    const int t = gi >> 2, k = gi & 3;
#pragma unroll
    for (int e = 0; e < NE; ++e) {
      mfma_ip(acc[e][t], af[k][e], bf[gi & 3]);
      if (e == 0 && gi + LAG < 28) bf[(gi + LAG) & 3] = lds128(lp(ba[(gi + LAG) & 3]) + ((gi + LAG) >> 2) * 4096);
      if (e == NE - 1 && k == 0 && t > 0) {
        asm volatile("s_nop %0" :: "i"(8 - NE));
#pragma unroll
        for (int e2 = 0; e2 < NE; ++e2) PIN(acc[e2][t - 1]);
      }
      if (e == NE - 1 && t > 0) out(t - 1, k);
      SB();
    }
  }
  MFMA_DONE();
#pragma unroll
  for (int e2 = 0; e2 < NE; ++e2) PIN(acc[e2][6]);
  SB();
#pragma unroll
  for (int k = 0; k < 4; ++k) out(6, k);
  if (NE == 5) __builtin_amdgcn_s_setprio(TPRIO);
  if (keep) prog_post(lds + KPROG, 1);
  if (NE != 5) { send_flag(c2, dst, NE == 2 ? 8 : 0, lv, lds); __builtin_amdgcn_s_waitcnt(0x0F70); }
  PST();
}

DI void oproj_res(const int j, const Ctx& c0, lptr lds, const unsigned l0, int& nst) {
  const int lane = fresh_lane();
  const bool pstv = j == 0; (void)pstv;
  const Ctx c = c0.fresh();
  const int side = (c.g ^ 1) & 1, dst = 2 * j + side;
  const rsrc_t rwt = c.rwt(), rpx = c.rpx(dst);
  const unsigned lv = lane * 16, tb = (side * 64 + j) * 5120, po = c.g * 17920 + 1024 + lv;
  f32x4 acc[2][7]; u32x4 af[16][2], bf[4];
#pragma unroll
  for (int i = 0; i < 16; ++i)
#pragma unroll
    for (int e = 0; e < 2; ++e) { af[i][e] = wload<2>(rwt, lv, tb + i * 20480, e); SB(); }
  auto brd = [&](int n) { const int r = n / 28, gi = n % 28, k = r < 3 ? gi / 7 : gi & 3, t = r < 3 ? gi % 7 : gi >> 2;
    const int off = (r == 0 ? RB : (r == 1 ? RA : (r == 2 ? RC : OWN3))) + t * 4096 + k * 1024;
    bf[n & 3] = lds128(lp(l0 + lv + (off & ~0xFFFF)) + (off & 0xFFFF)); };
  auto out = [&](int t) { const f32x4 x = acc[0][t], y = acc[1][t];
    __builtin_amdgcn_raw_buffer_store_b128(u32x4{cvtpk(x[0], y[0]), cvtpk(x[1], y[1]), cvtpk(x[2], y[2]), cvtpk(x[3], y[3])}, rpx, po + t * 2560, 0, 16); };
#pragma unroll
  for (int t = 0; t < 7; ++t) { acc[0][t] = acc[1][t] = f32x4{0.f, 0.f, 0.f, 0.f}; PIN(acc[0][t]); PIN(acc[1][t]); }
#pragma unroll
  for (int n = 0; n < LAG; ++n) brd(n);
  asm volatile("s_nop 1"); SB();
  auto head = [&](auto R) {
#pragma unroll
    for (int gi = 0; gi < 28; ++gi) {
      constexpr int r = decltype(R)::value;
      const int n = 28 * r + gi, k = r < 3 ? gi / 7 : gi & 3, t = r < 3 ? gi % 7 : gi >> 2;
      if (n == 28) send_flag(c0.fresh(), 2 * j + (side ^ 1), 0, lv, lds);
      mfma_ip(acc[0][t], af[4 * r + k][0], bf[n & 3]);
      if (n + LAG < 112) brd(n + LAG);
      SB();
      mfma_ip(acc[1][t], af[4 * r + k][1], bf[n & 3]);
      if (r == 3 && k == 1 && t > 0) { asm volatile("s_nop 3"); PIN(acc[0][t - 1]); PIN(acc[1][t - 1]); out(t - 1); }
      SB();
    }
    PST();
  };
  head(IC<0>{}); head(IC<1>{}); head(IC<2>{}); head(IC<3>{});
  MFMA_DONE(); PIN(acc[0][6]); PIN(acc[1][6]); SB();
  out(6);
  send_flag(c0.fresh(), dst, 8, lv, lds);
  PST();
}

DI void final_part(const int j, const Ctx& c0, lptr lds, const unsigned l0, char* out, int& nst) {
  const Ctx c = c0.fresh(); const int b = c.b, g = c.g; const rsrc_t rpx = c.rpx(g);
  const int lane = fresh_lane(), p = g >> 1, lg = lane >> 4, lr = lane & 15;
  const bool pstv = false; (void)pstv;
  const unsigned long long tg = *(const LAS unsigned long long*)(lds + TAGW), tag = ((unsigned long long)rfl((unsigned)(tg >> 32)) << 32) | rfl((unsigned)tg);
  auto lo = [](unsigned v) { return __uint_as_float(v << 16); };
  auto hi = [](unsigned v) { return __uint_as_float(v & 0xffff0000u); };
  f32x4 a[2][5];
#pragma unroll
  for (int st = 0; st < 3; ++st) {
    const int ns = st ? 4 : 3;
    {
      const int k = lane & 3;
      gu64* f = c.xflag(g) + (st ? 2 * k + 1 - (g & 1) + (st == 2 ? 8 : 0) : 2 * (k == p ? (p + 1) & 3 : k) + (g & 1));
      wait_flag(f, __hip_atomic_load(f, RLX_AGENT), tag);
    }
    u32x4 in[2][4][2]; u32x2 in2[2][4];
#pragma unroll
    for (int i = 0; i < 2; ++i)
#pragma unroll
      for (int k = 0; k < ns; ++k) {
        const int src = st ? 2 * k + 1 - (g & 1) : 2 * (k + (k >= p)) + (g & 1);
        const unsigned o = (i == 1 && j == 3) ? OOB : src * 17920 + (j + 4 * i) * 2560;
        if (st != 2) in[i][k][0] = __builtin_amdgcn_raw_buffer_load_b128(rpx, lane * 16 + o, 0, 16);
        if (st != 1) in[i][k][1] = __builtin_amdgcn_raw_buffer_load_b128(rpx, lane * 16 + o + 1024, 0, 16);
        if (st != 2) in2[i][k] = __builtin_amdgcn_raw_buffer_load_b64(rpx, lane * 8 + o + 2048, 0, 16);
      }
    if (st == 0) {
      prog_wait<0, 1>(l0 + KPROG, 1);
#pragma unroll
      for (int i = 0; i < 2; ++i)
#pragma unroll
        for (int e = 0; e < 5; ++e) a[i][e] = *(LAS f32x4*)(lds + OWNP + (((i == 1 && j == 3) ? 0 : j + 4 * i) * 5 + e) * 1024 + lane * 16);
    }
#pragma unroll
    for (int i = 0; i < 2; ++i)
#pragma unroll
      for (int k = 0; k < ns; ++k) {
#pragma unroll
        for (int d = (st == 2 ? 4 : 0); d < (st == 1 ? 4 : 8); ++d) { const int reg = d & 3, e = 2 * (d >> 2); a[i][e][reg] += lo(in[i][k][d >> 2][d & 3]); a[i][e + 1][reg] += hi(in[i][k][d >> 2][d & 3]); }
        if (st != 2) {
#pragma unroll
          for (int d = 0; d < 2; ++d) { a[i][4][2 * d] += lo(in2[i][k][d]); a[i][4][2 * d + 1] += hi(in2[i][k][d]); }
        }
      }
    PST();
  }
  if (j == 0 && g == 0 && b == 0 && lane == 0) __hip_atomic_store(c.epoch(), tag, RLX_AGENT);
  const rsrc_t rout = mkrsrc(out + (size_t)b * 143360, 143360);
#pragma unroll
  for (int i = 0; i < 2; ++i) {
    const unsigned ob = (i == 1 && j == 3) ? OOB : ((j + 4 * i) * 16 + lr) * 1280 + (g & 1) * 640;
#pragma unroll
    for (int hf = 0; hf < 2; ++hf)
      __builtin_amdgcn_raw_buffer_store_b128(u32x4{cvtpk(a[i][0][2 * hf], a[i][1][2 * hf]), cvtpk(a[i][2][2 * hf], a[i][3][2 * hf]), cvtpk(a[i][0][2 * hf + 1], a[i][1][2 * hf + 1]),
                                                   cvtpk(a[i][2][2 * hf + 1], a[i][3][2 * hf + 1])}, rout, ob + p * 128 + lg * 32 + hf * 16, 0, 0);
    __builtin_amdgcn_raw_buffer_store_b64(u32x2{cvtpk(a[i][4][0], a[i][4][1]), cvtpk(a[i][4][2], a[i][4][3])}, rout, ob + 512 + p * 32 + lg * 8, 0, 0);
  }
}

template <int NE> struct QpState { f32x4 acc[NE][7]; u32x4 xf[7]; lptr xa0, xa1; };
template <int NE> DI void qp_init(QpState<NE>& q, lptr xb, int lane) {
  const int lg = lane >> 4, lr = lane & 15;
  q.xa0 = xb + lr * 128 + ((lg ^ (lr >> 1)) << 4); q.xa1 = xb + lr * 128 + (((4 + lg) ^ (lr >> 1)) << 4);
#pragma unroll
  for (int t = 0; t < 7; ++t) { q.xf[t] = u32x4{0u, 0u, 0u, 0u}; PIN(q.xf[t]);
#pragma unroll
    for (int e = 0; e < NE; ++e) { q.acc[e][t] = f32x4{0.f, 0.f, 0.f, 0.f}; PIN(q.acc[e][t]); } }
  asm volatile("s_nop 1"); SB();
}
template <int PAR, int NE, bool DEEP>
DI void qp2_chunk(QpState<NE>& q, u32x4 (&af)[4][NE], const int c, const int j, const int rot, unsigned& pc, const unsigned pa, const unsigned xvo, const unsigned lv, const unsigned tb,
                  lptr lds, const unsigned l0, rsrc_t rx, rsrc_t rqt) {
  SB(); asm volatile("s_waitcnt vmcnt(%0) lgkmcnt(0)" :: "i"(DEEP ? 6 * NE + 8 : 2 * NE) : "memory"); SB();
  if (DEEP) asm volatile("s_cmp_gt_u32 %0, 1\n\ts_cbranch_scc1 1f\n\ts_waitcnt vmcnt(12)\n1:" :: "s"(c) : "memory", "scc");
  ++pc; prog_post(lds + PPROG + j * 4, pc);
  const unsigned pg = prog_read<16>(pa);
  const int c1 = c + 1 + rot >= 10 ? c + 1 + rot - 10 : c + 1 + rot, c2 = c + 2 + rot >= 10 ? c + 2 + rot - 10 : c + 2 + rot;
  const unsigned s1 = c < 9 ? (2 * c1 + 1) * 24576 + tb : OOB, s2 = c < 8 ? (2 * c2) * 24576 + tb : OOB;
  lptr x0 = q.xa0 + (DEEP ? (c & 2) * XCH : 0), x1 = q.xa1 + (DEEP ? (c & 2) * XCH : 0);
  SB();
#pragma unroll
  for (int u = 0; u < 14; ++u) {
    const int v = (u + 14 - LAG) % 14, t = v % 7, set = u < LAG ? 2 * (PAR ^ 1) + 1 : 2 * PAR + v / 7;
#pragma unroll
    for (int e = 0; e < NE; ++e) {
      mfma_ip(q.acc[e][t], af[set][e], q.xf[t]);
      if (e == 0 && u >= 2) q.xf[u % 7] = lds128((u >= 7 ? x1 : x0) + PAR * XCH + (u % 7) * 2048);
      if (e == NE / 2 && u >= LAG && u < LAG + NE) af[2 * (PAR ^ 1) + 1][u - LAG] = __builtin_amdgcn_raw_buffer_load_b128(rqt, lv + (u - LAG) * 1024, s1, 16);
      if (e == NE / 2 && u >= 10 && u < 10 + NE) af[2 * PAR][u - 10] = __builtin_amdgcn_raw_buffer_load_b128(rqt, lv + (u - 10) * 1024, s2, 16);
      SB();
    }
    if (u == 0) { prog_wait<16, 0>(pa, pg, pc); q.xf[0] = lds128(x0 + PAR * XCH); q.xf[1] = lds128(x0 + PAR * XCH + 2048); SB(); }
    if (u == 1) {
      const bool on = DEEP ? c < 8 : c < 9;
      const int cx = !DEEP ? c1 : (c + 3 + rot >= 10 ? c + 3 + rot - 10 : c + 3 + rot);
      const unsigned xs = on ? j * 10240 + cx * 128 : OOB, xd = l0 + (on ? (DEEP ? RB + ((c + 3) & 3) * XCH : RC + (PAR ^ 1) * XCH) + j * 1024 : DUMMY), st = on ? 4096u : 0u;
#pragma unroll
      for (int i = 0; i < 3; ++i) dma_df(rx, xvo, xs + i * 40960, xd + i * st);
      dma_df(rx, xvo, xs + 3 * 40960, j < 2 ? xd + 3 * st : l0 + DUMMY);
      SB();
    }
  }
}
template <int NE> DI void qp2_first(u32x4 (&af)[4][NE], const int rot, const unsigned lv, const unsigned tb, rsrc_t rqt) {
  const int r1 = rot == 9 ? 0 : rot + 1;
#pragma unroll
  for (int k = 0; k < 3; ++k)
#pragma unroll
    for (int e = 0; e < NE; ++e) { af[k][e] = __builtin_amdgcn_raw_buffer_load_b128(rqt, lv + e * 1024, (k < 2 ? 2 * rot + k : 2 * r1) * 24576 + tb, 16); SB(); }
#pragma unroll
  for (int e = 0; e < NE; ++e) { af[3][e] = u32x4{0u, 0u, 0u, 0u}; PIN(af[3][e]); }
}
template <int NE> DI void qp_tail(QpState<NE>& q, const u32x4 (&a)[NE]) {
#pragma unroll
  for (int t = 7 - LAG; t < 7; ++t)
#pragma unroll
    for (int e = 0; e < NE; ++e) mfma_ip(q.acc[e][t], a[e], q.xf[t]);
  MFMA_DONE(); SB();
}

DI void proj_wave(const int j, const int lane, const Ctx& c0, const int rot, lptr lds, const unsigned l0, rsrc_t rx, const char* wo, int& nst) {
  unsigned long long tag, tv;
  const bool pstv = j == 0; (void)pstv;
  const int lg = lane >> 4, lr = lane & 15;
  {
    QpState<2> q;
    u32x4 af[2][2];
    af[1][0] = af[1][1] = u32x4{0u, 0u, 0u, 0u}; PIN(af[1][0]); PIN(af[1][1]);
    qp_init(q, lds + VOFF + SLOT, lane);
    const int q4 = lr >> 2;
    const unsigned wa = l0 + VOFF + (8 * lg + q4) * 256 + (((j ^ q4) * 4 + (lane & 3)) << 4);
#pragma nounroll
    for (int c = 0; c < 10; ++c) {
      const int bo = (c & 3) * PBUF;
      BAR_L();
#pragma unroll
      for (int u = 0; u < 14; ++u) {
        const int v = (u + 14 - LAG) % 14, t = v % 7, set = u < LAG ? 1 : v / 7;
        auto ard = [&](int ks, int e) { lptr p = lp(wa + bo) + ks * 8192 + e * 8; const u32x2 lo = ldstr(p), hi = ldstr(p + 1024); af[ks][e] = u32x4{lo[0], lo[1], hi[0], hi[1]}; };
        if (u == 0) { ard(0, 0); q.xf[0] = lds128(q.xa0 + bo); ard(0, 1); SB(); }
#pragma unroll
        for (int e = 0; e < 2; ++e) {
          mfma_ip(q.acc[e][t], af[set][e], q.xf[t]);
          if (e == 0 && u >= 1) q.xf[u % 7] = lds128((u >= 7 ? q.xa1 : q.xa0) + bo + (u % 7) * 2048);
          if (e == 1 && (u == 4 || u == 5)) ard(1, u - 4);
          SB();
        }
      }
      PST();
    }
    tv = __hip_atomic_load(c0.fresh().tflags() + (lane & 31), RLX_AGENT);
    qp_tail(q, af[1]);
    BAR_L();
#pragma unroll
    for (int t = 0; t < 7; ++t)
      *(LAS u32x4*)(lds + RA + t * 4096 + j * 1024 + lane * 16) = u32x4{cvtpk(q.acc[0][t][0] * QSCALE, q.acc[0][t][1] * QSCALE), cvtpk(q.acc[0][t][2] * QSCALE, q.acc[0][t][3] * QSCALE),
                                                                        cvtpk(q.acc[1][t][0] * QSCALE, q.acc[1][t][1] * QSCALE), cvtpk(q.acc[1][t][2] * QSCALE, q.acc[1][t][3] * QSCALE)};
    { const unsigned long long tg = *(const LAS unsigned long long*)(lds + TAGW); tag = ((unsigned long long)rfl((unsigned)(tg >> 32)) << 32) | rfl((unsigned)tg); }
    BAR_L();
    PST();
  }
  unsigned pc = 0;
  {
    const Ctx c = c0.fresh();
    const rsrc_t rqt = c.rqt();
    const unsigned pa = l0 + APROG + (lane & 3) * 4, lv = lane * 16;
    const unsigned xvo = (lane >> 3) * 1280 + (((lane & 7) ^ ((4 * (j & 1) + lg) & 7)) << 4);
    auto xfirst = [&](int k) {
      const unsigned xs = j * 10240 + (k + rot >= 10 ? k + rot - 10 : k + rot) * 128, xd = l0 + RB + k * XCH + j * 1024;
#pragma unroll
      for (int i = 0; i < 3; ++i) dma_df(rx, xvo, xs + i * 40960, xd + i * 4096);
      dma_df(rx, xvo, xs + 3 * 40960, j < 2 ? xd + 3 * 4096 : l0 + DUMMY);
    };
    wait_flag(c.tflags() + (lane & 31), tv, tag);
    u32x4 a2[4][2];
    {
      QpState<4> q; u32x4 af[4][4];
      xfirst(0);
      qp2_first(af, rot, lv, j * 4096, rqt);
      xfirst(1); xfirst(2);
      qp_init(q, lds + RB, lane);
      PST();
#pragma nounroll
      for (int cc = 0; cc < 10; cc += 2) {
        qp2_chunk<0, 4, true>(q, af, cc, j, rot, pc, pa, xvo, lv, j * 4096, lds, l0, rx, rqt);
        qp2_chunk<1, 4, true>(q, af, cc + 1, j, rot, pc, pa, xvo, lv, j * 4096, lds, l0, rx, rqt);
        PST();
      }
      qp_tail(q, af[3]);
      parrive(pc, j, lds);
      __builtin_amdgcn_s_setprio(XPRIO);
      u32x4 qf[7][2];
#pragma unroll
      for (int t = 0; t < 7; ++t)
#pragma unroll
        for (int hf = 0; hf < 2; ++hf) {
          qf[t][hf] = u32x4{cvtpk(q.acc[0][t][2 * hf] * QSCALE, q.acc[1][t][2 * hf] * QSCALE), cvtpk(q.acc[2][t][2 * hf] * QSCALE, q.acc[3][t][2 * hf] * QSCALE),
                            cvtpk(q.acc[0][t][2 * hf + 1] * QSCALE, q.acc[1][t][2 * hf + 1] * QSCALE), cvtpk(q.acc[2][t][2 * hf + 1] * QSCALE, q.acc[3][t][2 * hf + 1] * QSCALE)};
          PIN(qf[t][hf]);
        }
      prog_wait<16, 0>(pa, pc);
      prog_wait<32, 0>(pa, 1);
#pragma unroll
      for (int t = 0; t < 7; ++t)
#pragma unroll
        for (int hf = 0; hf < 2; ++hf) *(LAS u32x4*)(lds + ((j >> 1) ? RA : RB) + (4 * (j & 1) + lg) * 512 + lr * 16 + t * 4096 + hf * 256) = qf[t][hf];
      parrive(pc, j, lds);
      __builtin_amdgcn_s_setprio(0);
      {
        const Ctx cc = c0.fresh(); const int b = cc.b, g = cc.g; const rsrc_t rwo = mkrsrc(wo, 4096 * 1280);
        u32x2 rw[8]; unsigned short r5[8];
        const unsigned rb = 4 * lg * 1280 + (b >> 4) * 640, wv0 = rb + j * 128 + lr * 8, wv1 = rb + 512 + j * 32 + lr * 2;
        const unsigned s2 = ((4 * g + ((b >> 2) & 3)) * 128 + (b & 3) * 32) * 1280;
#pragma unroll
        for (int jj = 0; jj < 8; ++jj) rw[jj] = __builtin_amdgcn_raw_buffer_load_b64(rwo, wv0 + (jj & 3) * 1280, s2 + (jj >> 2) * 16 * 1280, 0);
#pragma unroll
        for (int jj = 0; jj < 8; ++jj) r5[jj] = __builtin_amdgcn_raw_buffer_load_b16(rwo, wv1 + (jj & 3) * 1280, s2 + (jj >> 2) * 16 * 1280, 0);
#pragma unroll
        for (int e = 0; e < 5; ++e) { u32x4 f;
#pragma unroll
          for (int pp = 0; pp < 4; ++pp) f[pp] = e < 4 ? __builtin_amdgcn_perm(rw[2 * pp + 1][(e >> 1) & 1], rw[2 * pp][(e >> 1) & 1], (e & 1) ? 0x07060302u : 0x05040100u)
                                                       : __builtin_amdgcn_perm((unsigned)r5[2 * pp + 1], (unsigned)r5[2 * pp], 0x05040100u);
          __builtin_amdgcn_raw_buffer_store_b128(f, cc.rwt(), (b * 4 + j) * 5120 + e * 1024 + lv, 0, 16); }
        qp2_first(a2, rot, lv, 16384 + j * 2048, rqt);
        publish(cc.tflags() + 32 + b, lds + CNT + 20, tag, lane);
      }
      PST();
    }
    QpState<2> q;
    qp_init(q, lds + RC, lane);
#pragma nounroll
    for (int cc = 0; cc < 10; cc += 2) {
      qp2_chunk<0, 2, false>(q, a2, cc, j, rot, pc, pa, xvo, lv, 16384 + j * 2048, lds, l0, rx, rqt);
      qp2_chunk<1, 2, false>(q, a2, cc + 1, j, rot, pc, pa, xvo, lv, 16384 + j * 2048, lds, l0, rx, rqt);
    }
    qp_tail(q, a2[3]);
    parrive(pc, j, lds);
    prog_wait<16, 0>(pa, pc);
    lptr qp = lds + RC + (8 * (j >> 1) + 2 * lg) * 256 + lr * 16 + (j & 1) * 4;
#pragma unroll
    for (int t = 0; t < 7; ++t)
#pragma unroll
      for (int r = 0; r < 4; ++r) *(LAS unsigned*)(qp + t * 4096 + (r >> 1) * 256 + (r & 1) * 8) = cvtpk(q.acc[0][t][r] * QSCALE, q.acc[1][t][r] * QSCALE);
    wait_flag(c.tflags() + 32 + (lane & 31), __hip_atomic_load(c.tflags() + 32 + (lane & 31), RLX_AGENT), tag);
    parrive(pc, j, lds);
    PST();
  }
  __builtin_amdgcn_s_waitcnt(0x0F70);
}

__global__ __launch_bounds__(512, 2) void attn_fused(const char* __restrict__ x, const char* __restrict__ K, const char* __restrict__ V,
                                                     const char* __restrict__ wq, const char* __restrict__ wo, char* __restrict__ ws, char* __restrict__ out) {
  extern __shared__ __attribute__((aligned(1024))) char smem[];
  const int w = rfl(threadIdx.x >> 6);
  unsigned long long pc0 = 0;
  if (CODEPF) asm volatile("s_getpc_b64 %0" : "=s"(pc0));
  const int bid = blockIdx.x;
  const int b = ((bid >> 1) & 3) * 8 + (bid >> 5), g = (bid & 1) * 4 + ((bid >> 3) & 3);
  const int rot = ROT ? (((b & 7) * 5) / 4 + ((g & 3) * 5) / 2) % 10 : 0;
  lptr lds = (lptr)smem;
  const unsigned l0 = (unsigned)(uintptr_t)lds;
  const Ctx c = {ws, out, b, g};
  const rsrc_t rx = mkrsrc(x + (size_t)b * 143360, 143360), rwq = mkrsrc(wq, 640 * 8192);
  int nst = 0; (void)nst;
  if (w < 4) {
    const size_t kvb = ((size_t)b * 32 + g * 4) * 131072;
    attn_wave(w, fresh_lane(), rot, lds, l0, mkrsrc(K + kvb, 524288), mkrsrc(V + kvb, 524288), rx, rwq, c, nst);
    oproj_pass<3>(w, false, c, lds, l0, nst);
    ST(); ST();
    final_part(w, c, lds, l0, out, nst);
  } else {
    if (CODEPF && w < 6) dma_df(mkrsrc((const void*)(pc0 & ~255ull), CODEPF), fresh_lane() * 16, ((blockIdx.x >> 3) * 2 + (w - 4)) * 1024, l0 + DUMMY + 1024);
    if (TOUCH) {
      const unsigned ln = fresh_lane(); const size_t kvb = ((size_t)b * 32 + g * 4) * 131072;
      if (w == 4) touch(mkrsrc(V + kvb, 524288), ln * 65536, l0 + DUMMY);
      if (w == 5) touch(mkrsrc(K + kvb, 524288), ln * 65536, l0 + DUMMY);
      if (w == 6) touch(c.rqt(), ln * 65536, l0 + DUMMY);
      if (w == 7) touch(mkrsrc(ws, 262144), ln * 65536 + 4096, l0 + DUMMY);
    }
    proj_wave(w - 4, fresh_lane(), c, rot, lds, l0, rx, wo, nst);
    oproj_pass<5>(w - 4, w == 4, c, lds, l0, nst);
    oproj_res(w - 4, c, lds, l0, nst);
    __builtin_amdgcn_s_waitcnt(0x0F70);
  }
}

extern "C" void kernel_launch(void* const* d_in, const int* in_sizes, int n_in, void* d_out, int out_size, void* d_ws, size_t ws_size, hipStream_t stream) {
  (void)hipFuncSetAttribute((const void*)attn_fused, hipFuncAttributeMaxDynamicSharedMemorySize, LDS_BYTES);
  attn_fused<<<256, 512, LDS_BYTES, stream>>>((const char*)d_in[0], (const char*)d_in[1], (const char*)d_in[2], (const char*)d_in[3], (const char*)d_in[4], (char*)d_ws + WSOFF, (char*)d_out);
}
```

```cpp
#include <hip/hip_runtime.h>
#include <stdint.h>
typedef __attribute__((ext_vector_type(8))) __bf16 bf16x8;
typedef __attribute__((ext_vector_type(2))) __bf16 bf16x2;
typedef __attribute__((ext_vector_type(4))) float f32x4;
typedef __attribute__((ext_vector_type(16))) float f32x16;
typedef __attribute__((ext_vector_type(2))) float f32x2;
typedef __attribute__((ext_vector_type(4))) unsigned u32x4;
typedef __attribute__((ext_vector_type(2))) unsigned u32x2;
typedef __attribute__((ext_vector_type(4))) short s16x4;
#define LAS __attribute__((address_space(3)))
#define DI __device__ __forceinline__
typedef LAS char* lptr;
typedef __amdgpu_buffer_rsrc_t rsrc_t;
typedef __attribute__((address_space(1))) unsigned long long gu64;
#define RLX_AGENT __ATOMIC_RELAXED, __HIP_MEMORY_SCOPE_AGENT

constexpr int SLOT = 16384, VOFF = 2 * SLOT, XCH = 14336, RA = 4 * SLOT, RB = RA + 2 * XCH, RC = RB + 2 * XCH, PBUF = SLOT + XCH, DUMMY = VOFF + 4 * PBUF,
              TAGW = DUMMY + 4096, CNT = TAGW + 16, APROG = CNT + 32, PPROG = APROG + 16, QPROG = PPROG + 16, KPROG = QPROG + 16, STMP = CNT + 128, ARR = STMP + 512, LDS_BYTES = ARR + 1024;
constexpr float QSCALE = 0.08838834764831845f * 1.4426950408889634f;
constexpr unsigned OOB = 0x40000000u;
constexpr size_t WS_TF = 8192, WS_OF = 16384, WS_XF = WS_OF + 131072, WS_O = 1u << 20, WS_Q = 40u << 20, WS_WT = 48u << 20, WS_QT = 56u << 20, WS_PX = 64u << 20, WS_ST = 128u << 20;
constexpr int OWN3 = 0, OWNP = 2 * XCH;
constexpr unsigned P_Q1 = 12, P_Q23 = 24;

DI unsigned rfl(unsigned x) { return __builtin_amdgcn_readfirstlane(x); }
DI unsigned cvtpk(float lo, float hi) { f32x2 v = {lo, hi}; bf16x2 b = __builtin_convertvector(v, bf16x2); return __builtin_bit_cast(unsigned, b); }
DI f32x4 mfma(u32x4 a, u32x4 b, f32x4 c) { return __builtin_amdgcn_mfma_f32_16x16x32_bf16(__builtin_bit_cast(bf16x8, a), __builtin_bit_cast(bf16x8, b), c, 0, 0, 0); }
DI void mfma_ip(f32x4& c, const u32x4& a, const u32x4& b) { asm volatile("v_mfma_f32_16x16x32_bf16 %0, %1, %2, %0" : "+v"(c) : "v"(a), "v"(b)); }
#define MFMA_DONE() asm volatile("s_nop 15")
DI f32x16 mfma32(u32x4 a, u32x4 b, f32x16 c) { return __builtin_amdgcn_mfma_f32_32x32x16_bf16(__builtin_bit_cast(bf16x8, a), __builtin_bit_cast(bf16x8, b), c, 0, 0, 0); }
DI u32x4 lds128(lptr p) { return *(const LAS u32x4*)p; }
DI int fresh_lane() { int l; asm volatile("v_mbcnt_lo_u32_b32 %0, -1, 0\n\tv_mbcnt_hi_u32_b32 %0, -1, %0" : "=v"(l)); return l; }
DI lptr lp(unsigned a) { return (lptr)(uintptr_t)a; }
DI u32x2 ldstr(lptr p) { return __builtin_bit_cast(u32x2, __builtin_amdgcn_ds_read_tr16_b64_v4i16((LAS s16x4*)p)); }
DI rsrc_t mkrsrc(const void* p, unsigned bytes) { return __builtin_amdgcn_make_buffer_rsrc((void*)p, 0, bytes, 0x00020000); }
DI float xmax(float m) {
  auto a = __builtin_amdgcn_permlane16_swap(__float_as_uint(m), __float_as_uint(m), false, false); m = fmaxf(__uint_as_float(a[0]), __uint_as_float(a[1]));
  auto b = __builtin_amdgcn_permlane32_swap(__float_as_uint(m), __float_as_uint(m), false, false); return fmaxf(__uint_as_float(b[0]), __uint_as_float(b[1]));
}
DI float xsum(float m) {
  auto a = __builtin_amdgcn_permlane16_swap(__float_as_uint(m), __float_as_uint(m), false, false); m = __uint_as_float(a[0]) + __uint_as_float(a[1]);
  auto b = __builtin_amdgcn_permlane32_swap(__float_as_uint(m), __float_as_uint(m), false, false); return __uint_as_float(b[0]) + __uint_as_float(b[1]);
}
#define DFPOL ""
#define TAUX 0
#define KVPOL "nt"
#define VPOL "nt"
template <int OFF, bool V = false> DI void dma_nt(rsrc_t r, unsigned voff, unsigned soff, unsigned ldsaddr) {
  if (V) asm volatile("s_mov_b32 m0, %3\n\ts_nop 0\n\tbuffer_load_dwordx4 %0, %1, %2 offen offset:%4 " VPOL " lds" :: "v"(voff), "s"(r), "s"(soff), "s"(ldsaddr), "i"(OFF) : "memory");
  else asm volatile("s_mov_b32 m0, %3\n\ts_nop 0\n\tbuffer_load_dwordx4 %0, %1, %2 offen offset:%4 " KVPOL " lds" :: "v"(voff), "s"(r), "s"(soff), "s"(ldsaddr), "i"(OFF) : "memory");
}
DI void dma_df(rsrc_t r, unsigned voff, unsigned soff, unsigned ldsaddr) {
  asm volatile("s_mov_b32 m0, %3\n\ts_nop 0\n\tbuffer_load_dwordx4 %0, %1, %2 offen " DFPOL "lds" :: "v"(voff), "s"(r), "s"(soff), "s"(ldsaddr) : "memory");
}
DI void dma_sc1(rsrc_t r, unsigned voff, unsigned soff, unsigned ldsaddr) {
  asm volatile("s_mov_b32 m0, %3\n\ts_nop 0\n\tbuffer_load_dwordx4 %0, %1, %2 offen sc1 lds" :: "v"(voff), "s"(r), "s"(soff), "s"(ldsaddr) : "memory");
}
DI void touch(rsrc_t r, unsigned voff, unsigned ldsaddr) {
  asm volatile("s_mov_b32 m0, %2\n\ts_nop 0\n\tbuffer_load_dword %0, %1, 0 offen lds" :: "v"(voff), "s"(r), "s"(ldsaddr) : "memory");
}
#define SB() __builtin_amdgcn_sched_barrier(0)
#define WAITV(N) do { SB(); asm volatile("s_waitcnt vmcnt(" #N ") lgkmcnt(0)" ::: "memory"); SB(); } while (0)
#define WAITVM(N) do { SB(); asm volatile("s_waitcnt vmcnt(" #N ")" ::: "memory"); SB(); } while (0)
#define WAITL() do { SB(); asm volatile("s_waitcnt lgkmcnt(0)" ::: "memory"); SB(); } while (0)
#define BAR_L() do { SB(); asm volatile("s_waitcnt lgkmcnt(0)\n\ts_barrier" ::: "memory"); SB(); } while (0)
#define ST() do {} while (0)
#define PST() do {} while (0)
#define PST2() do {} while (0)
#define PCAP 0
#define PC() do { if (PCAP) asm volatile("s_waitcnt vmcnt(%0)" :: "i"(PCAP - 1) : "memory"); } while (0)
#define PIN(x) asm volatile("" : "+v"(x))
#define NODMA 0
#define DPOS 2
#define DSTR 8
#define CAP 16
#define SAUX 16
#define PPRIO 0
#define ABL 0
#define TE0 2
#define XPRIO 0
#define PF 0
#define VP0 2
#define VST 4
#define KP0 18
#define KST 4
#define QPACE 10
#define OPACE 0
#define OP0 26
#define MAP 4
#define OSA 16
#define WSOFF 0
#define CODEPF 0
#define NH 2
#define SSL 0
#define DSL 0
#define LPRIO 3
#define ILV 0
#define TC 1
#define TN0 0, 9, 8, 8, 0, 4, 0, 5, 0, 0
#define LCAP 0
#define RPACE 0
#define WARM 0
#define TPRIO 2
#define OSL 3
#define HSL 0
#define BURST 0
#define QPRIO 0
#define RN0 0
#define RP0 76
#define ROT 1
#define QLD0 6
#define PFD 16
constexpr float PLIM = 1024.f;

template <int OFF> DI unsigned prog_read(unsigned pa) { return *(volatile LAS unsigned*)(lp(pa) + OFF); }
template <int OFF, int SLEEP> DI void prog_wait(unsigned pa, unsigned v, unsigned t) {
  unsigned n;
  asm volatile("s_mov_b32 %1, 0x1000000\n"
               "1:\n\tv_cmp_gt_u32 vcc, %3, %0\n\ts_cbranch_vccz 2f\n\ts_sleep %5\n\tds_read_b32 %0, %2 offset:%4\n\ts_sub_u32 %1, %1, 1\n\ts_waitcnt lgkmcnt(0)\n\ts_cbranch_scc0 1b\n2:"
               : "+v"(v), "=&s"(n) : "v"(pa), "s"(t), "i"(OFF), "i"(SLEEP) : "memory", "vcc", "scc");
}
template <int OFF, int SLEEP> DI void prog_wait(unsigned pa, unsigned t) { prog_wait<OFF, SLEEP>(pa, prog_read<OFF>(pa), t); }
DI void prog_post(lptr p, unsigned v) { asm volatile("" ::: "memory"); *(volatile LAS unsigned*)p = v; }

DI void publish(gu64* flag, lptr cnt, unsigned long long tag, int lane) {
  asm volatile("s_waitcnt vmcnt(0)" ::: "memory");
  if (lane == 0) {
    const unsigned old = __hip_atomic_fetch_add((LAS unsigned*)cnt, 1u, __ATOMIC_RELAXED, __HIP_MEMORY_SCOPE_WORKGROUP);
    asm volatile("" : "+s"(tag));
    if (old == 3u) __hip_atomic_store(flag, tag, RLX_AGENT);
  }
}
DI void wait_flag(gu64* f, unsigned long long v, unsigned long long tag) {
#pragma nounroll
  for (unsigned spins = 0; spins < (1u << 15); ++spins) {
    if (__all(v == tag)) break;
    __builtin_amdgcn_s_sleep(2);
    v = __hip_atomic_load(f, RLX_AGENT);
  }
  __builtin_amdgcn_s_waitcnt(0x0F70);
  __builtin_amdgcn_fence(__ATOMIC_ACQUIRE, "wavefront");
  asm volatile("" ::: "memory");
}

struct Ctx {
  char* ws; int b, g;
  DI Ctx fresh() const { Ctx c = *this; asm volatile("" : "+s"(c.b), "+s"(c.g)); return c; }
  DI gu64* epoch() const { return (gu64*)ws; }
  DI gu64* tflags() const { return (gu64*)(ws + WS_TF + g * 256); }
  DI gu64* xflag(int dst) const { return (gu64*)(ws + WS_XF + (b * 8 + dst) * 128); }
  DI rsrc_t rpx(int dst) const { return mkrsrc(ws + WS_PX + (size_t)(b * 8 + dst) * 143360, (ABL & 64) ? 0 : 143360); }
  DI rsrc_t rwt() const { return mkrsrc(ws + WS_WT + (size_t)g * 655360, 655360); }
  DI rsrc_t rqt() const { return mkrsrc(ws + WS_QT + (size_t)g * 491520, 491520); }
};
template <int NQ> struct AttnState {
  u32x4 q[NQ][4], fr[8], p[2][NQ];
  f32x4 o[8][NQ], negm[NQ], sc[2][4][NQ];
  float l[NQ], rs[NQ], inv[NQ];
  unsigned ka[4], va[8], kvo, vvo, pa, pb, pg, oad, last;
};

DI int qreg(int r) { return r == 3 ? RC : ((r & 1) ? RB : RA); }
template <int NQ> DI void sm_pair(AttnState<NQ>& a, const f32x4 (&x)[4][NQ], u32x4 (&p)[NQ], const int hf, const int n) {
  const int t = n >> 2, d = n & 3;
  if (t >= NQ) return;
  const float e0 = __builtin_amdgcn_exp2f(x[2 * hf + (d >> 1)][t][2 * (d & 1)]), e1 = __builtin_amdgcn_exp2f(x[2 * hf + (d >> 1)][t][2 * (d & 1) + 1]);
  a.rs[t] = d ? (a.rs[t] + e0) + e1 : e0 + e1;
  p[t][d] = cvtpk(e0, e1);
  PIN(p[t][d]); PIN(a.rs[t]);
}
template <int NQ, int HF, bool ADJ>
DI void fixup(AttnState<NQ>& a, f32x4 (&x)[4][NQ], f32x4 (&y)[4][NQ]) {
#pragma unroll
  for (int t = 0; t < NQ; ++t) {
    float mx = x[3][t][3];
#pragma unroll
    for (int i = 8 * HF; i < 15; ++i) mx = fmaxf(mx, x[i >> 2][t][i & 3]);
    const float dl = __builtin_ceilf(fmaxf(xmax(mx), 0.f)), sc = __builtin_amdgcn_exp2f(-dl);
    a.l[t] *= sc; a.negm[t] -= dl;
#pragma unroll
    for (int dt = 0; dt < 8; ++dt) a.o[dt][t] *= sc;
    if (HF) {
#pragma unroll
      for (int d = 0; d < 4; ++d) { const unsigned v = a.p[0][t][d]; a.p[0][t][d] = cvtpk(__uint_as_float(v << 16) * sc, __uint_as_float(v & 0xffff0000u) * sc); }
    }
#pragma unroll
    for (int kt = 2 * HF; kt < 4; ++kt) x[kt][t] -= dl;
    float r = 0.f;
#pragma unroll
    for (int d = 0; d < 4; ++d) { const float e0 = __builtin_amdgcn_exp2f(x[2 * HF + (d >> 1)][t][2 * (d & 1)]), e1 = __builtin_amdgcn_exp2f(x[2 * HF + (d >> 1)][t][2 * (d & 1) + 1]);
      r += e0 + e1; a.p[HF][t][d] = cvtpk(e0, e1); }
    a.rs[t] = r;
    if (ADJ) {
#pragma unroll
      for (int kt = 0; kt < 4; ++kt) y[kt][t] -= dl;
    }
  }
}
template <int NQ, int HF, bool CHECK, bool ADJ>
DI void sm_close(AttnState<NQ>& a, f32x4 (&x)[4][NQ], f32x4 (&y)[4][NQ]) {
  if (CHECK) {
    if (NQ == 1) PIN(a.p[HF][0]);
    float m = NQ == 2 ? a.rs[0] + a.rs[NQ - 1] : a.rs[0]; PIN(m);
    if (__builtin_expect(__any(!(m <= PLIM)), 0)) fixup<NQ, HF, ADJ>(a, x, y);
  }
#pragma unroll
  for (int t = 0; t < NQ; ++t) { PIN(a.rs[t]); a.l[t] += a.rs[t]; PIN(a.l[t]); }
}
template <int NQ> DI u32x4 o_frag(const AttnState<NQ>& a, const int m, const int t) {
  const f32x4 x = a.o[2 * m][t] * a.inv[t], y = a.o[2 * m + 1][t] * a.inv[t];
  return u32x4{cvtpk(x[0], x[1]), cvtpk(x[2], x[3]), cvtpk(y[0], y[1]), cvtpk(y[2], y[3])};
}
template <bool V, int NQ> DI void frag_read(AttnState<NQ>& a, const int m, const int par) {
  if (!V) a.fr[m & 7] = lds128(lp(a.ka[m & 3]) + par * SLOT + (m >> 2) * 4096);
  else { lptr p = lp(a.va[m & 7]) + par * SLOT + (m >> 3) * 8192; const u32x2 v0 = ldstr(p), v1 = ldstr(p + 4096); a.fr[m & 7] = u32x4{v0[0], v0[1], v1[0], v1[1]}; }
}
template <bool V, int NQ> DI void tile_issue(const AttnState<NQ>& a, rsrc_t r, const unsigned gsrc, const unsigned dst, const unsigned st = 4096) {
  if (NODMA) return;
#pragma unroll
  for (int i = 0; i < 4; ++i) {
    if (LCAP) asm volatile("s_waitcnt vmcnt(%0)" :: "i"(LCAP - 4) : "memory");
    dma_nt<0, V>(r, V ? a.vvo : a.kvo, gsrc + i * 4096, dst + i * st);          dma_nt<1024, V>(r, V ? a.vvo ^ 128 : a.kvo ^ 64, gsrc + i * 4096, dst + i * st);
    dma_nt<2048, V>(r, V ? a.vvo : a.kvo ^ 128, gsrc + i * 4096, dst + i * st); dma_nt<3072, V>(r, V ? a.vvo ^ 128 : a.kvo ^ 192, gsrc + i * 4096, dst + i * st);
  }
}
template <bool CV, int NQ> DI void phase_io(AttnState<NQ>& a, const int i, const int cp, const int np, const unsigned val, const int w, lptr lds) {
  if (i <= 10) frag_read<CV>(a, i + 5, cp);
  if (NQ == 2 && i == 10) {
    SB();
    prog_post(lds + APROG + w * 4, val);
    a.pg = prog_read<0>(a.pb);
  }
  if (NQ == 2 && i == 12) prog_wait<0, HSL>(a.pb, a.pg, val);
  if (i == 13 || i == 14) { frag_read<!CV>(a, 2 * (i - 13), np); frag_read<!CV>(a, 2 * (i - 13) + 1, np); }
  if (i == 15) frag_read<!CV>(a, 4, np);
}
template <bool V, int NQ> DI void loader_turn(AttnState<NQ>& a, const unsigned val, lptr lds, rsrc_t r, const unsigned gsrc, const unsigned dst) {
  if (NQ == 1) {
    WAITVM(16);
    prog_post(lds + APROG + 12, val + 1);
    prog_wait<0, 1>(a.pb, val);
    if (RPACE) {
      asm volatile("1:\n\ts_memrealtime vcc\n\ts_waitcnt lgkmcnt(0)\n\ts_sub_u32 vcc_hi, vcc_lo, %0\n\ts_cmp_lt_u32 vcc_hi, %1\n\ts_cbranch_scc1 1b\n\ts_mov_b32 %0, vcc_lo"
                   : "+s"(a.last) : "s"(val < RN0 ? RP0 : RPACE) : "memory", "scc", "vcc");
    }
    tile_issue<V>(a, r, gsrc, gsrc < 32 * SLOT ? dst : (unsigned)(uintptr_t)lds + DUMMY, gsrc < 32 * SLOT ? 4096u : 0u);
    SB();
  }
}
template <int KIND, int PAR, int NQ>
DI void attn_step(AttnState<NQ>& a, const int s, const int w, const int lane, lptr lds, const unsigned l0, rsrc_t rk, rsrc_t rv) {
  const f32x4 zero = {0.f, 0.f, 0.f, 0.f};
  f32x4 (&c)[4][NQ] = a.sc[PAR]; f32x4 (&d)[4][NQ] = a.sc[PAR ^ 1];
  SB();
  if (KIND == 1) {
    prog_wait<16, 1>(a.pa, s == 0 ? 0u : (s == 8 ? P_Q1 : P_Q23));
    const unsigned qa = l0 + qreg(s >> 3) + 2 * w * 4096 + lane * 16;
#pragma unroll
    for (int i = 0; i < 4; ++i)
#pragma unroll
      for (int t = 0; t < NQ; ++t) a.q[t][i] = lds128(lp(qa) + t * 4096 + i * 1024);
    a.oad = qa;
    SB();
  }
#pragma unroll
  for (int i = 0; i < 16; ++i) {
    constexpr int PA[16] = {-1, 0, 4, -1, 1, 5, -1, 2, 6, -1, -1, 3, 7, -1, -1, -1};
#pragma unroll
    for (int t = 0; t < NQ; ++t) {
      c[i >> 2][t] = mfma(a.fr[i & 7], a.q[t][i & 3], (i & 3) ? c[i >> 2][t] : (KIND == 1 ? zero : a.negm[t]));
      if (t == 0) { if (PA[i] >= 0) sm_pair(a, d, a.p[1], 1, PA[i]); SB(); }
    }
    if (i == 12) PIN(c[3][NQ - 1]);
    phase_io<false>(a, i, PAR, PAR ^ 1, 2 * s + 1, w, lds);
    if (KIND == 2 && i >= 8 && ((i - 8) >> 2) < NQ) *(LAS u32x4*)(lp(a.oad) + ((i - 8) >> 2) * 4096 + (i & 3) * 1024) = o_frag(a, i & 3, ((i - 8) >> 2) % NQ);
    SB();
  }
  if (KIND == 2) prog_post(lds + QPROG + w * 4, s);
  if (BURST) { if (NQ == 1) { WAITVM(16); prog_post(lds + APROG + 12, 2 * s + 2); } } else
  loader_turn<false>(a, 2 * s + 1, lds, rk, (s + 2) * SLOT, l0 + PAR * SLOT);
  sm_close<NQ, 1, KIND != 2, KIND == 0>(a, d, c);
  if (KIND == 1) {
#pragma unroll
    for (int t = 0; t < NQ; ++t) { a.inv[t] = __builtin_amdgcn_rcpf(xsum(a.l[t])); a.l[t] = 0.f; }
  }
  SB();
  u32x4 pn[NQ];
#pragma unroll
  for (int i = 0; i < 16; ++i) {
    constexpr int PB[16] = {-1, -1, -1, 0, -1, 4, -1, 1, -1, 5, -1, 2, 6, -1, 3, 7};
#pragma unroll
    for (int t = 0; t < NQ; ++t) {
      a.o[i & 7][t] = mfma(a.fr[i & 7], a.p[i >> 3][t], (KIND == 2 && i < 8) ? zero : a.o[i & 7][t]);
      if (t == 0) { if (PB[i] >= 0) sm_pair(a, c, pn, 0, PB[i]); SB(); }
    }
    if (i == 12) PIN(a.o[4][NQ - 1]);
    phase_io<true>(a, i, PAR ^ 1, PAR ^ 1, 2 * s + 2, w, lds);
    if (KIND == 1 && (i == 1 || i == 2) && i - 1 < NQ) {
      const int t = (i - 1) % NQ;
      float m0 = c[3][t][3];
#pragma unroll
      for (int e = 0; e < 15; ++e) m0 = fmaxf(m0, c[e >> 2][t][e & 3]);
      m0 = xmax(m0);
#pragma unroll
      for (int kt = 0; kt < 4; ++kt) { c[kt][t] -= m0; PIN(c[kt][t]); }
      a.negm[t] = f32x4{-m0, -m0, -m0, -m0}; PIN(a.negm[t]);
    }
    SB();
  }
#pragma unroll
  for (int t = 0; t < NQ; ++t) a.p[0][t] = pn[t];
  if (BURST) { if (NQ == 1) {
    WAITVM(0); prog_post(lds + APROG + 12, 2 * s + 3); prog_wait<0, 1>(a.pb, 2 * s + 2);
    tile_issue<false>(a, rk, (s + 2) * SLOT, s < 30 ? l0 + PAR * SLOT : l0 + DUMMY, s < 30 ? 4096u : 0u);
    tile_issue<true>(a, rv, (s + 1) * SLOT, s < 31 ? l0 + VOFF + (PAR ^ 1) * SLOT : l0 + DUMMY, s < 31 ? 4096u : 0u); SB(); } } else
  loader_turn<true>(a, 2 * s + 2, lds, rv, (s + 1) * SLOT, l0 + VOFF + (PAR ^ 1) * SLOT);
  sm_close<NQ, 0, KIND != 1, false>(a, c, c);
}

template <int NQ>
DI void attn_main(const int w, lptr lds, const unsigned l0, rsrc_t rk, rsrc_t rv, const Ctx& c0, int& nst) {
  const int lane = fresh_lane(), lg = lane >> 4, lr = lane & 15;
  AttnState<NQ> a;
  a.kvo = lg * 256 + ((lr ^ lg) << 4);
  a.vvo = lg * 256 + ((lr ^ (lg << 1)) << 4);
  if (NQ == 1) { tile_issue<false>(a, rk, 0, l0); }
  BAR_L();
  if (NQ == 1) { tile_issue<false>(a, rk, SLOT, l0 + SLOT); }
  BAR_L();
  if (NQ == 1) { tile_issue<true>(a, rv, 0, l0 + VOFF); prog_post(lds + APROG + 12, 1); __builtin_amdgcn_s_setprio(LPRIO); }
#pragma unroll
  for (int i = 0; i < 4; ++i) { a.ka[i] = l0 + lr * 256 + (((4 * i + lg) ^ lr) << 4); PIN(a.ka[i]); }
#pragma unroll
  for (int i = 0; i < 8; ++i) { a.va[i] = l0 + VOFF + (lane >> 2) * 256 + ((i ^ ((lane >> 2) & 7)) << 5) + (lane & 3) * 8; PIN(a.va[i]); }
  a.pg = 0; a.pa = l0 + APROG + (lane & 3) * 4; a.pb = l0 + APROG + (NQ == 2 ? 12 : ((lane & 3) == 3 ? 0 : (lane & 3) * 4)); a.oad = 0; a.last = RPACE ? (unsigned)__builtin_amdgcn_s_memrealtime() - 1000u : 0u;
#pragma unroll
  for (int t = 0; t < NQ; ++t) {
    a.l[t] = 0.f; a.inv[t] = 0.f; a.rs[t] = 0.f; a.p[0][t] = a.p[1][t] = u32x4{0u, 0u, 0u, 0u}; a.negm[t] = f32x4{0.f, 0.f, 0.f, 0.f};
#pragma unroll
    for (int i = 0; i < 8; ++i) { a.o[i][t] = f32x4{0.f, 0.f, 0.f, 0.f}; a.sc[1][i & 3][t] = f32x4{0.f, 0.f, 0.f, 0.f}; }
  }
  WAITVM(32);
  BAR_L(); ST();
#pragma unroll
  for (int m = 0; m < 5; ++m) frag_read<false>(a, m, 0);
#pragma nounroll
  for (int r = 0; r < 4; ++r) {
    attn_step<1, 0>(a, 8 * r, w, lane, lds, l0, rk, rv); ST();
    attn_step<2, 1>(a, 8 * r + 1, w, lane, lds, l0, rk, rv); ST();
#pragma nounroll
    for (int t = 2; t < 8; t += 2) {
      attn_step<0, 0>(a, 8 * r + t, w, lane, lds, l0, rk, rv); ST();
      attn_step<0, 1>(a, 8 * r + t + 1, w, lane, lds, l0, rk, rv); ST();
    }
  }
  {
#pragma unroll
    for (int i = 0; i < 8; ++i) sm_pair(a, a.sc[1], a.p[1], 1, i);
    if (NQ == 2) prog_wait<0, 0>(a.pb, 65);
    if (BURST) prog_wait<0, 0>(a.pa, 63);
#pragma unroll
    for (int m = 0; m < 8; ++m) frag_read<true>(a, m, 1);
    sm_close<NQ, 1, true, false>(a, a.sc[1], a.sc[1]);
    SB();
#pragma unroll
    for (int i = 0; i < 16; ++i) {
#pragma unroll
      for (int t = 0; t < NQ; ++t) a.o[i & 7][t] = mfma(a.fr[i & 7], a.p[i >> 3][t], a.o[i & 7][t]);
      if (i < 8) frag_read<true>(a, i + 8, 1);
      SB();
    }
#pragma unroll
    for (int t = 0; t < NQ; ++t) a.inv[t] = __builtin_amdgcn_rcpf(xsum(a.l[t]));
  }
#pragma unroll
  for (int t = 0; t < NQ; ++t)
#pragma unroll
    for (int m = 0; m < 4; ++m) *(LAS u32x4*)(lds + OWN3 + (2 * w + t) * 4096 + m * 1024 + lane * 16) = o_frag(a, m, t);
  prog_post(lds + QPROG + w * 4, 33);
  __builtin_amdgcn_s_setprio(0);
  ST();
}

DI void attn_wave(const int w, const int lane, const int rot, lptr lds, const unsigned l0, rsrc_t rk, rsrc_t rv, rsrc_t rx, rsrc_t rwq, const char* wo, const Ctx& c0, int& nst) {
  const int lg = lane >> 4, lr = lane & 15;
  ST();
  {
    const unsigned wvo = lg * 8192 + ((lr ^ (lg << 2)) << 4) + c0.g * 1024;
    const unsigned xvo = (lane >> 3) * 1280 + (((lane & 7) ^ ((4 * (w & 1) + lg) & 7)) << 4);
    auto issue = [&](int c) {
      const int cr = c + rot >= 10 ? c + rot - 10 : c + rot;
      const unsigned wd = l0 + VOFF + (c & 3) * PBUF + w * 1024, xd = wd + SLOT, xs = w * 10240 + cr * 128, ws_ = (cr * 64 + 4 * w) * 8192;
#pragma unroll
      for (int i = 0; i < 3; ++i) dma_df(rx, xvo, xs + i * 40960, xd + i * 4096);
      dma_df(rx, xvo, xs + 3 * 40960, w < 2 ? xd + 3 * 4096 : l0 + DUMMY);
#pragma unroll
      for (int i = 0; i < 4; ++i) dma_df(rwq, wvo, ws_ + i * 131072, wd + i * 4096);
    };
    issue(0); issue(1); issue(2);
    if (w == 0) { *(LAS u32x4*)(lds + CNT + (lane & 7) * 16) = u32x4{0u, 0u, 0u, 0u}; }
    const int b = c0.b, g = c0.g; const rsrc_t rwo = mkrsrc(wo, 4096 * 1280);
    u32x2 rw[8], rq2[8]; unsigned short r5[8]; unsigned long long e1;
    const unsigned rb = 4 * lg * 1280 + (b >> 4) * 640, wv0 = rb + w * 128 + lr * 8, wv1 = rb + 512 + w * 32 + lr * 2;
    const unsigned s2 = ((4 * g + ((b >> 2) & 3)) * 128 + (b & 3) * 32) * 1280;
    const int ti = (b - 20) * 4 + w, tk = b < 20 ? b : ti >> 1, tu = b < 20 ? w + 2 : ti & 1;
    const unsigned qv = lg * 8 * 8192 + lr * 8 + g * 1024 + 256, sq = tk * 32 * 8192 + tu * 128;
    constexpr int TN[13] = {0, 0, 0, TN0};
#pragma unroll
    for (int c = 0; c < 9; ++c) {
      SB(); asm volatile("s_waitcnt vmcnt(%0) lgkmcnt(0)\n\ts_barrier" :: "i"((c < 8 ? 16 : 8) + TN[c] + TN[c + 1] + TN[c + 2]) : "memory"); SB();
      if (c == TC + 4) { _Pragma("unroll") for (int i = 0; i < 8; ++i) PIN(rq2[i]); }
      if (c == TC + 6) { _Pragma("unroll") for (int i = 0; i < 8; ++i) { PIN(rw[i]); PIN(r5[i]); } }
      SB();
      if (c < 7) issue(c + 3);
      SB();
      if (c == TC) {
        e1 = __hip_atomic_load(c0.epoch(), RLX_AGENT) + 1;
#pragma unroll
        for (int jj = 0; jj < 8; ++jj) rq2[jj] = __builtin_amdgcn_raw_buffer_load_b64(rwq, qv, sq + jj * 8192, 0);
      }
      if (c == TC + 1) {
#pragma unroll
        for (int jj = 0; jj < 8; ++jj) rw[jj] = __builtin_amdgcn_raw_buffer_load_b64(rwo, wv0 + (jj & 3) * 1280, s2 + (jj >> 2) * 16 * 1280, 0);
      }
      if (c == TC + 2) {
#pragma unroll
        for (int jj = 0; jj < 8; ++jj) r5[jj] = __builtin_amdgcn_raw_buffer_load_b16(rwo, wv1 + (jj & 3) * 1280, s2 + (jj >> 2) * 16 * 1280, 0);
      }
      if (c == TC + 4) {
#pragma unroll
        for (int e = 0; e < 4; ++e) { u32x4 f;
#pragma unroll
          for (int pp = 0; pp < 4; ++pp) f[pp] = __builtin_amdgcn_perm(rq2[2 * pp + 1][e >> 1], rq2[2 * pp][e >> 1], (e & 1) ? 0x07060302u : 0x05040100u);
          __builtin_amdgcn_raw_buffer_store_b128(f, c0.rqt(), (tk * 6 + tu) * 4096 + e * 1024 + lane * 16, 0, 16); }
      }
      if (c == TC + 6) {
#pragma unroll
        for (int e = 0; e < 5; ++e) { u32x4 f;
#pragma unroll
          for (int pp = 0; pp < 4; ++pp) f[pp] = e < 4 ? __builtin_amdgcn_perm(rw[2 * pp + 1][(e >> 1) & 1], rw[2 * pp][(e >> 1) & 1], (e & 1) ? 0x07060302u : 0x05040100u)
                                                       : __builtin_amdgcn_perm((unsigned)r5[2 * pp + 1], (unsigned)r5[2 * pp], 0x05040100u);
          __builtin_amdgcn_raw_buffer_store_b128(f, c0.rwt(), (b * 4 + w) * 5120 + e * 1024 + lane * 16, 0, 16); }
      }
    }
    {
      const unsigned long long tag = ((unsigned long long)rfl((unsigned)(e1 >> 32)) << 32) | rfl((unsigned)e1);
      *(LAS unsigned long long*)(lds + TAGW) = tag;
      publish(c0.tflags() + b, lds + CNT + 16, tag, lane);
    }
  }
  if (w < 3) attn_main<2>(w, lds, l0, rk, rv, c0, nst); else attn_main<1>(w, lds, l0, rk, rv, c0, nst);
}

constexpr int LAG = 3;
DI void parrive(unsigned& pc, const int j, lptr lds) { WAITL(); ++pc; prog_post(lds + PPROG + j * 4, pc); }
template <int NE> constexpr int efrag(int e) { return NE == 5 ? e : (NE == 3 ? (e < 2 ? e : 4) : e + 2); }
template <int NE> DI u32x4 wload(rsrc_t rwt, const unsigned lv, const unsigned so, const int e) {
  return __builtin_amdgcn_raw_buffer_load_b128(rwt, lv + (efrag<NE>(e) & 3) * 1024, so + (efrag<NE>(e) >> 2) * 4096, 16);
}
DI void send_flag(const Ctx& c, const int dst, const int part, const unsigned lv, lptr lds) {
  const unsigned long long tag = *(const LAS unsigned long long*)(lds + TAGW);
  asm volatile("s_waitcnt vmcnt(0)" ::: "memory");
  if (lv == 0) __hip_atomic_store(c.xflag(dst) + c.g + part, tag, RLX_AGENT);
}
template <int NE>
DI void oproj_pass(const int j, const bool pstv, const Ctx& c0, lptr lds, const unsigned l0, int& nst) {
  const int lane = fresh_lane();
  const Ctx c = c0.fresh();
  const int mrot = ROT ? c.b : 0, side = (c.g ^ (NE != 5)) & 1;
  const rsrc_t rwt = c.rwt();
  const unsigned pa = l0 + APROG + (lane & 3) * 4, lv = lane * 16, tb = (side * 64 + j) * 5120;
  f32x4 acc[NE][7]; u32x4 af[4][NE], bf[4];
#pragma unroll
  for (int k = 0; k < 3; ++k)
#pragma unroll
    for (int e = 0; e < NE; ++e) { af[k][e] = wload<NE>(rwt, lv, tb + ((k + mrot) & 3) * 20480, e); SB(); }
#pragma unroll
  for (int e = 0; e < NE; ++e)
#pragma unroll
    for (int t = 0; t < 7; ++t) { acc[e][t] = f32x4{0.f, 0.f, 0.f, 0.f}; PIN(acc[e][t]); }
  asm volatile("s_nop 1"); SB();
#pragma nounroll
  for (int r = 0; r < 3; ++r) {
    const unsigned reg = qreg(r + 1);
    if (NE != 2) prog_wait<32, 2>(pa, 8 * r + 9);
    if (NE == 2 && r == 2) send_flag(c0.fresh(), 2 * j + (side ^ 1), 0, lv, lds);
    PST();
    auto baddr = [&](int gi) { return lp(l0 + reg + ((gi / 7 + mrot) & 3) * 1024 + lv) + (gi % 7) * 4096; };
#pragma unroll
    for (int gi = 0; gi < LAG; ++gi) bf[gi] = lds128(baddr(gi));
    SB();
#pragma unroll
    for (int k = 0; k < 4; ++k) {
      int r2 = r; asm volatile("" : "+s"(r2));
      const unsigned so = tb + ((k ? r2 + 1 : r2) * 4 + ((k + 3 + mrot) & 3)) * 20480;
#pragma unroll
      for (int t = 0; t < 7; ++t) {
        const int gi = 7 * k + t;
#pragma unroll
        for (int e = 0; e < NE; ++e) {
          mfma_ip(acc[e][t], af[k][e], bf[gi & 3]);
          if (e == 0 && gi + LAG < 28) bf[(gi + LAG) & 3] = lds128(baddr(gi + LAG));
          if (e == NE / 2 && t >= 1 && t <= NE) af[(k + 3) & 3][t - 1] = wload<NE>(rwt, lv, so, t - 1);
          if (NE == 5 && OSL && e == 4) __builtin_amdgcn_s_sleep(OSL);
          SB();
        }
      }
    }
    PST();
  }
#pragma unroll
  for (int e = 0; e < NE; ++e) af[3][e] = wload<NE>(rwt, lv, tb + (12 + ((3 + mrot) & 3)) * 20480, e);
  if (NE == 5 && WARM) {
    prog_wait<0, 8>(pa, WARM);
#pragma unroll
    for (int i = 0; i < 10; ++i) dma_sc1(rwt, lv, (side ^ 1) * 327680 + ((c.b & 7) * 40 + j * 10 + i) * 1024, l0 + DUMMY);
  }
  if (NE != 2) prog_wait<32, 1>(pa, 33);
  if (NE == 5) __builtin_amdgcn_s_setprio(2);
  PST();
  const Ctx c2 = c0.fresh(); const int dst = 2 * j + side;
  const bool keep = NE == 5 && dst == c2.g;
  const rsrc_t rpx = c2.rpx(dst); const unsigned po = c2.g * 17920;
  unsigned ba[4];
#pragma unroll
  for (int k = 0; k < 4; ++k) ba[k] = l0 + OWN3 + ((k + mrot) & 3) * 1024 + lv;
  auto out = [&](int t, int part) {
    if (keep) { *(LAS f32x4*)(lp(l0 + OWNP + lv) + (t * 5 + part) * 1024) = acc[part % NE][t]; if (part == 3) *(LAS f32x4*)(lp(l0 + OWNP + lv) + (t * 5 + 4) * 1024) = acc[NE - 1][t]; }
    else if (part < NE / 2) {
      const f32x4 x = acc[2 * part][t], y = acc[2 * part + 1][t];
      __builtin_amdgcn_raw_buffer_store_b128(u32x4{cvtpk(x[0], y[0]), cvtpk(x[1], y[1]), cvtpk(x[2], y[2]), cvtpk(x[3], y[3])}, rpx, po + lv + t * 2560 + efrag<NE>(2 * part) * 512, 0, 16);
    } else if (part == 2 && (NE & 1)) { const f32x4 x = acc[NE - 1][t];
      __builtin_amdgcn_raw_buffer_store_b64(u32x2{cvtpk(x[0], x[1]), cvtpk(x[2], x[3])}, rpx, po + (lv >> 1) + t * 2560 + 2048, 0, 16); }
  };
#pragma unroll
  for (int gi = 0; gi < LAG; ++gi) bf[gi] = lds128(lp(ba[gi & 3]) + (gi >> 2) * 4096);
  SB();
#pragma unroll
  for (int gi = 0; gi < 28; ++gi) {
    const int t = gi >> 2, k = gi & 3;
#pragma unroll
    for (int e = 0; e < NE; ++e) {
      mfma_ip(acc[e][t], af[k][e], bf[gi & 3]);
      if (e == 0 && gi + LAG < 28) bf[(gi + LAG) & 3] = lds128(lp(ba[(gi + LAG) & 3]) + ((gi + LAG) >> 2) * 4096);
      if (e == NE - 1 && k == 0 && t > 0) {
        asm volatile("s_nop %0" :: "i"(8 - NE));
#pragma unroll
        for (int e2 = 0; e2 < NE; ++e2) PIN(acc[e2][t - 1]);
      }
      if (e == NE - 1 && t > 0) out(t - 1, k);
      SB();
    }
  }
  MFMA_DONE();
#pragma unroll
  for (int e2 = 0; e2 < NE; ++e2) PIN(acc[e2][6]);
  SB();
#pragma unroll
  for (int k = 0; k < 4; ++k) out(6, k);
  if (NE == 5) __builtin_amdgcn_s_setprio(TPRIO);
  if (keep) prog_post(lds + KPROG, 1);
  if (NE != 5) { send_flag(c2, dst, NE == 2 ? 8 : 0, lv, lds); __builtin_amdgcn_s_waitcnt(0x0F70); }
  PST();
}

DI void final_part(const int j, const Ctx& c0, lptr lds, const unsigned l0, char* out, int& nst) {
  const Ctx c = c0.fresh(); const int b = c.b, g = c.g; const rsrc_t rpx = c.rpx(g);
  const int lane = fresh_lane(), p = g >> 1, lg = lane >> 4, lr = lane & 15;
  const bool pstv = j == 0; (void)pstv;
  const unsigned long long tg = *(const LAS unsigned long long*)(lds + TAGW), tag = ((unsigned long long)rfl((unsigned)(tg >> 32)) << 32) | rfl((unsigned)tg);
  auto lo = [](unsigned v) { return __uint_as_float(v << 16); };
  auto hi = [](unsigned v) { return __uint_as_float(v & 0xffff0000u); };
  f32x4 a[2][5];
#pragma unroll
  for (int st = 0; st < 3; ++st) {
    const int ns = st ? 4 : 3;
    {
      const int k = lane & 3;
      gu64* f = c.xflag(g) + (st ? 2 * k + 1 - (g & 1) + (st == 1 ? 8 : 0) : 2 * (k == p ? (p + 1) & 3 : k) + (g & 1));
      wait_flag(f, __hip_atomic_load(f, RLX_AGENT), tag);
    }
    u32x4 in[2][4][2]; u32x2 in2[2][4];
#pragma unroll
    for (int i = 0; i < 2; ++i)
#pragma unroll
      for (int k = 0; k < ns; ++k) {
        const int src = st ? 2 * k + 1 - (g & 1) : 2 * (k + (k >= p)) + (g & 1);
        const unsigned o = (i == 1 && j == 3) ? OOB : src * 17920 + (j + 4 * i) * 2560;
        if (st != 1) in[i][k][0] = __builtin_amdgcn_raw_buffer_load_b128(rpx, lane * 16 + o, 0, 16);
        if (st != 2) in[i][k][1] = __builtin_amdgcn_raw_buffer_load_b128(rpx, lane * 16 + o + 1024, 0, 16);
        if (st != 1) in2[i][k] = __builtin_amdgcn_raw_buffer_load_b64(rpx, lane * 8 + o + 2048, 0, 16);
      }
    if (st == 0) {
      prog_wait<0, 1>(l0 + KPROG, 1);
#pragma unroll
      for (int i = 0; i < 2; ++i)
#pragma unroll
        for (int e = 0; e < 5; ++e) a[i][e] = *(LAS f32x4*)(lds + OWNP + (((i == 1 && j == 3) ? 0 : j + 4 * i) * 5 + e) * 1024 + lane * 16);
    }
#pragma unroll
    for (int i = 0; i < 2; ++i)
#pragma unroll
      for (int k = 0; k < ns; ++k) {
#pragma unroll
        for (int d = (st == 1 ? 4 : 0); d < (st == 2 ? 4 : 8); ++d) { const int reg = d & 3, e = 2 * (d >> 2); a[i][e][reg] += lo(in[i][k][d >> 2][d & 3]); a[i][e + 1][reg] += hi(in[i][k][d >> 2][d & 3]); }
        if (st != 1) {
#pragma unroll
          for (int d = 0; d < 2; ++d) { a[i][4][2 * d] += lo(in2[i][k][d]); a[i][4][2 * d + 1] += hi(in2[i][k][d]); }
        }
      }
    PST();
  }
  if (j == 0 && g == 0 && b == 0 && lane == 0) __hip_atomic_store(c.epoch(), tag, RLX_AGENT);
  const rsrc_t rout = mkrsrc(out + (size_t)b * 143360, 143360);
#pragma unroll
  for (int i = 0; i < 2; ++i) {
    const unsigned ob = (i == 1 && j == 3) ? OOB : ((j + 4 * i) * 16 + lr) * 1280 + (g & 1) * 640;
#pragma unroll
    for (int hf = 0; hf < 2; ++hf)
      __builtin_amdgcn_raw_buffer_store_b128(u32x4{cvtpk(a[i][0][2 * hf], a[i][1][2 * hf]), cvtpk(a[i][2][2 * hf], a[i][3][2 * hf]), cvtpk(a[i][0][2 * hf + 1], a[i][1][2 * hf + 1]),
                                                   cvtpk(a[i][2][2 * hf + 1], a[i][3][2 * hf + 1])}, rout, ob + p * 128 + lg * 32 + hf * 16, 0, 0);
    __builtin_amdgcn_raw_buffer_store_b64(u32x2{cvtpk(a[i][4][0], a[i][4][1]), cvtpk(a[i][4][2], a[i][4][3])}, rout, ob + 512 + p * 32 + lg * 8, 0, 0);
  }
}

template <int NE> struct QpState { f32x4 acc[NE][7]; u32x4 xf[7]; lptr xa0, xa1; };
template <int NE> DI void qp_init(QpState<NE>& q, lptr xb, int lane) {
  const int lg = lane >> 4, lr = lane & 15;
  q.xa0 = xb + lr * 128 + ((lg ^ (lr >> 1)) << 4); q.xa1 = xb + lr * 128 + (((4 + lg) ^ (lr >> 1)) << 4);
#pragma unroll
  for (int t = 0; t < 7; ++t) { q.xf[t] = u32x4{0u, 0u, 0u, 0u}; PIN(q.xf[t]);
#pragma unroll
    for (int e = 0; e < NE; ++e) { q.acc[e][t] = f32x4{0.f, 0.f, 0.f, 0.f}; PIN(q.acc[e][t]); } }
  asm volatile("s_nop 1"); SB();
}
template <int PAR, int NE, bool DEEP>
DI void qp2_chunk(QpState<NE>& q, u32x4 (&af)[4][NE], const int c, const int j, const int rot, unsigned& pc, const unsigned pa, const unsigned xvo, const unsigned lv, const unsigned tb,
                  lptr lds, const unsigned l0, rsrc_t rx, rsrc_t rqt) {
  SB(); asm volatile("s_waitcnt vmcnt(%0) lgkmcnt(0)" :: "i"(DEEP ? 6 * NE + 8 : 2 * NE) : "memory"); SB();
  ++pc; prog_post(lds + PPROG + j * 4, pc);
  const unsigned pg = prog_read<16>(pa);
  const int c1 = c + 1 + rot >= 10 ? c + 1 + rot - 10 : c + 1 + rot, c2 = c + 2 + rot >= 10 ? c + 2 + rot - 10 : c + 2 + rot;
  const unsigned s1 = c < 9 ? (2 * c1 + 1) * 24576 + tb : OOB, s2 = c < 8 ? (2 * c2) * 24576 + tb : OOB;
  lptr x0 = q.xa0 + (DEEP ? (c & 2) * XCH : 0), x1 = q.xa1 + (DEEP ? (c & 2) * XCH : 0);
  SB();
#pragma unroll
  for (int u = 0; u < 14; ++u) {
    const int v = (u + 14 - LAG) % 14, t = v % 7, set = u < LAG ? 2 * (PAR ^ 1) + 1 : 2 * PAR + v / 7;
#pragma unroll
    for (int e = 0; e < NE; ++e) {
      mfma_ip(q.acc[e][t], af[set][e], q.xf[t]);
      if (e == 0 && u >= 2) q.xf[u % 7] = lds128((u >= 7 ? x1 : x0) + PAR * XCH + (u % 7) * 2048);
      if (e == NE / 2 && u >= LAG && u < LAG + NE) af[2 * (PAR ^ 1) + 1][u - LAG] = __builtin_amdgcn_raw_buffer_load_b128(rqt, lv + (u - LAG) * 1024, s1, 16);
      if (e == NE / 2 && u >= 10 && u < 10 + NE) af[2 * PAR][u - 10] = __builtin_amdgcn_raw_buffer_load_b128(rqt, lv + (u - 10) * 1024, s2, 16);
      SB();
    }
    if (u == 0) { prog_wait<16, 0>(pa, pg, pc); q.xf[0] = lds128(x0 + PAR * XCH); q.xf[1] = lds128(x0 + PAR * XCH + 2048); SB(); }
    if (u == 1) {
      const bool on = DEEP ? c < 8 : c < 9;
      const int cx = !DEEP ? c1 : (c + 3 + rot >= 10 ? c + 3 + rot - 10 : c + 3 + rot);
      const unsigned xs = on ? j * 10240 + cx * 128 : OOB, xd = l0 + (on ? (DEEP ? RB + ((c + 3) & 3) * XCH : RC + (PAR ^ 1) * XCH) + j * 1024 : DUMMY), st = on ? 4096u : 0u;
#pragma unroll
      for (int i = 0; i < 3; ++i) dma_df(rx, xvo, xs + i * 40960, xd + i * st);
      dma_df(rx, xvo, xs + 3 * 40960, j < 2 ? xd + 3 * st : l0 + DUMMY);
      SB();
    }
  }
}
template <int I>
DI void qp1_chunk(QpState<2>& q, u32x4 (&af)[10][2], const int c, const int j, const int rot, unsigned& pc, const unsigned pa, const unsigned xvo, const unsigned lv,
                  lptr lds, const unsigned l0, rsrc_t rx, rsrc_t rqt) {
  WAITV(20);
  ++pc; prog_post(lds + PPROG + j * 4, pc);
  const unsigned pg = prog_read<16>(pa);
  const int c4 = c + 4 + rot >= 10 ? c + 4 + rot - 10 : c + 4 + rot, c5 = c + 5 + rot >= 10 ? c + 5 + rot - 10 : c + 5 + rot;
  const unsigned s1 = c < 6 ? (2 * c4 + 1) * 24576 + j * 2048 : OOB, s2 = c < 5 ? (2 * c5) * 24576 + j * 2048 : OOB;
  lptr x0 = q.xa0 + (c & 3) * XCH, x1 = q.xa1 + (c & 3) * XCH;
  constexpr int SP = (2 * I + 9) % 10;
  SB();
#pragma unroll
  for (int u = 0; u < 14; ++u) {
    const int v = (u + 14 - LAG) % 14, t = v % 7, set = u < LAG ? SP : 2 * I + v / 7;
#pragma unroll
    for (int e = 0; e < 2; ++e) {
      mfma_ip(q.acc[e][t], af[set][e], q.xf[t]);
      if (e == 0 && u >= 2) q.xf[u % 7] = lds128((u >= 7 ? x1 : x0) + (u % 7) * 2048);
      if (e == 1 && (u == 4 || u == 6)) af[SP][(u - 4) >> 1] = __builtin_amdgcn_raw_buffer_load_b128(rqt, lv + ((u - 4) >> 1) * 1024, s1, 16);
      if (e == 1 && (u == 10 || u == 12)) af[2 * I][(u - 10) >> 1] = __builtin_amdgcn_raw_buffer_load_b128(rqt, lv + ((u - 10) >> 1) * 1024, s2, 16);
      SB();
    }
    if (u == 0) { prog_wait<16, 0>(pa, pg, pc); q.xf[0] = lds128(x0); q.xf[1] = lds128(x0 + 2048); SB(); }
    if (u == 1) {
      const bool on = c < 8;
      const int cx = c + 3 + rot >= 10 ? c + 3 + rot - 10 : c + 3 + rot;
      const unsigned xs = on ? j * 10240 + cx * 128 : OOB, xd = l0 + (on ? RB + ((c + 3) & 3) * XCH + j * 1024 : DUMMY), st = on ? 4096u : 0u;
#pragma unroll
      for (int i = 0; i < 3; ++i) dma_df(rx, xvo, xs + i * 40960, xd + i * st);
      dma_df(rx, xvo, xs + 3 * 40960, j < 2 ? xd + 3 * st : l0 + DUMMY);
      SB();
    }
  }
}
template <int NE> DI void qp2_first(u32x4 (&af)[4][NE], const int rot, const unsigned lv, const unsigned tb, rsrc_t rqt) {
  const int r1 = rot == 9 ? 0 : rot + 1;
#pragma unroll
  for (int k = 0; k < 3; ++k)
#pragma unroll
    for (int e = 0; e < NE; ++e) { af[k][e] = __builtin_amdgcn_raw_buffer_load_b128(rqt, lv + e * 1024, (k < 2 ? 2 * rot + k : 2 * r1) * 24576 + tb, 16); SB(); }
#pragma unroll
  for (int e = 0; e < NE; ++e) { af[3][e] = u32x4{0u, 0u, 0u, 0u}; PIN(af[3][e]); }
}
template <int NE> DI void qp_tail(QpState<NE>& q, const u32x4 (&a)[NE]) {
#pragma unroll
  for (int t = 7 - LAG; t < 7; ++t)
#pragma unroll
    for (int e = 0; e < NE; ++e) mfma_ip(q.acc[e][t], a[e], q.xf[t]);
  MFMA_DONE(); SB();
}

DI void proj_wave(const int j, const int lane, const Ctx& c0, const int rot, lptr lds, const unsigned l0, rsrc_t rx, int& nst) {
  unsigned long long tag;
  const bool pstv = j == 0; (void)pstv;
  const int lg = lane >> 4, lr = lane & 15;
  {
    QpState<2> q;
    u32x4 af[2][2];
    af[1][0] = af[1][1] = u32x4{0u, 0u, 0u, 0u}; PIN(af[1][0]); PIN(af[1][1]);
    qp_init(q, lds + VOFF + SLOT, lane);
    const int q4 = lr >> 2;
    const unsigned wa = l0 + VOFF + (8 * lg + q4) * 256 + (((j ^ q4) * 4 + (lane & 3)) << 4);
#pragma nounroll
    for (int c = 0; c < 10; ++c) {
      const int bo = (c & 3) * PBUF;
      BAR_L();
#pragma unroll
      for (int u = 0; u < 14; ++u) {
        const int v = (u + 14 - LAG) % 14, t = v % 7, set = u < LAG ? 1 : v / 7;
        auto ard = [&](int ks, int e) { lptr p = lp(wa + bo) + ks * 8192 + e * 8; const u32x2 lo = ldstr(p), hi = ldstr(p + 1024); af[ks][e] = u32x4{lo[0], lo[1], hi[0], hi[1]}; };
        if (u == 0) { ard(0, 0); q.xf[0] = lds128(q.xa0 + bo); ard(0, 1); SB(); }
#pragma unroll
        for (int e = 0; e < 2; ++e) {
          mfma_ip(q.acc[e][t], af[set][e], q.xf[t]);
          if (e == 0 && u >= 1) q.xf[u % 7] = lds128((u >= 7 ? q.xa1 : q.xa0) + bo + (u % 7) * 2048);
          if (e == 1 && (u == 4 || u == 5)) ard(1, u - 4);
          SB();
        }
      }
      PST();
    }
    qp_tail(q, af[1]);
    BAR_L();
#pragma unroll
    for (int t = 0; t < 7; ++t)
      *(LAS u32x4*)(lds + RA + t * 4096 + j * 1024 + lane * 16) = u32x4{cvtpk(q.acc[0][t][0] * QSCALE, q.acc[0][t][1] * QSCALE), cvtpk(q.acc[0][t][2] * QSCALE, q.acc[0][t][3] * QSCALE),
                                                                        cvtpk(q.acc[1][t][0] * QSCALE, q.acc[1][t][1] * QSCALE), cvtpk(q.acc[1][t][2] * QSCALE, q.acc[1][t][3] * QSCALE)};
    { const unsigned long long tg = *(const LAS unsigned long long*)(lds + TAGW); tag = ((unsigned long long)rfl((unsigned)(tg >> 32)) << 32) | rfl((unsigned)tg); }
    BAR_L();
    PST();
  }
  unsigned pc = 0;
  __builtin_amdgcn_s_setprio(QPRIO);
  {
    const Ctx c = c0.fresh();
    const rsrc_t rqt = c.rqt();
    const unsigned pa = l0 + APROG + (lane & 3) * 4, lv = lane * 16;
    const unsigned xvo = (lane >> 3) * 1280 + (((lane & 7) ^ ((4 * (j & 1) + lg) & 7)) << 4);
#pragma unroll
    for (int k = 0; k < 3; ++k) {
      const unsigned xs = j * 10240 + (k + rot >= 10 ? k + rot - 10 : k + rot) * 128, xd = l0 + RB + k * XCH + j * 1024;
#pragma unroll
      for (int i = 0; i < 3; ++i) dma_df(rx, xvo, xs + i * 40960, xd + i * 4096);
      dma_df(rx, xvo, xs + 3 * 40960, j < 2 ? xd + 3 * 4096 : l0 + DUMMY);
    }
    wait_flag(c.tflags() + (lane & 31), __hip_atomic_load(c.tflags() + (lane & 31), RLX_AGENT), tag);
    u32x4 af[4][4];
    {
      QpState<2> q; u32x4 a2[10][2];
#pragma unroll
      for (int k = 0; k < 9; ++k) {
        const int ck = (k >> 1) + rot >= 10 ? (k >> 1) + rot - 10 : (k >> 1) + rot;
#pragma unroll
        for (int e = 0; e < 2; ++e) { a2[k][e] = __builtin_amdgcn_raw_buffer_load_b128(rqt, lv + e * 1024, (2 * ck + (k & 1)) * 24576 + j * 2048, 16); SB(); }
      }
      a2[9][0] = a2[9][1] = u32x4{0u, 0u, 0u, 0u}; PIN(a2[9][0]); PIN(a2[9][1]);
      qp_init(q, lds + RB, lane);
      PST();
#pragma nounroll
      for (int cc = 0; cc < 10; cc += 5) {
        qp1_chunk<0>(q, a2, cc, j, rot, pc, pa, xvo, lv, lds, l0, rx, rqt); qp1_chunk<1>(q, a2, cc + 1, j, rot, pc, pa, xvo, lv, lds, l0, rx, rqt);
        qp1_chunk<2>(q, a2, cc + 2, j, rot, pc, pa, xvo, lv, lds, l0, rx, rqt); qp1_chunk<3>(q, a2, cc + 3, j, rot, pc, pa, xvo, lv, lds, l0, rx, rqt);
        qp1_chunk<4>(q, a2, cc + 4, j, rot, pc, pa, xvo, lv, lds, l0, rx, rqt);
      }
      qp_tail(q, a2[9]);
      parrive(pc, j, lds);
      qp2_first(af, rot, lv, (j + 2) * 4096, rqt);
      prog_wait<16, 0>(pa, pc);
      lptr qp = lds + RB + (8 * (j >> 1) + 2 * lg) * 256 + lr * 16 + (j & 1) * 4;
#pragma unroll
      for (int t = 0; t < 7; ++t)
#pragma unroll
        for (int r = 0; r < 4; ++r) *(LAS unsigned*)(qp + t * 4096 + (r >> 1) * 256 + (r & 1) * 8) = cvtpk(q.acc[0][t][r] * QSCALE, q.acc[1][t][r] * QSCALE);
      parrive(pc, j, lds);
      PST();
    }
    QpState<4> q;
    qp_init(q, lds + RC, lane);
#pragma nounroll
    for (int cc = 0; cc < 10; cc += 2) {
      qp2_chunk<0, 4, false>(q, af, cc, j, rot, pc, pa, xvo, lv, (j + 2) * 4096, lds, l0, rx, rqt);
      qp2_chunk<1, 4, false>(q, af, cc + 1, j, rot, pc, pa, xvo, lv, (j + 2) * 4096, lds, l0, rx, rqt);
      PST();
    }
    qp_tail(q, af[3]);
    parrive(pc, j, lds);
    u32x4 qf[7][2];
#pragma unroll
    for (int t = 0; t < 7; ++t)
#pragma unroll
      for (int hf = 0; hf < 2; ++hf) {
        qf[t][hf] = u32x4{cvtpk(q.acc[0][t][2 * hf] * QSCALE, q.acc[1][t][2 * hf] * QSCALE), cvtpk(q.acc[2][t][2 * hf] * QSCALE, q.acc[3][t][2 * hf] * QSCALE),
                          cvtpk(q.acc[0][t][2 * hf + 1] * QSCALE, q.acc[1][t][2 * hf + 1] * QSCALE), cvtpk(q.acc[2][t][2 * hf + 1] * QSCALE, q.acc[3][t][2 * hf + 1] * QSCALE)};
        PIN(qf[t][hf]);
      }
    prog_wait<16, 0>(pa, pc);
    prog_wait<32, 0>(pa, 1);
#pragma unroll
    for (int t = 0; t < 7; ++t)
#pragma unroll
      for (int hf = 0; hf < 2; ++hf) *(LAS u32x4*)(lds + ((j >> 1) ? RC : RA) + (4 * (j & 1) + lg) * 512 + lr * 16 + t * 4096 + hf * 256) = qf[t][hf];
    parrive(pc, j, lds);
    PST();
  }
  __builtin_amdgcn_s_setprio(0);
  __builtin_amdgcn_s_waitcnt(0x0F70);
}

__global__ __launch_bounds__(512, 2) void attn_fused(const char* __restrict__ x, const char* __restrict__ K, const char* __restrict__ V,
                                                     const char* __restrict__ wq, const char* __restrict__ wo, char* __restrict__ ws, char* __restrict__ out) {
  extern __shared__ __attribute__((aligned(1024))) char smem[];
  const int w = rfl(threadIdx.x >> 6);
  unsigned long long pc0 = 0;
  if (CODEPF) asm volatile("s_getpc_b64 %0" : "=s"(pc0));
  const int bid = blockIdx.x;
  const int b = ((bid >> 1) & 3) * 8 + (bid >> 5), g = (bid & 1) * 4 + ((bid >> 3) & 3);
  const int rot = ROT ? (((b & 7) * 5) / 4 + ((g & 3) * 5) / 2) % 10 : 0;
  lptr lds = (lptr)smem;
  const unsigned l0 = (unsigned)(uintptr_t)lds;
  const Ctx c = {ws, b, g};
  const rsrc_t rx = mkrsrc(x + (size_t)b * 143360, 143360), rwq = mkrsrc(wq, 640 * 8192);
  int nst = 0; (void)nst;
  if (w < 4) {
    const size_t kvb = ((size_t)b * 32 + g * 4) * 131072;
    attn_wave(w, fresh_lane(), rot, lds, l0, mkrsrc(K + kvb, 524288), mkrsrc(V + kvb, 524288), rx, rwq, wo, c, nst);
    oproj_pass<3>(w, false, c, lds, l0, nst);
    ST(); ST();
  } else {
    if (CODEPF && w < 6) dma_df(mkrsrc((const void*)(pc0 & ~255ull), CODEPF), fresh_lane() * 16, ((blockIdx.x >> 3) * 2 + (w - 4)) * 1024, l0 + DUMMY + 1024);
    proj_wave(w - 4, fresh_lane(), c, rot, lds, l0, rx, nst);
    oproj_pass<5>(w - 4, w == 4, c, lds, l0, nst);
    oproj_pass<2>(w - 4, w == 4, c, lds, l0, nst);
    final_part(w - 4, c, lds, l0, out, nst);
    __builtin_amdgcn_s_waitcnt(0x0F70);
  }
}

extern "C" void kernel_launch(void* const* d_in, const int* in_sizes, int n_in, void* d_out, int out_size, void* d_ws, size_t ws_size, hipStream_t stream) {
  (void)hipFuncSetAttribute((const void*)attn_fused, hipFuncAttributeMaxDynamicSharedMemorySize, LDS_BYTES);
  attn_fused<<<256, 512, LDS_BYTES, stream>>>((const char*)d_in[0], (const char*)d_in[1], (const char*)d_in[2], (const char*)d_in[3], (const char*)d_in[4], (char*)d_ws + WSOFF, (char*)d_out);
}
```

```cpp
#include <hip/hip_runtime.h>
#include <stdint.h>
typedef __attribute__((ext_vector_type(8))) __bf16 bf16x8;
typedef __attribute__((ext_vector_type(2))) __bf16 bf16x2;
typedef __attribute__((ext_vector_type(4))) float f32x4;
typedef __attribute__((ext_vector_type(16))) float f32x16;
typedef __attribute__((ext_vector_type(2))) float f32x2;
typedef __attribute__((ext_vector_type(4))) unsigned u32x4;
typedef __attribute__((ext_vector_type(2))) unsigned u32x2;
typedef __attribute__((ext_vector_type(4))) short s16x4;
#define LAS __attribute__((address_space(3)))
#define DI __device__ __forceinline__
typedef LAS char* lptr;
typedef __amdgpu_buffer_rsrc_t rsrc_t;
typedef __attribute__((address_space(1))) unsigned long long gu64;
#define RLX_AGENT __ATOMIC_RELAXED, __HIP_MEMORY_SCOPE_AGENT

constexpr int SLOT = 16384, VOFF = 2 * SLOT, XCH = 14336, RA = 4 * SLOT, RB = RA + 2 * XCH, RC = RB + 2 * XCH, PBUF = SLOT + XCH, DUMMY = VOFF + 4 * PBUF,
              TAGW = DUMMY + 4096, CNT = TAGW + 16, APROG = CNT + 32, PPROG = APROG + 16, QPROG = PPROG + 16, KPROG = QPROG + 16, STMP = CNT + 128, ARR = STMP + 512, LDS_BYTES = ARR + 1024;
constexpr float QSCALE = 0.08838834764831845f * 1.4426950408889634f;
constexpr unsigned OOB = 0x40000000u;
constexpr size_t WS_TF = 8192, WS_OF = 16384, WS_XF = WS_OF + 131072, WS_O = 1u << 20, WS_Q = 40u << 20, WS_WT = 48u << 20, WS_QT = 56u << 20, WS_PX = 64u << 20, WS_ST = 128u << 20;
constexpr int OWN3 = 0, OWNP = 2 * XCH;
constexpr unsigned P_Q1 = 12, P_Q23 = 24;

DI unsigned rfl(unsigned x) { return __builtin_amdgcn_readfirstlane(x); }
DI unsigned cvtpk(float lo, float hi) { f32x2 v = {lo, hi}; bf16x2 b = __builtin_convertvector(v, bf16x2); return __builtin_bit_cast(unsigned, b); }
DI f32x4 mfma(u32x4 a, u32x4 b, f32x4 c) { return __builtin_amdgcn_mfma_f32_16x16x32_bf16(__builtin_bit_cast(bf16x8, a), __builtin_bit_cast(bf16x8, b), c, 0, 0, 0); }
DI void mfma_ip(f32x4& c, const u32x4& a, const u32x4& b) { asm volatile("v_mfma_f32_16x16x32_bf16 %0, %1, %2, %0" : "+v"(c) : "v"(a), "v"(b)); }
#define MFMA_DONE() asm volatile("s_nop 15")
DI f32x16 mfma32(u32x4 a, u32x4 b, f32x16 c) { return __builtin_amdgcn_mfma_f32_32x32x16_bf16(__builtin_bit_cast(bf16x8, a), __builtin_bit_cast(bf16x8, b), c, 0, 0, 0); }
DI u32x4 lds128(lptr p) { return *(const LAS u32x4*)p; }
DI int fresh_lane() { int l; asm volatile("v_mbcnt_lo_u32_b32 %0, -1, 0\n\tv_mbcnt_hi_u32_b32 %0, -1, %0" : "=v"(l)); return l; }
DI lptr lp(unsigned a) { return (lptr)(uintptr_t)a; }
DI u32x2 ldstr(lptr p) { return __builtin_bit_cast(u32x2, __builtin_amdgcn_ds_read_tr16_b64_v4i16((LAS s16x4*)p)); }
DI rsrc_t mkrsrc(const void* p, unsigned bytes) { return __builtin_amdgcn_make_buffer_rsrc((void*)p, 0, bytes, 0x00020000); }
DI float xmax(float m) {
  auto a = __builtin_amdgcn_permlane16_swap(__float_as_uint(m), __float_as_uint(m), false, false); m = fmaxf(__uint_as_float(a[0]), __uint_as_float(a[1]));
  auto b = __builtin_amdgcn_permlane32_swap(__float_as_uint(m), __float_as_uint(m), false, false); return fmaxf(__uint_as_float(b[0]), __uint_as_float(b[1]));
}
DI float xsum(float m) {
  auto a = __builtin_amdgcn_permlane16_swap(__float_as_uint(m), __float_as_uint(m), false, false); m = __uint_as_float(a[0]) + __uint_as_float(a[1]);
  auto b = __builtin_amdgcn_permlane32_swap(__float_as_uint(m), __float_as_uint(m), false, false); return __uint_as_float(b[0]) + __uint_as_float(b[1]);
}
#define DFPOL ""
#define TAUX 0
#define KVPOL "nt"
#define VPOL "nt"
template <int OFF, bool V = false> DI void dma_nt(rsrc_t r, unsigned voff, unsigned soff, unsigned ldsaddr) {
  if (V) asm volatile("s_mov_b32 m0, %3\n\ts_nop 0\n\tbuffer_load_dwordx4 %0, %1, %2 offen offset:%4 " VPOL " lds" :: "v"(voff), "s"(r), "s"(soff), "s"(ldsaddr), "i"(OFF) : "memory");
  else asm volatile("s_mov_b32 m0, %3\n\ts_nop 0\n\tbuffer_load_dwordx4 %0, %1, %2 offen offset:%4 " KVPOL " lds" :: "v"(voff), "s"(r), "s"(soff), "s"(ldsaddr), "i"(OFF) : "memory");
}
template <int OFF> DI void dma_more(rsrc_t r, unsigned voff, unsigned soff) {
  asm volatile("buffer_load_dwordx4 %0, %1, %2 offen offset:%3 nt lds" :: "v"(voff), "s"(r), "s"(soff), "i"(OFF) : "memory");
}
DI void dma_df(rsrc_t r, unsigned voff, unsigned soff, unsigned ldsaddr) {
  asm volatile("s_mov_b32 m0, %3\n\ts_nop 0\n\tbuffer_load_dwordx4 %0, %1, %2 offen " DFPOL "lds" :: "v"(voff), "s"(r), "s"(soff), "s"(ldsaddr) : "memory");
}
DI void dma_sc1(rsrc_t r, unsigned voff, unsigned soff, unsigned ldsaddr) {
  asm volatile("s_mov_b32 m0, %3\n\ts_nop 0\n\tbuffer_load_dwordx4 %0, %1, %2 offen sc1 lds" :: "v"(voff), "s"(r), "s"(soff), "s"(ldsaddr) : "memory");
}
DI void touch(rsrc_t r, unsigned voff, unsigned ldsaddr) {
  asm volatile("s_mov_b32 m0, %2\n\ts_nop 0\n\tbuffer_load_dword %0, %1, 0 offen lds" :: "v"(voff), "s"(r), "s"(ldsaddr) : "memory");
}
#define SB() __builtin_amdgcn_sched_barrier(0)
#define WAITV(N) do { SB(); asm volatile("s_waitcnt vmcnt(" #N ") lgkmcnt(0)" ::: "memory"); SB(); } while (0)
#define WAITVM(N) do { SB(); asm volatile("s_waitcnt vmcnt(" #N ")" ::: "memory"); SB(); } while (0)
#define WAITL() do { SB(); asm volatile("s_waitcnt lgkmcnt(0)" ::: "memory"); SB(); } while (0)
#define BAR_L() do { SB(); asm volatile("s_waitcnt lgkmcnt(0)\n\ts_barrier" ::: "memory"); SB(); } while (0)
#define ST() do {} while (0)
#define PST() do {} while (0)
#define PST2() do {} while (0)
#define PCAP 0
#define PC() do { if (PCAP) asm volatile("s_waitcnt vmcnt(%0)" :: "i"(PCAP - 1) : "memory"); } while (0)
#define PIN(x) asm volatile("" : "+v"(x))
#define NODMA 0
#define DPOS 2
#define DSTR 8
#define CAP 16
#define SAUX 16
#define PPRIO 0
#define ABL 0
#define TE0 2
#define XPRIO 0
#define PF 0
#define VP0 2
#define VST 4
#define KP0 18
#define KST 4
#define QPACE 10
#define OPACE 0
#define OP0 26
#define MAP 4
#define OSA 16
#define WSOFF 0
#define CODEPF 0
#define NH 2
#define SSL 0
#define DSL 0
#define LPRIO 3
#define ILV 0
#define TC 1
#define TN0 0, 9, 8, 8, 0, 4, 0, 5, 0, 0
#define LCAP 0
#define RPACE 0
#define WARM 0
#define TPRIO 2
#define OSL 3
#define HSL 0
#define BURST 0
#define QPRIO 0
#define RN0 0
#define RP0 76
#define HPRIO 0
#define ROT 1
#define QLD0 6
#define PFD 16
constexpr float PLIM = 1024.f;

template <int OFF> DI unsigned prog_read(unsigned pa) { return *(volatile LAS unsigned*)(lp(pa) + OFF); }
template <int OFF, int SLEEP> DI void prog_wait(unsigned pa, unsigned v, unsigned t) {
  unsigned n;
  asm volatile("s_mov_b32 %1, 0x1000000\n"
               "1:\n\tv_cmp_gt_u32 vcc, %3, %0\n\ts_cbranch_vccz 2f\n\ts_sleep %5\n\tds_read_b32 %0, %2 offset:%4\n\ts_sub_u32 %1, %1, 1\n\ts_waitcnt lgkmcnt(0)\n\ts_cbranch_scc0 1b\n2:"
               : "+v"(v), "=&s"(n) : "v"(pa), "s"(t), "i"(OFF), "i"(SLEEP) : "memory", "vcc", "scc");
}
template <int OFF, int SLEEP> DI void prog_wait(unsigned pa, unsigned t) { prog_wait<OFF, SLEEP>(pa, prog_read<OFF>(pa), t); }
DI void prog_post(lptr p, unsigned v) { asm volatile("" ::: "memory"); *(volatile LAS unsigned*)p = v; }

DI void publish(gu64* flag, lptr cnt, unsigned long long tag, int lane) {
  asm volatile("s_waitcnt vmcnt(0)" ::: "memory");
  if (lane == 0) {
    const unsigned old = __hip_atomic_fetch_add((LAS unsigned*)cnt, 1u, __ATOMIC_RELAXED, __HIP_MEMORY_SCOPE_WORKGROUP);
    asm volatile("" : "+s"(tag));
    if (old == 3u) __hip_atomic_store(flag, tag, RLX_AGENT);
  }
}
DI void wait_flag(gu64* f, unsigned long long v, unsigned long long tag) {
#pragma nounroll
  for (unsigned spins = 0; spins < (1u << 15); ++spins) {
    if (__all(v == tag)) break;
    __builtin_amdgcn_s_sleep(2);
    v = __hip_atomic_load(f, RLX_AGENT);
  }
  __builtin_amdgcn_s_waitcnt(0x0F70);
  __builtin_amdgcn_fence(__ATOMIC_ACQUIRE, "wavefront");
  asm volatile("" ::: "memory");
}

struct Ctx {
  char* ws; int b, g;
  DI Ctx fresh() const { Ctx c = *this; asm volatile("" : "+s"(c.b), "+s"(c.g)); return c; }
  DI gu64* epoch() const { return (gu64*)ws; }
  DI gu64* tflags() const { return (gu64*)(ws + WS_TF + g * 256); }
  DI gu64* xflag(int dst) const { return (gu64*)(ws + WS_XF + (b * 8 + dst) * 128); }
  DI rsrc_t rpx(int dst) const { return mkrsrc(ws + WS_PX + (size_t)(b * 8 + dst) * 143360, (ABL & 64) ? 0 : 143360); }
  DI rsrc_t rwt() const { return mkrsrc(ws + WS_WT + (size_t)g * 655360, 655360); }
  DI rsrc_t rqt() const { return mkrsrc(ws + WS_QT + (size_t)g * 491520, 491520); }
};
template <int NQ> struct AttnState {
  u32x4 q[NQ][4], fr[NQ == 1 ? 16 : 8], p[2][NQ];
  f32x4 o[8][NQ], negm[NQ], sc[2][4][NQ];
  float l[NQ], rs[NQ], inv[NQ];
  unsigned ka[4], va[8], kvo, vvo, pa, pb, pg, oad, last;
};

DI int qreg(int r) { return r == 3 ? RC : ((r & 1) ? RB : RA); }
template <int NQ> DI void sm_pair(AttnState<NQ>& a, const f32x4 (&x)[4][NQ], u32x4 (&p)[NQ], const int hf, const int n) {
  const int t = n >> 2, d = n & 3;
  if (t >= NQ) return;
  const float e0 = __builtin_amdgcn_exp2f(x[2 * hf + (d >> 1)][t][2 * (d & 1)]), e1 = __builtin_amdgcn_exp2f(x[2 * hf + (d >> 1)][t][2 * (d & 1) + 1]);
  a.rs[t] = d ? (a.rs[t] + e0) + e1 : e0 + e1;
  p[t][d] = cvtpk(e0, e1);
  PIN(p[t][d]); PIN(a.rs[t]);
}
template <int NQ, int HF, bool ADJ>
DI void fixup(AttnState<NQ>& a, f32x4 (&x)[4][NQ], f32x4 (&y)[4][NQ]) {
#pragma unroll
  for (int t = 0; t < NQ; ++t) {
    float mx = x[3][t][3];
#pragma unroll
    for (int i = 8 * HF; i < 15; ++i) mx = fmaxf(mx, x[i >> 2][t][i & 3]);
    const float dl = __builtin_ceilf(fmaxf(xmax(mx), 0.f)), sc = __builtin_amdgcn_exp2f(-dl);
    a.l[t] *= sc; a.negm[t] -= dl;
#pragma unroll
    for (int dt = 0; dt < 8; ++dt) a.o[dt][t] *= sc;
    if (HF) {
#pragma unroll
      for (int d = 0; d < 4; ++d) { const unsigned v = a.p[0][t][d]; a.p[0][t][d] = cvtpk(__uint_as_float(v << 16) * sc, __uint_as_float(v & 0xffff0000u) * sc); }
    }
#pragma unroll
    for (int kt = 2 * HF; kt < 4; ++kt) x[kt][t] -= dl;
    float r = 0.f;
#pragma unroll
    for (int d = 0; d < 4; ++d) { const float e0 = __builtin_amdgcn_exp2f(x[2 * HF + (d >> 1)][t][2 * (d & 1)]), e1 = __builtin_amdgcn_exp2f(x[2 * HF + (d >> 1)][t][2 * (d & 1) + 1]);
      r += e0 + e1; a.p[HF][t][d] = cvtpk(e0, e1); }
    a.rs[t] = r;
    if (ADJ) {
#pragma unroll
      for (int kt = 0; kt < 4; ++kt) y[kt][t] -= dl;
    }
  }
}
template <int NQ, int HF, bool CHECK, bool ADJ>
DI void sm_close(AttnState<NQ>& a, f32x4 (&x)[4][NQ], f32x4 (&y)[4][NQ]) {
  if (CHECK) {
    float m = NQ == 2 ? a.rs[0] + a.rs[NQ - 1] : a.rs[0]; PIN(m);
    if (NQ == 2) { if (__builtin_expect(__any(!(m <= PLIM)), 0)) fixup<NQ, HF, ADJ>(a, x, y); }
    else {
#pragma nounroll
      while (__builtin_expect(__any(!(m <= PLIM)), 0)) { fixup<NQ, HF, ADJ>(a, x, y); m = 0.f; PIN(m); }
    }
  }
#pragma unroll
  for (int t = 0; t < NQ; ++t) { PIN(a.rs[t]); a.l[t] += a.rs[t]; PIN(a.l[t]); }
}
template <int NQ> DI u32x4 o_frag(const AttnState<NQ>& a, const int m, const int t) {
  const f32x4 x = a.o[2 * m][t] * a.inv[t], y = a.o[2 * m + 1][t] * a.inv[t];
  return u32x4{cvtpk(x[0], x[1]), cvtpk(x[2], x[3]), cvtpk(y[0], y[1]), cvtpk(y[2], y[3])};
}
template <bool V, int NQ> DI void frag_read(AttnState<NQ>& a, const int m, const int par) {
  constexpr int FM = NQ == 1 ? 15 : 7;
  if (!V) a.fr[m & FM] = lds128(lp(a.ka[m & 3]) + par * SLOT + (m >> 2) * 4096);
  else { lptr p = lp(a.va[m & 7]) + par * SLOT + (m >> 3) * 8192; const u32x2 v0 = ldstr(p), v1 = ldstr(p + 4096); a.fr[m & FM] = u32x4{v0[0], v0[1], v1[0], v1[1]}; }
}
template <bool V, int NQ> DI void tile_issue(const AttnState<NQ>& a, rsrc_t r, const unsigned gsrc, const unsigned dst, const unsigned st = 4096) {
  if (NODMA) return;
#pragma unroll
  for (int i = 0; i < 4; ++i) {
    if (LCAP) asm volatile("s_waitcnt vmcnt(%0)" :: "i"(LCAP - 4) : "memory");
    dma_nt<0, V>(r, V ? a.vvo : a.kvo, gsrc + i * 4096, dst + i * st);          dma_nt<1024, V>(r, V ? a.vvo ^ 128 : a.kvo ^ 64, gsrc + i * 4096, dst + i * st);
    dma_nt<2048, V>(r, V ? a.vvo : a.kvo ^ 128, gsrc + i * 4096, dst + i * st); dma_nt<3072, V>(r, V ? a.vvo ^ 128 : a.kvo ^ 192, gsrc + i * 4096, dst + i * st);
  }
}
template <bool CV, int NQ> DI void phase_io(AttnState<NQ>& a, const int i, const int cp, const int np, const unsigned val, const int w, lptr lds) {
  if (NQ == 1) { if (i == 12) a.pg = prog_read<0>(a.pb); return; }
  if (i <= 10) frag_read<CV>(a, i + 5, cp);
  if (NQ == 2 && i == 10) {
    SB();
    prog_post(lds + APROG + w * 4, val);
    a.pg = prog_read<0>(a.pb);
  }
  if (NQ == 2 && i == 12) prog_wait<0, HSL>(a.pb, a.pg, val);
  if (i == 13 || i == 14) { frag_read<!CV>(a, 2 * (i - 13), np); frag_read<!CV>(a, 2 * (i - 13) + 1, np); }
  if (i == 15) frag_read<!CV>(a, 4, np);
}
template <bool V, int NQ> DI void loader_turn(AttnState<NQ>& a, const unsigned val, lptr lds, rsrc_t r, const unsigned gsrc, const unsigned dst0, const int np) {
  if (NQ == 1) {
    WAITVM(16);
    prog_post(lds + APROG + 12, val + 1);
    prog_wait<0, 1>(a.pb, a.pg, val);
    const unsigned dst = gsrc < 32 * SLOT ? dst0 : (unsigned)(uintptr_t)lds + DUMMY, st = gsrc < 32 * SLOT ? 4096u : 0u;
#pragma unroll
    for (int i = 0; i < 4; ++i) {
      dma_nt<0, V>(r, V ? a.vvo : a.kvo, gsrc + i * 4096, dst + i * st);                frag_read<!V>(a, 4 * i, np); SB();
      dma_more<1024>(r, V ? a.vvo ^ 128 : a.kvo ^ 64, gsrc + i * 4096);                 frag_read<!V>(a, 4 * i + 1, np); SB();
      dma_more<2048>(r, V ? a.vvo : a.kvo ^ 128, gsrc + i * 4096);                      frag_read<!V>(a, 4 * i + 2, np); SB();
      dma_more<3072>(r, V ? a.vvo ^ 128 : a.kvo ^ 192, gsrc + i * 4096);                frag_read<!V>(a, 4 * i + 3, np); SB();
    }
  }
}
template <int KIND, int PAR, int NQ>
DI void attn_step(AttnState<NQ>& a, const int s, const int w, const int lane, lptr lds, const unsigned l0, rsrc_t rk, rsrc_t rv) {
  const f32x4 zero = {0.f, 0.f, 0.f, 0.f};
  f32x4 (&c)[4][NQ] = a.sc[PAR]; f32x4 (&d)[4][NQ] = a.sc[PAR ^ 1];
  SB();
  if (KIND == 1) {
    prog_wait<16, 1>(a.pa, s == 0 ? 0u : (s == 8 ? P_Q1 : P_Q23));
    const unsigned qa = l0 + qreg(s >> 3) + 2 * w * 4096 + lane * 16;
#pragma unroll
    for (int i = 0; i < 4; ++i)
#pragma unroll
      for (int t = 0; t < NQ; ++t) a.q[t][i] = lds128(lp(qa) + t * 4096 + i * 1024);
    a.oad = qa;
    SB();
  }
#pragma unroll
  for (int i = 0; i < 16; ++i) {
    constexpr int PA[16] = {-1, 0, 4, -1, 1, 5, -1, 2, 6, -1, -1, 3, 7, -1, -1, -1};
#pragma unroll
    for (int t = 0; t < NQ; ++t) {
      c[i >> 2][t] = mfma(a.fr[i & (NQ == 1 ? 15 : 7)], a.q[t][i & 3], (i & 3) ? c[i >> 2][t] : (KIND == 1 ? zero : a.negm[t]));
      if (t == 0) { if (PA[i] >= 0) sm_pair(a, d, a.p[1], 1, PA[i]); SB(); }
    }
    if (i == 12) PIN(c[3][NQ - 1]);
    phase_io<false>(a, i, PAR, PAR ^ 1, 2 * s + 1, w, lds);
    if (KIND == 2 && i >= 8 && ((i - 8) >> 2) < NQ) *(LAS u32x4*)(lp(a.oad) + ((i - 8) >> 2) * 4096 + (i & 3) * 1024) = o_frag(a, i & 3, ((i - 8) >> 2) % NQ);
    SB();
  }
  if (KIND == 2) prog_post(lds + QPROG + w * 4, s);
  sm_close<NQ, 1, KIND != 2, KIND == 0>(a, d, c);
  loader_turn<false>(a, 2 * s + 1, lds, rk, (s + 2) * SLOT, l0 + PAR * SLOT, PAR ^ 1);
  if (KIND == 1) {
#pragma unroll
    for (int t = 0; t < NQ; ++t) { a.inv[t] = __builtin_amdgcn_rcpf(xsum(a.l[t])); a.l[t] = 0.f; }
  }
  SB();
  u32x4 pn[NQ];
#pragma unroll
  for (int i = 0; i < 16; ++i) {
    constexpr int PB[16] = {-1, -1, -1, 0, -1, 4, -1, 1, -1, 5, -1, 2, 6, -1, 3, 7};
#pragma unroll
    for (int t = 0; t < NQ; ++t) {
      a.o[i & 7][t] = mfma(a.fr[i & (NQ == 1 ? 15 : 7)], a.p[i >> 3][t], (KIND == 2 && i < 8) ? zero : a.o[i & 7][t]);
      if (t == 0) { if (PB[i] >= 0) sm_pair(a, c, pn, 0, PB[i]); SB(); }
    }
    if (i == 12) PIN(a.o[4][NQ - 1]);
    phase_io<true>(a, i, PAR ^ 1, PAR ^ 1, 2 * s + 2, w, lds);
    if (KIND == 1 && (i == 1 || i == 2) && i - 1 < NQ) {
      const int t = (i - 1) % NQ;
      float m0 = c[3][t][3];
#pragma unroll
      for (int e = 0; e < 15; ++e) m0 = fmaxf(m0, c[e >> 2][t][e & 3]);
      m0 = xmax(m0);
#pragma unroll
      for (int kt = 0; kt < 4; ++kt) { c[kt][t] -= m0; PIN(c[kt][t]); }
      a.negm[t] = f32x4{-m0, -m0, -m0, -m0}; PIN(a.negm[t]);
    }
    SB();
  }
#pragma unroll
  for (int t = 0; t < NQ; ++t) a.p[0][t] = pn[t];
  sm_close<NQ, 0, KIND != 1, false>(a, c, c);
  loader_turn<true>(a, 2 * s + 2, lds, rv, (s + 1) * SLOT, l0 + VOFF + (PAR ^ 1) * SLOT, PAR ^ 1);
}

template <int NQ>
DI void attn_main(const int w, lptr lds, const unsigned l0, rsrc_t rk, rsrc_t rv, const Ctx& c0, int& nst) {
  const int lane = fresh_lane(), lg = lane >> 4, lr = lane & 15;
  AttnState<NQ> a;
  a.kvo = lg * 256 + ((lr ^ lg) << 4);
  a.vvo = lg * 256 + ((lr ^ (lg << 1)) << 4);
  if (NQ == 1) { tile_issue<false>(a, rk, 0, l0); }
  BAR_L();
  if (NQ == 1) { tile_issue<false>(a, rk, SLOT, l0 + SLOT); }
  BAR_L();
  if (NQ == 1) { tile_issue<true>(a, rv, 0, l0 + VOFF); prog_post(lds + APROG + 12, 1); __builtin_amdgcn_s_setprio(LPRIO); } else __builtin_amdgcn_s_setprio(HPRIO);
#pragma unroll
  for (int i = 0; i < 4; ++i) { a.ka[i] = l0 + lr * 256 + (((4 * i + lg) ^ lr) << 4); PIN(a.ka[i]); }
#pragma unroll
  for (int i = 0; i < 8; ++i) { a.va[i] = l0 + VOFF + (lane >> 2) * 256 + ((i ^ ((lane >> 2) & 7)) << 5) + (lane & 3) * 8; PIN(a.va[i]); }
  a.pg = 0; a.pa = l0 + APROG + (lane & 3) * 4; a.pb = l0 + APROG + (NQ == 2 ? 12 : ((lane & 3) == 3 ? 0 : (lane & 3) * 4)); a.oad = 0; a.last = RPACE ? (unsigned)__builtin_amdgcn_s_memrealtime() - 1000u : 0u;
#pragma unroll
  for (int t = 0; t < NQ; ++t) {
    a.l[t] = 0.f; a.inv[t] = 0.f; a.rs[t] = 0.f; a.p[0][t] = a.p[1][t] = u32x4{0u, 0u, 0u, 0u}; a.negm[t] = f32x4{0.f, 0.f, 0.f, 0.f};
#pragma unroll
    for (int i = 0; i < 8; ++i) { a.o[i][t] = f32x4{0.f, 0.f, 0.f, 0.f}; a.sc[1][i & 3][t] = f32x4{0.f, 0.f, 0.f, 0.f}; }
  }
  WAITVM(32);
  BAR_L(); ST();
#pragma unroll
  for (int m = 0; m < (NQ == 1 ? 16 : 5); ++m) frag_read<false>(a, m, 0);
#pragma nounroll
  for (int r = 0; r < 4; ++r) {
    attn_step<1, 0>(a, 8 * r, w, lane, lds, l0, rk, rv); ST();
    attn_step<2, 1>(a, 8 * r + 1, w, lane, lds, l0, rk, rv); ST();
#pragma nounroll
    for (int t = 2; t < 8; t += 2) {
      attn_step<0, 0>(a, 8 * r + t, w, lane, lds, l0, rk, rv); ST();
      attn_step<0, 1>(a, 8 * r + t + 1, w, lane, lds, l0, rk, rv); ST();
    }
  }
  {
#pragma unroll
    for (int i = 0; i < 8; ++i) sm_pair(a, a.sc[1], a.p[1], 1, i);
    if (NQ == 2) prog_wait<0, 0>(a.pb, 65);
#pragma unroll
    for (int m = 0; m < 8; ++m) frag_read<true>(a, m, 1);
    sm_close<NQ, 1, true, false>(a, a.sc[1], a.sc[1]);
    SB();
#pragma unroll
    for (int i = 0; i < 16; ++i) {
#pragma unroll
      for (int t = 0; t < NQ; ++t) a.o[i & 7][t] = mfma(a.fr[i & (NQ == 1 ? 15 : 7)], a.p[i >> 3][t], a.o[i & 7][t]);
      if (i < 8) frag_read<true>(a, i + 8, 1);
      SB();
    }
#pragma unroll
    for (int t = 0; t < NQ; ++t) a.inv[t] = __builtin_amdgcn_rcpf(xsum(a.l[t]));
  }
#pragma unroll
  for (int t = 0; t < NQ; ++t)
#pragma unroll
    for (int m = 0; m < 4; ++m) *(LAS u32x4*)(lds + OWN3 + (2 * w + t) * 4096 + m * 1024 + lane * 16) = o_frag(a, m, t);
  prog_post(lds + QPROG + w * 4, 33);
  __builtin_amdgcn_s_setprio(0);
  ST();
}

DI void attn_wave(const int w, const int lane, const int rot, lptr lds, const unsigned l0, rsrc_t rk, rsrc_t rv, rsrc_t rx, rsrc_t rwq, const char* wo, const Ctx& c0, int& nst) {
  const int lg = lane >> 4, lr = lane & 15;
  ST();
  {
    const unsigned wvo = lg * 8192 + ((lr ^ (lg << 2)) << 4) + c0.g * 1024;
    const unsigned xvo = (lane >> 3) * 1280 + (((lane & 7) ^ ((4 * (w & 1) + lg) & 7)) << 4);
    auto issue = [&](int c) {
      const int cr = c + rot >= 10 ? c + rot - 10 : c + rot;
      const unsigned wd = l0 + VOFF + (c & 3) * PBUF + w * 1024, xd = wd + SLOT, xs = w * 10240 + cr * 128, ws_ = (cr * 64 + 4 * w) * 8192;
#pragma unroll
      for (int i = 0; i < 3; ++i) dma_df(rx, xvo, xs + i * 40960, xd + i * 4096);
      dma_df(rx, xvo, xs + 3 * 40960, w < 2 ? xd + 3 * 4096 : l0 + DUMMY);
#pragma unroll
      for (int i = 0; i < 4; ++i) dma_df(rwq, wvo, ws_ + i * 131072, wd + i * 4096);
    };
    issue(0); issue(1); issue(2);
    if (w == 0) { *(LAS u32x4*)(lds + CNT + (lane & 7) * 16) = u32x4{0u, 0u, 0u, 0u}; }
    const int b = c0.b, g = c0.g; const rsrc_t rwo = mkrsrc(wo, 4096 * 1280);
    u32x2 rw[8], rq2[8]; unsigned short r5[8]; unsigned long long e1;
    const unsigned rb = 4 * lg * 1280 + (b >> 4) * 640, wv0 = rb + w * 128 + lr * 8, wv1 = rb + 512 + w * 32 + lr * 2;
    const unsigned s2 = ((4 * g + ((b >> 2) & 3)) * 128 + (b & 3) * 32) * 1280;
    const int ti = (b - 20) * 4 + w, tk = b < 20 ? b : ti >> 1, tu = b < 20 ? w + 2 : ti & 1;
    const unsigned qv = lg * 8 * 8192 + lr * 8 + g * 1024 + 256, sq = tk * 32 * 8192 + tu * 128;
    constexpr int TN[13] = {0, 0, 0, TN0};
#pragma unroll
    for (int c = 0; c < 9; ++c) {
      SB(); asm volatile("s_waitcnt vmcnt(%0) lgkmcnt(0)\n\ts_barrier" :: "i"((c < 8 ? 16 : 8) + TN[c] + TN[c + 1] + TN[c + 2]) : "memory"); SB();
      if (c == TC + 4) { _Pragma("unroll") for (int i = 0; i < 8; ++i) PIN(rq2[i]); }
      if (c == TC + 6) { _Pragma("unroll") for (int i = 0; i < 8; ++i) { PIN(rw[i]); PIN(r5[i]); } }
      SB();
      if (c < 7) issue(c + 3);
      SB();
      if (c == TC) {
        e1 = __hip_atomic_load(c0.epoch(), RLX_AGENT) + 1;
#pragma unroll
        for (int jj = 0; jj < 8; ++jj) rq2[jj] = __builtin_amdgcn_raw_buffer_load_b64(rwq, qv, sq + jj * 8192, 0);
      }
      if (c == TC + 1) {
#pragma unroll
        for (int jj = 0; jj < 8; ++jj) rw[jj] = __builtin_amdgcn_raw_buffer_load_b64(rwo, wv0 + (jj & 3) * 1280, s2 + (jj >> 2) * 16 * 1280, 0);
      }
      if (c == TC + 2) {
#pragma unroll
        for (int jj = 0; jj < 8; ++jj) r5[jj] = __builtin_amdgcn_raw_buffer_load_b16(rwo, wv1 + (jj & 3) * 1280, s2 + (jj >> 2) * 16 * 1280, 0);
      }
      if (c == TC + 4) {
#pragma unroll
        for (int e = 0; e < 4; ++e) { u32x4 f;
#pragma unroll
          for (int pp = 0; pp < 4; ++pp) f[pp] = __builtin_amdgcn_perm(rq2[2 * pp + 1][e >> 1], rq2[2 * pp][e >> 1], (e & 1) ? 0x07060302u : 0x05040100u);
          __builtin_amdgcn_raw_buffer_store_b128(f, c0.rqt(), (tk * 6 + tu) * 4096 + e * 1024 + lane * 16, 0, 16); }
      }
      if (c == TC + 6) {
#pragma unroll
        for (int e = 0; e < 5; ++e) { u32x4 f;
#pragma unroll
          for (int pp = 0; pp < 4; ++pp) f[pp] = e < 4 ? __builtin_amdgcn_perm(rw[2 * pp + 1][(e >> 1) & 1], rw[2 * pp][(e >> 1) & 1], (e & 1) ? 0x07060302u : 0x05040100u)
                                                       : __builtin_amdgcn_perm((unsigned)r5[2 * pp + 1], (unsigned)r5[2 * pp], 0x05040100u);
          __builtin_amdgcn_raw_buffer_store_b128(f, c0.rwt(), (b * 4 + w) * 5120 + e * 1024 + lane * 16, 0, 16); }
      }
    }
    {
      const unsigned long long tag = ((unsigned long long)rfl((unsigned)(e1 >> 32)) << 32) | rfl((unsigned)e1);
      *(LAS unsigned long long*)(lds + TAGW) = tag;
      publish(c0.tflags() + b, lds + CNT + 16, tag, lane);
    }
  }
  if (w < 3) attn_main<2>(w, lds, l0, rk, rv, c0, nst); else attn_main<1>(w, lds, l0, rk, rv, c0, nst);
}

constexpr int LAG = 3;
DI void parrive(unsigned& pc, const int j, lptr lds) { WAITL(); ++pc; prog_post(lds + PPROG + j * 4, pc); }
template <int NE> constexpr int efrag(int e) { return NE == 5 ? e : (NE == 3 ? (e < 2 ? e : 4) : e + 2); }
template <int NE> DI u32x4 wload(rsrc_t rwt, const unsigned lv, const unsigned so, const int e) {
  return __builtin_amdgcn_raw_buffer_load_b128(rwt, lv + (efrag<NE>(e) & 3) * 1024, so + (efrag<NE>(e) >> 2) * 4096, 16);
}
DI void send_flag(const Ctx& c, const int dst, const int part, const unsigned lv, lptr lds) {
  const unsigned long long tag = *(const LAS unsigned long long*)(lds + TAGW);
  asm volatile("s_waitcnt vmcnt(0)" ::: "memory");
  if (lv == 0) __hip_atomic_store(c.xflag(dst) + c.g + part, tag, RLX_AGENT);
}
template <int NE>
DI void oproj_pass(const int j, const bool pstv, const Ctx& c0, lptr lds, const unsigned l0, int& nst) {
  const int lane = fresh_lane();
  const Ctx c = c0.fresh();
  const int mrot = ROT ? c.b : 0, side = (c.g ^ (NE != 5)) & 1;
  const rsrc_t rwt = c.rwt();
  const unsigned pa = l0 + APROG + (lane & 3) * 4, lv = lane * 16, tb = (side * 64 + j) * 5120;
  f32x4 acc[NE][7]; u32x4 af[4][NE], bf[4];
#pragma unroll
  for (int k = 0; k < 3; ++k)
#pragma unroll
    for (int e = 0; e < NE; ++e) { af[k][e] = wload<NE>(rwt, lv, tb + ((k + mrot) & 3) * 20480, e); SB(); }
#pragma unroll
  for (int e = 0; e < NE; ++e)
#pragma unroll
    for (int t = 0; t < 7; ++t) { acc[e][t] = f32x4{0.f, 0.f, 0.f, 0.f}; PIN(acc[e][t]); }
  asm volatile("s_nop 1"); SB();
#pragma nounroll
  for (int r = 0; r < 3; ++r) {
    const unsigned reg = qreg(r + 1);
    if (NE != 2) prog_wait<32, 2>(pa, 8 * r + 9);
    if (NE == 2 && r == 2) send_flag(c0.fresh(), 2 * j + (side ^ 1), 0, lv, lds);
    PST();
    auto baddr = [&](int gi) { return lp(l0 + reg + ((gi / 7 + mrot) & 3) * 1024 + lv) + (gi % 7) * 4096; };
#pragma unroll
    for (int gi = 0; gi < LAG; ++gi) bf[gi] = lds128(baddr(gi));
    SB();
#pragma unroll
    for (int k = 0; k < 4; ++k) {
      int r2 = r; asm volatile("" : "+s"(r2));
      const unsigned so = tb + ((k ? r2 + 1 : r2) * 4 + ((k + 3 + mrot) & 3)) * 20480;
#pragma unroll
      for (int t = 0; t < 7; ++t) {
        const int gi = 7 * k + t;
#pragma unroll
        for (int e = 0; e < NE; ++e) {
          mfma_ip(acc[e][t], af[k][e], bf[gi & 3]);
          if (e == 0 && gi + LAG < 28) bf[(gi + LAG) & 3] = lds128(baddr(gi + LAG));
          if (e == NE / 2 && t >= 1 && t <= NE) af[(k + 3) & 3][t - 1] = wload<NE>(rwt, lv, so, t - 1);
          if (NE == 5 && OSL && e == 4) __builtin_amdgcn_s_sleep(OSL);
          SB();
        }
      }
    }
    PST();
  }
#pragma unroll
  for (int e = 0; e < NE; ++e) af[3][e] = wload<NE>(rwt, lv, tb + (12 + ((3 + mrot) & 3)) * 20480, e);
  if (NE == 5 && WARM) {
    prog_wait<0, 8>(pa, WARM);
#pragma unroll
    for (int i = 0; i < 10; ++i) dma_sc1(rwt, lv, (side ^ 1) * 327680 + ((c.b & 7) * 40 + j * 10 + i) * 1024, l0 + DUMMY);
  }
  if (NE != 2) prog_wait<32, 1>(pa, 33);
  if (NE == 5) __builtin_amdgcn_s_setprio(2);
  PST();
  const Ctx c2 = c0.fresh(); const int dst = 2 * j + side;
  const bool keep = NE == 5 && dst == c2.g;
  const rsrc_t rpx = c2.rpx(dst); const unsigned po = c2.g * 17920;
  unsigned ba[4];
#pragma unroll
  for (int k = 0; k < 4; ++k) ba[k] = l0 + OWN3 + ((k + mrot) & 3) * 1024 + lv;
  auto out = [&](int t, int part) {
    if (keep) { *(LAS f32x4*)(lp(l0 + OWNP + lv) + (t * 5 + part) * 1024) = acc[part % NE][t]; if (part == 3) *(LAS f32x4*)(lp(l0 + OWNP + lv) + (t * 5 + 4) * 1024) = acc[NE - 1][t]; }
    else if (part < NE / 2) {
      const f32x4 x = acc[2 * part][t], y = acc[2 * part + 1][t];
      __builtin_amdgcn_raw_buffer_store_b128(u32x4{cvtpk(x[0], y[0]), cvtpk(x[1], y[1]), cvtpk(x[2], y[2]), cvtpk(x[3], y[3])}, rpx, po + lv + t * 2560 + efrag<NE>(2 * part) * 512, 0, 16);
    } else if (part == 2 && (NE & 1)) { const f32x4 x = acc[NE - 1][t];
      __builtin_amdgcn_raw_buffer_store_b64(u32x2{cvtpk(x[0], x[1]), cvtpk(x[2], x[3])}, rpx, po + (lv >> 1) + t * 2560 + 2048, 0, 16); }
  };
#pragma unroll
  for (int gi = 0; gi < LAG; ++gi) bf[gi] = lds128(lp(ba[gi & 3]) + (gi >> 2) * 4096);
  SB();
#pragma unroll
  for (int gi = 0; gi < 28; ++gi) {
    const int t = gi >> 2, k = gi & 3;
#pragma unroll
    for (int e = 0; e < NE; ++e) {
      mfma_ip(acc[e][t], af[k][e], bf[gi & 3]);
      if (e == 0 && gi + LAG < 28) bf[(gi + LAG) & 3] = lds128(lp(ba[(gi + LAG) & 3]) + ((gi + LAG) >> 2) * 4096);
      if (e == NE - 1 && k == 0 && t > 0) {
        asm volatile("s_nop %0" :: "i"(8 - NE));
#pragma unroll
        for (int e2 = 0; e2 < NE; ++e2) PIN(acc[e2][t - 1]);
      }
      if (e == NE - 1 && t > 0) out(t - 1, k);
      SB();
    }
  }
  MFMA_DONE();
#pragma unroll
  for (int e2 = 0; e2 < NE; ++e2) PIN(acc[e2][6]);
  SB();
#pragma unroll
  for (int k = 0; k < 4; ++k) out(6, k);
  if (NE == 5) __builtin_amdgcn_s_setprio(TPRIO);
  if (keep) prog_post(lds + KPROG, 1);
  if (NE != 5) { send_flag(c2, dst, NE == 2 ? 8 : 0, lv, lds); __builtin_amdgcn_s_waitcnt(0x0F70); }
  PST();
}

DI void final_part(const int j, const Ctx& c0, lptr lds, const unsigned l0, char* out, int& nst) {
  const Ctx c = c0.fresh(); const int b = c.b, g = c.g; const rsrc_t rpx = c.rpx(g);
  const int lane = fresh_lane(), p = g >> 1, lg = lane >> 4, lr = lane & 15;
  const bool pstv = j == 0; (void)pstv;
  const unsigned long long tg = *(const LAS unsigned long long*)(lds + TAGW), tag = ((unsigned long long)rfl((unsigned)(tg >> 32)) << 32) | rfl((unsigned)tg);
  auto lo = [](unsigned v) { return __uint_as_float(v << 16); };
  auto hi = [](unsigned v) { return __uint_as_float(v & 0xffff0000u); };
  f32x4 a[2][5];
#pragma unroll
  for (int st = 0; st < 3; ++st) {
    const int ns = st ? 4 : 3;
    {
      const int k = lane & 3;
      gu64* f = c.xflag(g) + (st ? 2 * k + 1 - (g & 1) + (st == 1 ? 8 : 0) : 2 * (k == p ? (p + 1) & 3 : k) + (g & 1));
      wait_flag(f, __hip_atomic_load(f, RLX_AGENT), tag);
    }
    u32x4 in[2][4][2]; u32x2 in2[2][4];
#pragma unroll
    for (int i = 0; i < 2; ++i)
#pragma unroll
      for (int k = 0; k < ns; ++k) {
        const int src = st ? 2 * k + 1 - (g & 1) : 2 * (k + (k >= p)) + (g & 1);
        const unsigned o = (i == 1 && j == 3) ? OOB : src * 17920 + (j + 4 * i) * 2560;
        if (st != 1) in[i][k][0] = __builtin_amdgcn_raw_buffer_load_b128(rpx, lane * 16 + o, 0, 16);
        if (st != 2) in[i][k][1] = __builtin_amdgcn_raw_buffer_load_b128(rpx, lane * 16 + o + 1024, 0, 16);
        if (st != 1) in2[i][k] = __builtin_amdgcn_raw_buffer_load_b64(rpx, lane * 8 + o + 2048, 0, 16);
      }
    if (st == 0) {
      prog_wait<0, 1>(l0 + KPROG, 1);
#pragma unroll
      for (int i = 0; i < 2; ++i)
#pragma unroll
        for (int e = 0; e < 5; ++e) a[i][e] = *(LAS f32x4*)(lds + OWNP + (((i == 1 && j == 3) ? 0 : j + 4 * i) * 5 + e) * 1024 + lane * 16);
    }
#pragma unroll
    for (int i = 0; i < 2; ++i)
#pragma unroll
      for (int k = 0; k < ns; ++k) {
#pragma unroll
        for (int d = (st == 1 ? 4 : 0); d < (st == 2 ? 4 : 8); ++d) { const int reg = d & 3, e = 2 * (d >> 2); a[i][e][reg] += lo(in[i][k][d >> 2][d & 3]); a[i][e + 1][reg] += hi(in[i][k][d >> 2][d & 3]); }
        if (st != 1) {
#pragma unroll
          for (int d = 0; d < 2; ++d) { a[i][4][2 * d] += lo(in2[i][k][d]); a[i][4][2 * d + 1] += hi(in2[i][k][d]); }
        }
      }
    PST();
  }
  if (j == 0 && g == 0 && b == 0 && lane == 0) __hip_atomic_store(c.epoch(), tag, RLX_AGENT);
  const rsrc_t rout = mkrsrc(out + (size_t)b * 143360, 143360);
#pragma unroll
  for (int i = 0; i < 2; ++i) {
    const unsigned ob = (i == 1 && j == 3) ? OOB : ((j + 4 * i) * 16 + lr) * 1280 + (g & 1) * 640;
#pragma unroll
    for (int hf = 0; hf < 2; ++hf)
      __builtin_amdgcn_raw_buffer_store_b128(u32x4{cvtpk(a[i][0][2 * hf], a[i][1][2 * hf]), cvtpk(a[i][2][2 * hf], a[i][3][2 * hf]), cvtpk(a[i][0][2 * hf + 1], a[i][1][2 * hf + 1]),
                                                   cvtpk(a[i][2][2 * hf + 1], a[i][3][2 * hf + 1])}, rout, ob + p * 128 + lg * 32 + hf * 16, 0, 0);
    __builtin_amdgcn_raw_buffer_store_b64(u32x2{cvtpk(a[i][4][0], a[i][4][1]), cvtpk(a[i][4][2], a[i][4][3])}, rout, ob + 512 + p * 32 + lg * 8, 0, 0);
  }
}

template <int NE> struct QpState { f32x4 acc[NE][7]; u32x4 xf[7]; lptr xa0, xa1; };
template <int NE> DI void qp_init(QpState<NE>& q, lptr xb, int lane) {
  const int lg = lane >> 4, lr = lane & 15;
  q.xa0 = xb + lr * 128 + ((lg ^ (lr >> 1)) << 4); q.xa1 = xb + lr * 128 + (((4 + lg) ^ (lr >> 1)) << 4);
#pragma unroll
  for (int t = 0; t < 7; ++t) { q.xf[t] = u32x4{0u, 0u, 0u, 0u}; PIN(q.xf[t]);
#pragma unroll
    for (int e = 0; e < NE; ++e) { q.acc[e][t] = f32x4{0.f, 0.f, 0.f, 0.f}; PIN(q.acc[e][t]); } }
  asm volatile("s_nop 1"); SB();
}
template <int PAR, int NE, bool DEEP>
DI void qp2_chunk(QpState<NE>& q, u32x4 (&af)[4][NE], const int c, const int j, const int rot, unsigned& pc, const unsigned pa, const unsigned xvo, const unsigned lv, const unsigned tb,
                  lptr lds, const unsigned l0, rsrc_t rx, rsrc_t rqt) {
  SB(); asm volatile("s_waitcnt vmcnt(%0) lgkmcnt(0)" :: "i"(DEEP ? 6 * NE + 8 : 2 * NE) : "memory"); SB();
  ++pc; prog_post(lds + PPROG + j * 4, pc);
  const unsigned pg = prog_read<16>(pa);
  const int c1 = c + 1 + rot >= 10 ? c + 1 + rot - 10 : c + 1 + rot, c2 = c + 2 + rot >= 10 ? c + 2 + rot - 10 : c + 2 + rot;
  const unsigned s1 = c < 9 ? (2 * c1 + 1) * 24576 + tb : OOB, s2 = c < 8 ? (2 * c2) * 24576 + tb : OOB;
  lptr x0 = q.xa0 + (DEEP ? (c & 2) * XCH : 0), x1 = q.xa1 + (DEEP ? (c & 2) * XCH : 0);
  SB();
#pragma unroll
  for (int u = 0; u < 14; ++u) {
    const int v = (u + 14 - LAG) % 14, t = v % 7, set = u < LAG ? 2 * (PAR ^ 1) + 1 : 2 * PAR + v / 7;
#pragma unroll
    for (int e = 0; e < NE; ++e) {
      mfma_ip(q.acc[e][t], af[set][e], q.xf[t]);
      if (e == 0 && u >= 2) q.xf[u % 7] = lds128((u >= 7 ? x1 : x0) + PAR * XCH + (u % 7) * 2048);
      if (e == NE / 2 && u >= LAG && u < LAG + NE) af[2 * (PAR ^ 1) + 1][u - LAG] = __builtin_amdgcn_raw_buffer_load_b128(rqt, lv + (u - LAG) * 1024, s1, 16);
      if (e == NE / 2 && u >= 10 && u < 10 + NE) af[2 * PAR][u - 10] = __builtin_amdgcn_raw_buffer_load_b128(rqt, lv + (u - 10) * 1024, s2, 16);
      SB();
    }
    if (u == 0) { prog_wait<16, 0>(pa, pg, pc); q.xf[0] = lds128(x0 + PAR * XCH); q.xf[1] = lds128(x0 + PAR * XCH + 2048); SB(); }
    if (u == 1) {
      const bool on = DEEP ? c < 8 : c < 9;
      const int cx = !DEEP ? c1 : (c + 3 + rot >= 10 ? c + 3 + rot - 10 : c + 3 + rot);
      const unsigned xs = on ? j * 10240 + cx * 128 : OOB, xd = l0 + (on ? (DEEP ? RB + ((c + 3) & 3) * XCH : RC + (PAR ^ 1) * XCH) + j * 1024 : DUMMY), st = on ? 4096u : 0u;
#pragma unroll
      for (int i = 0; i < 3; ++i) dma_df(rx, xvo, xs + i * 40960, xd + i * st);
      dma_df(rx, xvo, xs + 3 * 40960, j < 2 ? xd + 3 * st : l0 + DUMMY);
      SB();
    }
  }
}
template <int I>
DI void qp1_chunk(QpState<2>& q, u32x4 (&af)[10][2], const int c, const int j, const int rot, unsigned& pc, const unsigned pa, const unsigned xvo, const unsigned lv,
                  lptr lds, const unsigned l0, rsrc_t rx, rsrc_t rqt) {
  WAITV(20);
  ++pc; prog_post(lds + PPROG + j * 4, pc);
  const unsigned pg = prog_read<16>(pa);
  const int c4 = c + 4 + rot >= 10 ? c + 4 + rot - 10 : c + 4 + rot, c5 = c + 5 + rot >= 10 ? c + 5 + rot - 10 : c + 5 + rot;
  const unsigned s1 = c < 6 ? (2 * c4 + 1) * 24576 + j * 2048 : OOB, s2 = c < 5 ? (2 * c5) * 24576 + j * 2048 : OOB;
  lptr x0 = q.xa0 + (c & 3) * XCH, x1 = q.xa1 + (c & 3) * XCH;
  constexpr int SP = (2 * I + 9) % 10;
  SB();
#pragma unroll
  for (int u = 0; u < 14; ++u) {
    const int v = (u + 14 - LAG) % 14, t = v % 7, set = u < LAG ? SP : 2 * I + v / 7;
#pragma unroll
    for (int e = 0; e < 2; ++e) {
      mfma_ip(q.acc[e][t], af[set][e], q.xf[t]);
      if (e == 0 && u >= 2) q.xf[u % 7] = lds128((u >= 7 ? x1 : x0) + (u % 7) * 2048);
      if (e == 1 && (u == 4 || u == 6)) af[SP][(u - 4) >> 1] = __builtin_amdgcn_raw_buffer_load_b128(rqt, lv + ((u - 4) >> 1) * 1024, s1, 16);
      if (e == 1 && (u == 10 || u == 12)) af[2 * I][(u - 10) >> 1] = __builtin_amdgcn_raw_buffer_load_b128(rqt, lv + ((u - 10) >> 1) * 1024, s2, 16);
      SB();
    }
    if (u == 0) { prog_wait<16, 0>(pa, pg, pc); q.xf[0] = lds128(x0); q.xf[1] = lds128(x0 + 2048); SB(); }
    if (u == 1) {
      const bool on = c < 8;
      const int cx = c + 3 + rot >= 10 ? c + 3 + rot - 10 : c + 3 + rot;
      const unsigned xs = on ? j * 10240 + cx * 128 : OOB, xd = l0 + (on ? RB + ((c + 3) & 3) * XCH + j * 1024 : DUMMY), st = on ? 4096u : 0u;
#pragma unroll
      for (int i = 0; i < 3; ++i) dma_df(rx, xvo, xs + i * 40960, xd + i * st);
      dma_df(rx, xvo, xs + 3 * 40960, j < 2 ? xd + 3 * st : l0 + DUMMY);
      SB();
    }
  }
}
template <int NE> DI void qp2_first(u32x4 (&af)[4][NE], const int rot, const unsigned lv, const unsigned tb, rsrc_t rqt) {
  const int r1 = rot == 9 ? 0 : rot + 1;
#pragma unroll
  for (int k = 0; k < 3; ++k)
#pragma unroll
    for (int e = 0; e < NE; ++e) { af[k][e] = __builtin_amdgcn_raw_buffer_load_b128(rqt, lv + e * 1024, (k < 2 ? 2 * rot + k : 2 * r1) * 24576 + tb, 16); SB(); }
#pragma unroll
  for (int e = 0; e < NE; ++e) { af[3][e] = u32x4{0u, 0u, 0u, 0u}; PIN(af[3][e]); }
}
template <int NE> DI void qp_tail(QpState<NE>& q, const u32x4 (&a)[NE]) {
#pragma unroll
  for (int t = 7 - LAG; t < 7; ++t)
#pragma unroll
    for (int e = 0; e < NE; ++e) mfma_ip(q.acc[e][t], a[e], q.xf[t]);
  MFMA_DONE(); SB();
}

DI void proj_wave(const int j, const int lane, const Ctx& c0, const int rot, lptr lds, const unsigned l0, rsrc_t rx, int& nst) {
  unsigned long long tag;
  const bool pstv = j == 0; (void)pstv;
  const int lg = lane >> 4, lr = lane & 15;
  {
    QpState<2> q;
    u32x4 af[2][2];
    af[1][0] = af[1][1] = u32x4{0u, 0u, 0u, 0u}; PIN(af[1][0]); PIN(af[1][1]);
    qp_init(q, lds + VOFF + SLOT, lane);
    const int q4 = lr >> 2;
    const unsigned wa = l0 + VOFF + (8 * lg + q4) * 256 + (((j ^ q4) * 4 + (lane & 3)) << 4);
#pragma nounroll
    for (int c = 0; c < 10; ++c) {
      const int bo = (c & 3) * PBUF;
      BAR_L();
#pragma unroll
      for (int u = 0; u < 14; ++u) {
        const int v = (u + 14 - LAG) % 14, t = v % 7, set = u < LAG ? 1 : v / 7;
        auto ard = [&](int ks, int e) { lptr p = lp(wa + bo) + ks * 8192 + e * 8; const u32x2 lo = ldstr(p), hi = ldstr(p + 1024); af[ks][e] = u32x4{lo[0], lo[1], hi[0], hi[1]}; };
        if (u == 0) { ard(0, 0); q.xf[0] = lds128(q.xa0 + bo); ard(0, 1); SB(); }
#pragma unroll
        for (int e = 0; e < 2; ++e) {
          mfma_ip(q.acc[e][t], af[set][e], q.xf[t]);
          if (e == 0 && u >= 1) q.xf[u % 7] = lds128((u >= 7 ? q.xa1 : q.xa0) + bo + (u % 7) * 2048);
          if (e == 1 && (u == 4 || u == 5)) ard(1, u - 4);
          SB();
        }
      }
      PST();
    }
    qp_tail(q, af[1]);
    BAR_L();
#pragma unroll
    for (int t = 0; t < 7; ++t)
      *(LAS u32x4*)(lds + RA + t * 4096 + j * 1024 + lane * 16) = u32x4{cvtpk(q.acc[0][t][0] * QSCALE, q.acc[0][t][1] * QSCALE), cvtpk(q.acc[0][t][2] * QSCALE, q.acc[0][t][3] * QSCALE),
                                                                        cvtpk(q.acc[1][t][0] * QSCALE, q.acc[1][t][1] * QSCALE), cvtpk(q.acc[1][t][2] * QSCALE, q.acc[1][t][3] * QSCALE)};
    { const unsigned long long tg = *(const LAS unsigned long long*)(lds + TAGW); tag = ((unsigned long long)rfl((unsigned)(tg >> 32)) << 32) | rfl((unsigned)tg); }
    BAR_L();
    PST();
  }
  unsigned pc = 0;
  __builtin_amdgcn_s_setprio(QPRIO);
  {
    const Ctx c = c0.fresh();
    const rsrc_t rqt = c.rqt();
    const unsigned pa = l0 + APROG + (lane & 3) * 4, lv = lane * 16;
    const unsigned xvo = (lane >> 3) * 1280 + (((lane & 7) ^ ((4 * (j & 1) + lg) & 7)) << 4);
#pragma unroll
    for (int k = 0; k < 3; ++k) {
      const unsigned xs = j * 10240 + (k + rot >= 10 ? k + rot - 10 : k + rot) * 128, xd = l0 + RB + k * XCH + j * 1024;
#pragma unroll
      for (int i = 0; i < 3; ++i) dma_df(rx, xvo, xs + i * 40960, xd + i * 4096);
      dma_df(rx, xvo, xs + 3 * 40960, j < 2 ? xd + 3 * 4096 : l0 + DUMMY);
    }
    wait_flag(c.tflags() + (lane & 31), __hip_atomic_load(c.tflags() + (lane & 31), RLX_AGENT), tag);
    u32x4 af[4][4];
    {
      QpState<2> q; u32x4 a2[10][2];
#pragma unroll
      for (int k = 0; k < 9; ++k) {
        const int ck = (k >> 1) + rot >= 10 ? (k >> 1) + rot - 10 : (k >> 1) + rot;
#pragma unroll
        for (int e = 0; e < 2; ++e) { a2[k][e] = __builtin_amdgcn_raw_buffer_load_b128(rqt, lv + e * 1024, (2 * ck + (k & 1)) * 24576 + j * 2048, 16); SB(); }
      }
      a2[9][0] = a2[9][1] = u32x4{0u, 0u, 0u, 0u}; PIN(a2[9][0]); PIN(a2[9][1]);
      qp_init(q, lds + RB, lane);
      PST();
#pragma nounroll
      for (int cc = 0; cc < 10; cc += 5) {
        qp1_chunk<0>(q, a2, cc, j, rot, pc, pa, xvo, lv, lds, l0, rx, rqt); qp1_chunk<1>(q, a2, cc + 1, j, rot, pc, pa, xvo, lv, lds, l0, rx, rqt);
        qp1_chunk<2>(q, a2, cc + 2, j, rot, pc, pa, xvo, lv, lds, l0, rx, rqt); qp1_chunk<3>(q, a2, cc + 3, j, rot, pc, pa, xvo, lv, lds, l0, rx, rqt);
        qp1_chunk<4>(q, a2, cc + 4, j, rot, pc, pa, xvo, lv, lds, l0, rx, rqt);
      }
      qp_tail(q, a2[9]);
      parrive(pc, j, lds);
      qp2_first(af, rot, lv, (j + 2) * 4096, rqt);
      prog_wait<16, 0>(pa, pc);
      lptr qp = lds + RB + (8 * (j >> 1) + 2 * lg) * 256 + lr * 16 + (j & 1) * 4;
#pragma unroll
      for (int t = 0; t < 7; ++t)
#pragma unroll
        for (int r = 0; r < 4; ++r) *(LAS unsigned*)(qp + t * 4096 + (r >> 1) * 256 + (r & 1) * 8) = cvtpk(q.acc[0][t][r] * QSCALE, q.acc[1][t][r] * QSCALE);
      parrive(pc, j, lds);
      PST();
    }
    QpState<4> q;
    qp_init(q, lds + RC, lane);
#pragma nounroll
    for (int cc = 0; cc < 10; cc += 2) {
      qp2_chunk<0, 4, false>(q, af, cc, j, rot, pc, pa, xvo, lv, (j + 2) * 4096, lds, l0, rx, rqt);
      qp2_chunk<1, 4, false>(q, af, cc + 1, j, rot, pc, pa, xvo, lv, (j + 2) * 4096, lds, l0, rx, rqt);
      PST();
    }
    qp_tail(q, af[3]);
    parrive(pc, j, lds);
    u32x4 qf[7][2];
#pragma unroll
    for (int t = 0; t < 7; ++t)
#pragma unroll
      for (int hf = 0; hf < 2; ++hf) {
        qf[t][hf] = u32x4{cvtpk(q.acc[0][t][2 * hf] * QSCALE, q.acc[1][t][2 * hf] * QSCALE), cvtpk(q.acc[2][t][2 * hf] * QSCALE, q.acc[3][t][2 * hf] * QSCALE),
                          cvtpk(q.acc[0][t][2 * hf + 1] * QSCALE, q.acc[1][t][2 * hf + 1] * QSCALE), cvtpk(q.acc[2][t][2 * hf + 1] * QSCALE, q.acc[3][t][2 * hf + 1] * QSCALE)};
        PIN(qf[t][hf]);
      }
    prog_wait<16, 0>(pa, pc);
    prog_wait<32, 0>(pa, 1);
#pragma unroll
    for (int t = 0; t < 7; ++t)
#pragma unroll
      for (int hf = 0; hf < 2; ++hf) *(LAS u32x4*)(lds + ((j >> 1) ? RC : RA) + (4 * (j & 1) + lg) * 512 + lr * 16 + t * 4096 + hf * 256) = qf[t][hf];
    parrive(pc, j, lds);
    PST();
  }
  __builtin_amdgcn_s_setprio(0);
  __builtin_amdgcn_s_waitcnt(0x0F70);
}

__global__ __launch_bounds__(512, 2) void attn_fused(const char* __restrict__ x, const char* __restrict__ K, const char* __restrict__ V,
                                                     const char* __restrict__ wq, const char* __restrict__ wo, char* __restrict__ ws, char* __restrict__ out) {
  extern __shared__ __attribute__((aligned(1024))) char smem[];
  const int w = rfl(threadIdx.x >> 6);
  unsigned long long pc0 = 0;
  if (CODEPF) asm volatile("s_getpc_b64 %0" : "=s"(pc0));
  const int bid = blockIdx.x;
  const int b = ((bid >> 1) & 3) * 8 + (bid >> 5), g = (bid & 1) * 4 + ((bid >> 3) & 3);
  const int rot = ROT ? (((b & 7) * 5) / 4 + ((g & 3) * 5) / 2) % 10 : 0;
  lptr lds = (lptr)smem;
  const unsigned l0 = (unsigned)(uintptr_t)lds;
  const Ctx c = {ws, b, g};
  const rsrc_t rx = mkrsrc(x + (size_t)b * 143360, 143360), rwq = mkrsrc(wq, 640 * 8192);
  int nst = 0; (void)nst;
  if (w < 4) {
    const size_t kvb = ((size_t)b * 32 + g * 4) * 131072;
    attn_wave(w, fresh_lane(), rot, lds, l0, mkrsrc(K + kvb, 524288), mkrsrc(V + kvb, 524288), rx, rwq, wo, c, nst);
    oproj_pass<3>(w, false, c, lds, l0, nst);
    ST(); ST();
  } else {
    if (CODEPF && w < 6) dma_df(mkrsrc((const void*)(pc0 & ~255ull), CODEPF), fresh_lane() * 16, ((blockIdx.x >> 3) * 2 + (w - 4)) * 1024, l0 + DUMMY + 1024);
    proj_wave(w - 4, fresh_lane(), c, rot, lds, l0, rx, nst);
    oproj_pass<5>(w - 4, w == 4, c, lds, l0, nst);
    oproj_pass<2>(w - 4, w == 4, c, lds, l0, nst);
    final_part(w - 4, c, lds, l0, out, nst);
    __builtin_amdgcn_s_waitcnt(0x0F70);
  }
}

extern "C" void kernel_launch(void* const* d_in, const int* in_sizes, int n_in, void* d_out, int out_size, void* d_ws, size_t ws_size, hipStream_t stream) {
  (void)hipFuncSetAttribute((const void*)attn_fused, hipFuncAttributeMaxDynamicSharedMemorySize, LDS_BYTES);
  attn_fused<<<256, 512, LDS_BYTES, stream>>>((const char*)d_in[0], (const char*)d_in[1], (const char*)d_in[2], (const char*)d_in[3], (const char*)d_in[4], (char*)d_ws + WSOFF, (char*)d_out);
}
```
